```python
import torch

def _softmax(x, axis=-1):
    return torch.softmax(x, dim=axis)
def _gelu(x):
    return torch.nn.functional.gelu(x, approximate="tanh")
def _silu(x):
    return torch.nn.functional.silu(x)
def _sigmoid(x):
    return torch.sigmoid(x)
def _layernorm(x, axis=-1, eps=1e-5):
    m = x.mean(dim=axis, keepdim=True)
    v = x.var(dim=axis, keepdim=True, unbiased=False)
    return (x - m) / torch.sqrt(v + eps)
def _scatter_add(tgt, idx, src, axis=0):
    return tgt.clone().index_add_(axis, idx, src)
def _segment_sum(src, seg, n):
    out = torch.zeros((n,) + src.shape[1:], dtype=src.dtype)
    return out.index_add_(0, seg, src)
def _pad(x, pw):
    flat = tuple(v for pair in reversed(pw) for v in pair)
    return torch.nn.functional.pad(x, flat)

kernel_name = 'gated_mix__linear_attn_block__glu_gated_bilinear__prompt'

def setup_inputs():

    _w = (lambda a: (lambda t: t.to(torch.bfloat16) if t.is_floating_point() else t)(torch.as_tensor(a)))
    def _A_setup():
        B, S, D = 1, 2048, 512
        return {
            "x": _w((lambda *s: torch.randn(*s, dtype=torch.bfloat16) * 0.05)(B, S, D)),
            "wqkv": _w((lambda *s: torch.randn(*s, dtype=torch.bfloat16) * 0.8)(D, 3 * D)),
            "wo": _w((lambda *s: torch.randn(*s, dtype=torch.bfloat16) * 0.05)(D, D)),
        }
    _A_in = _A_setup()

    def _B_setup():
        B, S, D, F = 1, 2048, 512, 1792
        return {
            "x": _w((lambda *s: torch.randn(*s, dtype=torch.bfloat16) * 0.05)(B, S, D)),
            "w_gate": _w((lambda *s: torch.randn(*s, dtype=torch.bfloat16) * 1.6)(D, F) * (D ** -0.5)),
            "w_up": _w((lambda *s: torch.randn(*s, dtype=torch.bfloat16) * 1.6)(D, F) * (D ** -0.5)),
            "w_down": _w((lambda *s: torch.randn(*s, dtype=torch.bfloat16) * 1.6)(F, D) * (F ** -0.5)),
        }
    _B_in = _B_setup()

    return {
        "A_x": _A_in["x"],
        "A_wqkv": _A_in["wqkv"],
        "A_wo": _A_in["wo"],
        "B_x": _B_in["x"],
        "B_w_gate": _B_in["w_gate"],
        "B_w_up": _B_in["w_up"],
        "B_w_down": _B_in["w_down"],
        "gate_w": _w((lambda *s: torch.randn(*s, dtype=torch.bfloat16) * 0.05)(512) * 16.0),
    }

def reference(A_x, A_wqkv, A_wo, B_x, B_w_gate, B_w_up, B_w_down, gate_w):
    def _A_ref(x, wqkv, wo):
        B, S, D = x.shape
        H = 32; Dh = D // H
        qkv = (lambda x, *d: x.permute(*d))((x @ wqkv).reshape(B, S, 3, H, Dh), 2, 0, 3, 1, 4)
        q, k, v = qkv[0], qkv[1], qkv[2]
        phi = lambda t: torch.where(t > 0, t + 1, torch.exp(t)) + 1e-6
        pq, pk = phi(q), phi(k)
        kv = torch.einsum("bhsd,bhse->bhde", pk, v)
        z = torch.einsum("bhsd->bhd", pk)
        num = torch.einsum("bhqd,bhde->bhqe", pq, kv)
        den = torch.einsum("bhqd,bhd->bhq", pq, z)[..., None] + 1e-6
        o = num / den
        return (lambda x, *d: x.permute(*d))(o, 0, 2, 1, 3).reshape(B, S, D) @ wo

    def _B_ref(x, w_gate, w_up, w_down):
        g = torch.einsum("bsd,df->bsf", x, w_gate)
        u = torch.einsum("bsd,df->bsf", x, w_up)
        h = g * u
        return torch.einsum("bsf,fd->bsd", h, w_down)

    x = A_x
    g = _sigmoid((x * gate_w).sum(-1, keepdims=True))
    a = _A_ref(x, A_wqkv, A_wo)
    b = _B_ref(x, B_w_gate, B_w_up, B_w_down)
    return g * a + (1 - g) * b
```

```cpp
#include <hip/hip_runtime.h>
#include <hip/hip_bf16.h>
typedef unsigned short u16;
__device__ __forceinline__ float bf2f(u16 v){ return __uint_as_float(((unsigned)v)<<16); }
__device__ __forceinline__ u16 f2bf(float x){ unsigned u=__float_as_uint(x); u += 0x7fffu + ((u>>16)&1u); return (u16)(u>>16); }
__device__ __forceinline__ float rb(float x){ return bf2f(f2bf(x)); }
constexpr int S=2048,D=512,F=1792,H=32,DH=16;

__global__ void gemm_naive(const u16* A,int lda,const u16* B,int ldb,float* C,int ldc,int K){
  __shared__ float As[16][17], Bs[16][17];
  int tx=threadIdx.x, ty=threadIdx.y; int row=blockIdx.y*16+ty, col=blockIdx.x*16+tx; float acc=0;
  for(int k0=0;k0<K;k0+=16){ As[ty][tx]=bf2f(A[(size_t)row*lda+k0+tx]); Bs[ty][tx]=bf2f(B[(size_t)(k0+ty)*ldb+col]); __syncthreads();
    for(int k=0;k<16;k++) acc+=As[ty][k]*Bs[k][tx]; __syncthreads(); }
  C[(size_t)row*ldc+col]=acc;
}
__device__ __forceinline__ float phi(float t){ t=rb(t); float r = t>0.f? rb(t+1.f): rb(__expf(t)); return rb(r+1e-6f); }
__global__ void kv_k(const float* qkv,float* kv,float* z){
  int h=blockIdx.x, d=threadIdx.x>>4, e=threadIdx.x&15; float a=0,zz=0;
  for(int s=0;s<S;s++){ float pk=phi(qkv[(size_t)s*1536+512+h*16+d]); float v=rb(qkv[(size_t)s*1536+1024+h*16+e]); a+=pk*v; zz+=pk; }
  kv[(h*16+d)*16+e]=rb(a); if(e==0) z[h*16+d]=rb(zz);
}
__global__ void o_k(const float* qkv,const float* kv,const float* z,u16* o){
  int s=blockIdx.x, c=threadIdx.x, h=c>>4, e=c&15; float num=0,den=0;
  for(int d=0;d<16;d++){ float pq=phi(qkv[(size_t)s*1536+h*16+d]); num+=pq*kv[(h*16+d)*16+e]; den+=pq*z[h*16+d]; }
  den=rb(rb(den)+1e-6f); o[(size_t)s*512+c]=f2bf(rb(num)/den);
}
__global__ void h_k(const float* g,const float* u,u16* h,int n){ int i=blockIdx.x*256+threadIdx.x; if(i<n) h[i]=f2bf(rb(g[i])*rb(u[i])); }
__global__ void fin_k(const u16* x,const u16* gw,const float* a,const float* b,u16* out){
  int s=blockIdx.x, c=threadIdx.x; __shared__ float red[512];
  red[c]=rb(bf2f(x[(size_t)s*512+c])*bf2f(gw[c])); __syncthreads();
  for(int o=256;o>0;o>>=1){ if(c<o) red[c]+=red[c+o]; __syncthreads(); }
  float t=rb(red[0]); float g=rb(1.f/(1.f+__expf(-t)));
  float aa=rb(a[(size_t)s*512+c]), bb=rb(b[(size_t)s*512+c]);
  out[(size_t)s*512+c]=f2bf(rb(g*aa)+rb(rb(1.f-g)*bb));
}
extern "C" void kernel_launch(void* const* d_in, const int* in_sizes, int n_in, void* d_out, int out_size, void* d_ws, size_t ws_size, hipStream_t stream){
  const u16* x=(const u16*)d_in[0]; const u16* wqkv=(const u16*)d_in[1]; const u16* wo=(const u16*)d_in[2];
  const u16* wg=(const u16*)d_in[4]; const u16* wu=(const u16*)d_in[5]; const u16* wd=(const u16*)d_in[6]; const u16* gw=(const u16*)d_in[7];
  float* ws=(float*)d_ws; float* qkv=ws; float* G=qkv+S*1536; float* U=G+S*F; float* a=U+S*F; float* b=a+S*D; float* kv=b+S*D; float* z=kv+H*256;
  u16* o=(u16*)(z+512); u16* h=o+S*D;
  dim3 t(16,16);
  gemm_naive<<<dim3(1536/16,S/16),t,0,stream>>>(x,D,wqkv,1536,qkv,1536,D);
  gemm_naive<<<dim3(F/16,S/16),t,0,stream>>>(x,D,wg,F,G,F,D);
  gemm_naive<<<dim3(F/16,S/16),t,0,stream>>>(x,D,wu,F,U,F,D);
  kv_k<<<H,256,0,stream>>>(qkv,kv,z);
  o_k<<<S,512,0,stream>>>(qkv,kv,z,o);
  h_k<<<(S*F+255)/256,256,0,stream>>>(G,U,h,S*F);
  gemm_naive<<<dim3(D/16,S/16),t,0,stream>>>(o,D,wo,D,a,D,D);
  gemm_naive<<<dim3(D/16,S/16),t,0,stream>>>(h,F,wd,D,b,D,F);
  fin_k<<<S,512,0,stream>>>(x,gw,a,b,(u16*)d_out);
}
```

```cpp
#include <hip/hip_runtime.h>

struct Srd { const void* p; unsigned n; unsigned f; };
struct Args { Srd x, wg, wu, wq, wd, wo, ws, out, gw; };

__global__ __launch_bounds__(256, 1) void fused_kernel(Args args) {
  __shared__ __attribute__((aligned(16))) char lds[163840];
  unsigned long long ka = (unsigned long long)__builtin_amdgcn_kernarg_segment_ptr();
  unsigned b = __builtin_amdgcn_workgroup_id_x();
  unsigned w = __builtin_amdgcn_readfirstlane(__builtin_amdgcn_workitem_id_x() >> 6);
  asm volatile(
    "s_load_dwordx16 s[36:51], %0, 0\n"
    "s_load_dwordx16 s[52:67], %0, 64\n"
    "s_load_dwordx4 s[68:71], %0, 128\n"
    "s_bitcmp1_b32 %1, 6\n"
    "s_cbranch_scc1 LtypeB\n"
    "v_mbcnt_lo_u32_b32 v96, -1, 0\n"
    "v_mbcnt_hi_u32_b32 v96, -1, v96\n"
    "s_mov_b32 s74, %2\n"
    "s_mov_b32 s20, %1\n"
    "s_and_b32 s14, s20, 1\n"
    "s_lshl_b32 s14, s14, 3\n"
    "s_bfe_u32 s15, s20, 0x30003\n"
    "s_add_u32 s21, s14, s15\n"
    "s_bfe_u32 s22, s20, 0x20001\n"
    "s_lshr_b32 s23, s20, 6\n"
    "s_lshr_b32 s24, s20, 7\n"
    "s_lshl2_add_u32 s14, s21, s74\n"
    "s_lshl_b32 s84, s14, 15\n"
    "s_lshl_b32 s75, s74, 12\n"
    "s_lshl1_add_u32 s18, s22, s24\n"
    "s_getpc_b64 s[12:13]\n"
    "LpcA:\n"
    "s_lshl2_add_u32 s14, s15, s74\n"
    "s_lshl_b32 s14, s14, 10\n"
    "v_and_b32 v228, 7, v96\n"
    "v_lshl_add_u32 v228, v228, 7, s14\n"
    "v_min_u32 v228, LendA-LpcA-4, v228\n"
    "global_load_dword v228, v228, s[12:13]\n"
    "v_lshrrev_b32 v97, 3, v96\n"
    "v_and_b32 v98, 7, v96\n"
    "v_lshrrev_b32 v99, 4, v96\n"
    "v_xor_b32 v100, v98, v99\n"
    "v_lshlrev_b32 v100, 4, v100\n"
    "v_lshl_add_u32 v172, v97, 10, v100\n"
    "v_xor_b32 v100, 64, v172\n"
    "v_add_u32 v173, 7168, v100\n"
    "v_add_u32 v174, 14336, v172\n"
    "v_add_u32 v175, 14336, v173\n"
    "v_and_b32 v101, 1, v99\n"
    "v_lshlrev_b32 v101, 2, v101\n"
    "v_xor_b32 v101, v98, v101\n"
    "v_lshlrev_b32 v101, 4, v101\n"
    "v_mov_b32 v102, 3584\n"
    "v_mad_u32_u24 v242, v97, v102, v101\n"
    "v_add_u32 v243, 27648, v242\n"
    "v_lshlrev_b32 v248, 4, v96\n"
    "s_mul_i32 s85, s74, 57344\n"
    "s_lshl_b32 s19, s18, 8\n"
    "s_add_u32 s85, s85, s19\n"
    "s_lshl_b32 s16, s74, 10\n"
    "s_add_u32 s16, s16, 147456\n"
    "s_waitcnt lgkmcnt(0)\n"
    "s_add_u32 m0, s75, 0\n"
    "s_nop 0\n"
    "buffer_load_dwordx4 v172, s[36:39], s84 offen offset:0 lds\n"
    "buffer_load_dwordx4 v173, s[36:39], s84 offen offset:1024 lds\n"
    "buffer_load_dwordx4 v174, s[36:39], s84 offen offset:2048 lds\n"
    "buffer_load_dwordx4 v175, s[36:39], s84 offen offset:3072 lds\n"
    "s_load_dwordx2 s[72:73], s[60:61], 0\n"
    "s_add_u32 m0, s75, 16384\n"
    "s_nop 0\n"
    "buffer_load_dwordx4 v242, s[40:43], s85 offen offset:0 lds\n"
    "buffer_load_dwordx4 v243, s[40:43], s85 offen offset:1024 lds\n"
    "s_add_u32 m0, s75, 18432\n"
    "s_nop 0\n"
    "buffer_load_dwordx4 v242, s[44:47], s85 offen offset:0 lds\n"
    "buffer_load_dwordx4 v243, s[44:47], s85 offen offset:1024 lds\n"
    "s_mov_b32 m0, s16\n"
    "s_nop 0\n"
    "buffer_load_dwordx4 v248, s[68:71], 0 offen lds\n"
    "s_add_u32 m0, s16, 3072\n"
    "s_nop 0\n"
    "buffer_load_dwordx4 v248, s[68:71], 0 offen offset:1024 lds\n"
    "v_and_b32 v104, 31, v96\n"
    "v_lshrrev_b32 v105, 5, v96\n"
    "v_bfe_u32 v106, v96, 1, 3\n"
    "v_xor_b32 v106, v106, v105\n"
    "v_lshlrev_b32 v106, 4, v106\n"
    "v_lshl_add_u32 v106, v104, 7, v106\n"
    "v_add_u32 v176, s75, v106\n"
    "v_xor_b32 v177, 32, v176\n"
    "v_xor_b32 v178, 64, v176\n"
    "v_xor_b32 v179, 96, v176\n"
    "v_bfe_u32 v107, v96, 2, 2\n"
    "v_lshl_add_u32 v108, v105, 3, v107\n"
    "v_bfe_u32 v109, v96, 4, 1\n"
    "v_bfe_u32 v110, v96, 1, 1\n"
    "v_lshl_add_u32 v109, v109, 1, v110\n"
    "v_bfe_u32 v110, v96, 3, 1\n"
    "v_lshl_or_b32 v110, v110, 2, v109\n"
    "v_lshlrev_b32 v110, 4, v110\n"
    "v_and_b32 v109, 1, v96\n"
    "v_lshl_or_b32 v110, v109, 3, v110\n"
    "v_lshl_add_u32 v236, v108, 7, v110\n"
    "v_xor_b32 v237, 64, v236\n"
    "v_lshlrev_b32 v111, 4, v105\n"
    "v_add_u32 v188, s16, v111\n"
    "v_lshrrev_b32 v109, 2, v96\n"
    "v_xor_b32 v109, v109, v99\n"
    "v_and_b32 v109, 1, v109\n"
    "v_and_or_b32 v109, v96, 3, v109\n"
    "v_cmp_eq_u32 vcc, 0, v109\n"
    "v_mov_b32 v109, 151616\n"
    "s_nop 0\n"
    "v_cndmask_b32 v188, v109, v188, vcc\n"
    "s_add_u32 m0, s75, 32768\n"
    "s_add_u32 s14, s84, 128\n"
    "buffer_load_dwordx4 v172, s[36:39], s14 offen offset:0 lds\n"
    "buffer_load_dwordx4 v173, s[36:39], s14 offen offset:1024 lds\n"
    "buffer_load_dwordx4 v174, s[36:39], s14 offen offset:2048 lds\n"
    "buffer_load_dwordx4 v175, s[36:39], s14 offen offset:3072 lds\n"
    "s_add_u32 m0, s75, 49152\n"
    "s_add_u32 s15, s85, 229376\n"
    "buffer_load_dwordx4 v242, s[40:43], s15 offen offset:0 lds\n"
    "buffer_load_dwordx4 v243, s[40:43], s15 offen offset:1024 lds\n"
    "s_add_u32 m0, s75, 51200\n"
    "s_nop 0\n"
    "buffer_load_dwordx4 v242, s[44:47], s15 offen offset:0 lds\n"
    "buffer_load_dwordx4 v243, s[44:47], s15 offen offset:1024 lds\n"
    "s_lshr_b32 s12, s48, 7\n"
    "s_sub_u32 s12, 3, s12\n"
    "s_and_b32 s12, s12, 7\n"
    "s_and_b32 s13, s21, 7\n"
    "s_and_b32 s14, s18, 1\n"
    "s_lshl1_add_u32 s13, s13, s14\n"
    "s_lshl2_add_u32 s13, s13, s74\n"
    "s_mul_i32 s13, s13, 3072\n"
    "v_mul_u32_u24 v220, 196608, v98\n"
    "v_lshl_add_u32 v220, v97, 10, v220\n"
    "s_lshl_b32 s14, s12, 7\n"
    "s_add_u32 s13, s13, s14\n"
    "s_add_u32 s13, s13, 1024\n"
    "v_add_u32 v220, s13, v220\n"
    "s_lshr_b32 s12, s12, 1\n"
    "s_cmp_eq_u32 s12, s22\n"
    "s_cselect_b32 s12, 16, 0\n"
    "v_cmp_le_u32 s[14:15], 2, v98\n"
    "v_mov_b32 v221, -1.0\n"
    "v_cmp_gt_u32 vcc, s12, v96\n"
    "s_nop 0\n"
    "s_and_b64 vcc, vcc, s[14:15]\n"
    "v_cndmask_b32 v220, v221, v220, vcc\n"
    "buffer_load_dword v220, v220, s[48:51], 0 offen\n"
    "s_lshr_b32 s12, s20, 3\n"
    "s_and_b32 s13, s20, 1\n"
    "s_bitcmp1_b32 s74, 0\n"
    "s_cselect_b64 s[80:81], s[44:45], s[40:41]\n"
    "s_mov_b64 s[82:83], s[42:43]\n"
    "s_lshr_b32 s14, s74, 1\n"
    "s_lshl1_add_u32 s14, s12, s14\n"
    "s_lshl_b32 s14, s14, 3\n"
    "s_add_u32 s14, s14, s13\n"
    "v_and_b32 v229, 3, v96\n"
    "v_lshl_add_u32 v229, v229, 1, s14\n"
    "v_lshrrev_b32 v231, 2, v96\n"
    "v_min_u32 v231, 4, v231\n"
    "s_lshl2_add_u32 s14, s22, 1\n"
    "s_mul_i32 s15, s22, 3\n"
    "s_add_u32 s15, s15, 14\n"
    "v_lshl_add_u32 v232, v231, 1, s14\n"
    "v_cmp_gt_u32 vcc, 2, v231\n"
    "v_add_u32 v231, s15, v231\n"
    "s_nop 0\n"
    "v_cndmask_b32 v231, v231, v232, vcc\n"
    "v_mul_u32_u24 v229, 28, v229\n"
    "v_add_lshl_u32 v229, v229, v231, 7\n"
    "v_and_b32 v231, 3, v96\n"
    "s_lshl_b32 s14, s13, 2\n"
    "v_add_u32 v231, s14, v231\n"
    "v_lshrrev_b32 v232, 2, v96\n"
    "s_cmp_eq_u32 s74, 2\n"
    "s_cbranch_scc1 LtqA\n"
    "s_cmp_eq_u32 s74, 3\n"
    "s_cbranch_scc1 LtoA\n"
    "s_mov_b64 s[76:77], s[52:53]\n"
    "s_mov_b64 s[78:79], s[54:55]\n"
    "s_lshl1_add_u32 s14, s12, s74\n"
    "s_mul_i32 s14, s14, 7\n"
    "v_min_u32 v232, 6, v232\n"
    "v_add_u32 v232, s14, v232\n"
    "s_lshl_b32 s14, s22, 8\n"
    "s_mul_i32 s15, s22, 192\n"
    "s_add_u32 s15, s15, 768\n"
    "v_cmp_gt_u32 vcc, 0x100, v232\n"
    "v_add_u32 v230, s14, v232\n"
    "v_add_u32 v232, s15, v232\n"
    "v_cndmask_b32 v232, v232, v230, vcc\n"
    "v_lshlrev_b32 v232, 3, v232\n"
    "s_branch LtdA\n"
    "LtqA:\n"
    "s_mov_b64 s[76:77], s[48:49]\n"
    "s_mov_b64 s[78:79], s[50:51]\n"
    "s_lshl_b32 s14, s12, 4\n"
    "s_lshl_b32 s15, s22, 1\n"
    "s_add_u32 s14, s14, s13\n"
    "v_and_b32 v232, 14, v96\n"
    "v_and_b32 v231, 1, v96\n"
    "v_add_u32 v232, s14, v232\n"
    "v_add_u32 v231, s15, v231\n"
    "s_lshr_b32 s14, s48, 7\n"
    "s_sub_u32 s14, 3, s14\n"
    "s_and_b32 s14, s14, 7\n"
    "v_cmp_eq_u32 vcc, s14, v231\n"
    "s_nop 0\n"
    "s_and_b32 vcc_lo, vcc_lo, 0xffff0000\n"
    "s_mov_b32 vcc_hi, 0\n"
    "v_cndmask_b32 v230, 0, 1, vcc\n"
    "v_xor_b32 v232, v232, v230\n"
    "v_mul_u32_u24 v232, 24, v232\n"
    "s_branch LtdA\n"
    "LtoA:\n"
    "s_mov_b64 s[76:77], s[56:57]\n"
    "s_mov_b64 s[78:79], s[58:59]\n"
    "s_lshl_b32 s14, s22, 5\n"
    "s_add_u32 s14, s14, s12\n"
    "s_lshl_b32 s14, s14, 2\n"
    "v_and_b32 v232, 3, v232\n"
    "v_add_lshl_u32 v232, s14, v232, 3\n"
    "LtdA:\n"
    "v_add_lshl_u32 v230, v232, v231, 7\n"
    "v_lshlrev_b32 v233, 7, v98\n"
    "v_lshl_add_u32 v233, v97, 10, v233\n"
    "s_lshl_b32 s14, s23, 13\n"
    "s_add_u32 s14, s14, s84\n"
    "s_lshl_b32 s15, s22, 11\n"
    "s_add_u32 s14, s14, s15\n"
    "v_add_u32 v233, s14, v233\n"
    "v_mov_b32 v235, -1.0\n"
    "v_cmp_le_u32 vcc, 2, v98\n"
    "s_nop 1\n"
    "v_cndmask_b32 v233, v235, v233, vcc\n"
    "v_cmp_gt_u32 vcc, 16, v96\n"
    "s_nop 1\n"
    "v_cndmask_b32 v233, v235, v233, vcc\n"
    "s_and_b32 s14, s21, 7\n"
    "s_lshr_b32 s15, s74, 1\n"
    "s_lshl1_add_u32 s14, s14, s15\n"
    "s_and_b32 s15, s20, 1\n"
    "s_lshl1_add_u32 s14, s14, s15\n"
    "s_mul_i32 s14, s14, 43008\n"
    "s_lshl_b32 s15, s18, 8\n"
    "s_add_u32 s14, s14, s15\n"
    "s_add_u32 s14, s14, 458752\n"
    "v_mov_b32 v234, 3584\n"
    "v_mad_u32_u24 v234, v96, v234, s14\n"
    "v_cmp_gt_u32 vcc, 12, v96\n"
    "s_nop 1\n"
    "v_cndmask_b32 v234, v235, v234, vcc\n"
    "buffer_load_dword v234, v234, s[80:83], 0 offen sc1\n"
    "buffer_load_dword v233, v233, s[36:39], 0 offen sc1\n"
    "s_waitcnt vmcnt(11)\n"
    "s_barrier\n"
    "ds_read_b128 a[0:3], v176 offset:0\n"
    "ds_read_b64_tr_b16 a[128:129], v236 offset:16384\n"
    "ds_read_b64_tr_b16 a[130:131], v236 offset:16896\n"
    "ds_read_b64_tr_b16 a[132:133], v237 offset:16384\n"
    "ds_read_b64_tr_b16 a[134:135], v237 offset:16896\n"
    "ds_read_b64_tr_b16 a[136:137], v236 offset:18432\n"
    "ds_read_b64_tr_b16 a[138:139], v236 offset:18944\n"
    "ds_read_b64_tr_b16 a[140:141], v237 offset:18432\n"
    "ds_read_b64_tr_b16 a[142:143], v237 offset:18944\n"
    "ds_read_b128 a[176:179], v188 offset:0\n"
    "ds_read_b128 a[4:7], v177 offset:0\n"
    "ds_read_b64_tr_b16 a[144:145], v236 offset:20480\n"
    "ds_read_b64_tr_b16 a[146:147], v236 offset:20992\n"
    "ds_read_b64_tr_b16 a[148:149], v237 offset:20480\n"
    "ds_read_b64_tr_b16 a[150:151], v237 offset:20992\n"
    "ds_read_b64_tr_b16 a[152:153], v236 offset:22528\n"
    "ds_read_b64_tr_b16 a[154:155], v236 offset:23040\n"
    "ds_read_b64_tr_b16 a[156:157], v237 offset:22528\n"
    "ds_read_b64_tr_b16 a[158:159], v237 offset:23040\n"
    "ds_read_b128 a[180:183], v188 offset:32\n"
    "s_waitcnt lgkmcnt(10)\n"
    "v_mfma_f32_32x32x16_bf16 v[0:15], a[128:131], a[0:3], 0\n"
    "ds_read_b128 a[8:11], v178 offset:0\n"
    "ds_read_b64_tr_b16 a[160:161], v236 offset:24576\n"
    "s_add_u32 m0, s75, 65536\n"
    "s_add_u32 s12, s84, 256\n"
    "buffer_load_dwordx4 v172, s[36:39], s12 offen offset:0 lds\n"
    "v_mfma_f32_32x32x16_bf16 v[16:31], a[132:135], a[0:3], 0\n"
    "ds_read_b64_tr_b16 a[162:163], v236 offset:25088\n"
    "ds_read_b64_tr_b16 a[164:165], v237 offset:24576\n"
    "ds_read_b64_tr_b16 a[166:167], v237 offset:25088\n"
    "buffer_load_dwordx4 v173, s[36:39], s12 offen offset:1024 lds\n"
    "v_mfma_f32_32x32x16_bf16 v[32:47], a[136:139], a[0:3], 0\n"
    "ds_read_b64_tr_b16 a[168:169], v236 offset:26624\n"
    "ds_read_b64_tr_b16 a[170:171], v236 offset:27136\n"
    "buffer_load_dwordx4 v174, s[36:39], s12 offen offset:2048 lds\n"
    "buffer_load_dwordx4 v175, s[36:39], s12 offen offset:3072 lds\n"
    "v_mfma_f32_32x32x16_bf16 v[48:63], a[140:143], a[0:3], 0\n"
    "ds_read_b64_tr_b16 a[172:173], v237 offset:26624\n"
    "ds_read_b64_tr_b16 a[174:175], v237 offset:27136\n"
    "ds_read_b128 a[184:187], v188 offset:64\n"
    "s_add_u32 m0, s75, 81920\n"
    "s_add_u32 s13, s85, 458752\n"
    "buffer_load_dwordx4 v242, s[40:43], s13 offen offset:0 lds\n"
    "v_mfma_f32_16x16x32_bf16 v[156:159], a[176:179], a[0:3], 0\n"
    "buffer_load_dwordx4 v243, s[40:43], s13 offen offset:1024 lds\n"
    "s_waitcnt lgkmcnt(10)\n"
    "v_mfma_f32_32x32x16_bf16 v[0:15], a[144:147], a[4:7], v[0:15]\n"
    "ds_read_b128 a[12:15], v179 offset:0\n"
    "ds_read_b64_tr_b16 a[128:129], v236 offset:28672\n"
    "s_add_u32 m0, s75, 83968\n"
    "s_nop 0\n"
    "buffer_load_dwordx4 v242, s[44:47], s13 offen offset:0 lds\n"
    "v_mfma_f32_32x32x16_bf16 v[16:31], a[148:151], a[4:7], v[16:31]\n"
    "ds_read_b64_tr_b16 a[130:131], v236 offset:29184\n"
    "ds_read_b64_tr_b16 a[132:133], v237 offset:28672\n"
    "ds_read_b64_tr_b16 a[134:135], v237 offset:29184\n"
    "buffer_load_dwordx4 v243, s[44:47], s13 offen offset:1024 lds\n"
    "v_mfma_f32_32x32x16_bf16 v[32:47], a[152:155], a[4:7], v[32:47]\n"
    "ds_read_b64_tr_b16 a[136:137], v236 offset:30720\n"
    "ds_read_b64_tr_b16 a[138:139], v236 offset:31232\n"
    "v_add_u32 v180, 0x10000, v176\n"
    "v_add_u32 v181, 0x10000, v177\n"
    "v_add_u32 v182, 0x10000, v178\n"
    "v_add_u32 v183, 0x10000, v179\n"
    "v_mfma_f32_32x32x16_bf16 v[48:63], a[156:159], a[4:7], v[48:63]\n"
    "ds_read_b64_tr_b16 a[140:141], v237 offset:30720\n"
    "ds_read_b64_tr_b16 a[142:143], v237 offset:31232\n"
    "ds_read_b128 a[176:179], v188 offset:96\n"
    "v_add_u32 v238, 0x10000, v236\n"
    "v_add_u32 v239, 0x10000, v237\n"
    "v_add_u32 v184, 0x20000, v176\n"
    "v_mfma_f32_16x16x32_bf16 v[156:159], a[180:183], a[4:7], v[156:159]\n"
    "v_add_u32 v185, 0x20000, v177\n"
    "v_add_u32 v186, 0x20000, v178\n"
    "s_waitcnt vmcnt(11) lgkmcnt(10)\n"
    "s_barrier\n"
    "v_mfma_f32_32x32x16_bf16 v[0:15], a[160:163], a[8:11], v[0:15]\n"
    "ds_read_b128 a[16:19], v176 offset:32768\n"
    "ds_read_b64_tr_b16 a[144:145], v236 offset:49152\n"
    "s_add_u32 m0, s75, 98304\n"
    "s_add_u32 s14, s84, 384\n"
    "buffer_load_dwordx4 v172, s[36:39], s14 offen offset:0 lds\n"
    "v_mfma_f32_32x32x16_bf16 v[16:31], a[164:167], a[8:11], v[16:31]\n"
    "ds_read_b64_tr_b16 a[146:147], v236 offset:49664\n"
    "ds_read_b64_tr_b16 a[148:149], v237 offset:49152\n"
    "ds_read_b64_tr_b16 a[150:151], v237 offset:49664\n"
    "buffer_load_dwordx4 v173, s[36:39], s14 offen offset:1024 lds\n"
    "v_mfma_f32_32x32x16_bf16 v[32:47], a[168:171], a[8:11], v[32:47]\n"
    "ds_read_b64_tr_b16 a[152:153], v236 offset:51200\n"
    "ds_read_b64_tr_b16 a[154:155], v236 offset:51712\n"
    "buffer_load_dwordx4 v174, s[36:39], s14 offen offset:2048 lds\n"
    "buffer_load_dwordx4 v175, s[36:39], s14 offen offset:3072 lds\n"
    "v_mfma_f32_32x32x16_bf16 v[48:63], a[172:175], a[8:11], v[48:63]\n"
    "ds_read_b64_tr_b16 a[156:157], v237 offset:51200\n"
    "ds_read_b64_tr_b16 a[158:159], v237 offset:51712\n"
    "ds_read_b128 a[180:183], v188 offset:128\n"
    "s_add_u32 m0, s75, 114688\n"
    "s_add_u32 s15, s85, 688128\n"
    "buffer_load_dwordx4 v242, s[40:43], s15 offen offset:0 lds\n"
    "v_mfma_f32_16x16x32_bf16 v[156:159], a[184:187], a[8:11], v[156:159]\n"
    "buffer_load_dwordx4 v243, s[40:43], s15 offen offset:1024 lds\n"
    "s_waitcnt lgkmcnt(10)\n"
    "v_mfma_f32_32x32x16_bf16 v[0:15], a[128:131], a[12:15], v[0:15]\n"
    "ds_read_b128 a[20:23], v177 offset:32768\n"
    "ds_read_b64_tr_b16 a[160:161], v236 offset:53248\n"
    "s_add_u32 m0, s75, 116736\n"
    "s_nop 0\n"
    "buffer_load_dwordx4 v242, s[44:47], s15 offen offset:0 lds\n"
    "v_mfma_f32_32x32x16_bf16 v[16:31], a[132:135], a[12:15], v[16:31]\n"
    "ds_read_b64_tr_b16 a[162:163], v236 offset:53760\n"
    "ds_read_b64_tr_b16 a[164:165], v237 offset:53248\n"
    "ds_read_b64_tr_b16 a[166:167], v237 offset:53760\n"
    "buffer_load_dwordx4 v243, s[44:47], s15 offen offset:1024 lds\n"
    "v_mfma_f32_32x32x16_bf16 v[32:47], a[136:139], a[12:15], v[32:47]\n"
    "ds_read_b64_tr_b16 a[168:169], v236 offset:55296\n"
    "ds_read_b64_tr_b16 a[170:171], v236 offset:55808\n"
    "v_add_u32 v187, 0x20000, v179\n"
    "v_add_u32 v240, 0x20000, v236\n"
    "v_add_u32 v241, 0x20000, v237\n"
    "v_mov_b32 v102, 3072\n"
    "v_mfma_f32_32x32x16_bf16 v[48:63], a[140:143], a[12:15], v[48:63]\n"
    "ds_read_b64_tr_b16 a[172:173], v237 offset:55296\n"
    "ds_read_b64_tr_b16 a[174:175], v237 offset:55808\n"
    "ds_read_b128 a[184:187], v188 offset:160\n"
    "v_mad_u32_u24 v244, v97, v102, v101\n"
    "v_add_u32 v245, 23552, v244\n"
    "s_waitcnt lgkmcnt(0)\n"
    "s_add_u32 s72, s72, 0x7f4a7c15\n"
    "s_addc_u32 s73, s73, 0x9e3779b9\n"
    "v_mfma_f32_16x16x32_bf16 v[156:159], a[176:179], a[12:15], v[156:159]\n"
    "s_lshl3_add_u32 s16, s20, 4096\n"
    "s_cmp_eq_u32 s74, 0\n"
    "s_cselect_b32 s16, s16, -1.0\n"
    "v_mfma_f32_32x32x16_bf16 v[0:15], a[144:147], a[16:19], v[0:15]\n"
    "ds_read_b128 a[24:27], v178 offset:32768\n"
    "ds_read_b64_tr_b16 a[128:129], v236 offset:57344\n"
    "v_mov_b32 v112, -1.0\n"
    "v_mov_b32 v114, s72\n"
    "v_writelane_b32 v112, s16, 0\n"
    "v_mov_b32 v115, s73\n"
    "v_mfma_f32_32x32x16_bf16 v[16:31], a[148:151], a[16:19], v[16:31]\n"
    "ds_read_b64_tr_b16 a[130:131], v236 offset:57856\n"
    "ds_read_b64_tr_b16 a[132:133], v237 offset:57344\n"
    "ds_read_b64_tr_b16 a[134:135], v237 offset:57856\n"
    "s_lshl_b32 s68, s72, 1\n"
    "buffer_store_dwordx2 v[114:115], v112, s[60:63], 0 offen sc1\n"
    "v_mfma_f32_32x32x16_bf16 v[32:47], a[152:155], a[16:19], v[32:47]\n"
    "ds_read_b64_tr_b16 a[136:137], v236 offset:59392\n"
    "ds_read_b64_tr_b16 a[138:139], v236 offset:59904\n"
    "s_add_u32 s69, s68, s72\n"
    "s_lshl_b32 s70, s72, 4\n"
    "s_mov_b32 s98, 0x0000ffff\n"
    "s_mov_b32 s99, 0x0000ffff\n"
    "v_mfma_f32_32x32x16_bf16 v[48:63], a[156:159], a[16:19], v[48:63]\n"
    "ds_read_b64_tr_b16 a[140:141], v237 offset:59392\n"
    "ds_read_b64_tr_b16 a[142:143], v237 offset:59904\n"
    "ds_read_b128 a[176:179], v188 offset:192\n"
    "s_add_u32 m0, s75, 131072\n"
    "s_add_u32 s12, s84, 512\n"
    "buffer_load_dwordx4 v172, s[36:39], s12 offen offset:0 lds\n"
    "v_mfma_f32_16x16x32_bf16 v[156:159], a[180:183], a[16:19], v[156:159]\n"
    "s_lshr_b32 s16, s74, 1\n"
    "s_and_b32 s17, s74, 1\n"
    "v_mfma_f32_32x32x16_bf16 v[0:15], a[160:163], a[20:23], v[0:15]\n"
    "ds_read_b128 a[28:31], v179 offset:32768\n"
    "ds_read_b64_tr_b16 a[144:145], v236 offset:61440\n"
    "s_mul_i32 s86, s17, 98304\n"
    "s_mul_i32 s16, s16, 196608\n"
    "s_add_u32 s86, s86, s16\n"
    "s_lshl_b32 s17, s18, 7\n"
    "v_mfma_f32_32x32x16_bf16 v[16:31], a[164:167], a[20:23], v[16:31]\n"
    "ds_read_b64_tr_b16 a[146:147], v236 offset:61952\n"
    "ds_read_b64_tr_b16 a[148:149], v237 offset:61440\n"
    "ds_read_b64_tr_b16 a[150:151], v237 offset:61952\n"
    "buffer_load_dwordx4 v173, s[36:39], s12 offen offset:1024 lds\n"
    "v_mfma_f32_32x32x16_bf16 v[32:47], a[168:171], a[20:23], v[32:47]\n"
    "ds_read_b64_tr_b16 a[152:153], v236 offset:63488\n"
    "ds_read_b64_tr_b16 a[154:155], v236 offset:64000\n"
    "s_add_u32 s86, s86, s17\n"
    "s_add_u32 s87, s85, 128\n"
    "v_mov_b32 v204, 0x358637bd\n"
    "v_mov_b32 v205, 0x358637bd\n"
    "v_mfma_f32_32x32x16_bf16 v[48:63], a[172:175], a[20:23], v[48:63]\n"
    "ds_read_b64_tr_b16 a[156:157], v237 offset:63488\n"
    "ds_read_b64_tr_b16 a[158:159], v237 offset:64000\n"
    "ds_read_b128 a[180:183], v188 offset:224\n"
    "buffer_load_dwordx4 v174, s[36:39], s12 offen offset:2048 lds\n"
    "v_mfma_f32_16x16x32_bf16 v[156:159], a[184:187], a[20:23], v[156:159]\n"
    "v_mov_b32 v206, 0x358637bd\n"
    "v_mov_b32 v207, 0x358637bd\n"
    "s_waitcnt vmcnt(12) lgkmcnt(10)\n"
    "s_barrier\n"
    "v_mfma_f32_32x32x16_bf16 v[0:15], a[128:131], a[24:27], v[0:15]\n"
    "ds_read_b128 a[32:35], v180 offset:0\n"
    "ds_read_b64_tr_b16 a[160:161], v238 offset:16384\n"
    "buffer_load_dwordx4 v175, s[36:39], s12 offen offset:3072 lds\n"
    "s_add_u32 m0, s75, 0\n"
    "v_mfma_f32_32x32x16_bf16 v[16:31], a[132:135], a[24:27], v[16:31]\n"
    "ds_read_b64_tr_b16 a[162:163], v238 offset:16896\n"
    "ds_read_b64_tr_b16 a[164:165], v239 offset:16384\n"
    "ds_read_b64_tr_b16 a[166:167], v239 offset:16896\n"
    "s_add_u32 s13, s85, 917504\n"
    "buffer_load_dwordx4 v242, s[40:43], s13 offen offset:0 lds\n"
    "v_mfma_f32_32x32x16_bf16 v[32:47], a[136:139], a[24:27], v[32:47]\n"
    "ds_read_b64_tr_b16 a[168:169], v238 offset:18432\n"
    "ds_read_b64_tr_b16 a[170:171], v238 offset:18944\n"
    "buffer_load_dwordx4 v243, s[40:43], s13 offen offset:1024 lds\n"
    "s_add_u32 m0, s75, 2048\n"
    "v_mfma_f32_32x32x16_bf16 v[48:63], a[140:143], a[24:27], v[48:63]\n"
    "ds_read_b64_tr_b16 a[172:173], v239 offset:18432\n"
    "ds_read_b64_tr_b16 a[174:175], v239 offset:18944\n"
    "ds_read_b128 a[184:187], v188 offset:256\n"
    "buffer_load_dwordx4 v242, s[44:47], s13 offen offset:0 lds\n"
    "v_mfma_f32_16x16x32_bf16 v[156:159], a[176:179], a[24:27], v[156:159]\n"
    "buffer_load_dwordx4 v243, s[44:47], s13 offen offset:1024 lds\n"
    "s_waitcnt lgkmcnt(10)\n"
    "v_mfma_f32_32x32x16_bf16 v[0:15], a[144:147], a[28:31], v[0:15]\n"
    "ds_read_b128 a[36:39], v181 offset:0\n"
    "ds_read_b64_tr_b16 a[128:129], v238 offset:20480\n"
    "v_mov_b32 v208, 0x358637bd\n"
    "v_mov_b32 v209, 0x358637bd\n"
    "v_mov_b32 v210, 0x358637bd\n"
    "v_mov_b32 v211, 0x358637bd\n"
    "v_mfma_f32_32x32x16_bf16 v[16:31], a[148:151], a[28:31], v[16:31]\n"
    "ds_read_b64_tr_b16 a[130:131], v238 offset:20992\n"
    "ds_read_b64_tr_b16 a[132:133], v239 offset:20480\n"
    "ds_read_b64_tr_b16 a[134:135], v239 offset:20992\n"
    "v_mov_b32 v212, 0x358637bd\n"
    "v_mov_b32 v213, 0x358637bd\n"
    "v_mov_b32 v214, 0x358637bd\n"
    "v_mfma_f32_32x32x16_bf16 v[32:47], a[152:155], a[28:31], v[32:47]\n"
    "ds_read_b64_tr_b16 a[136:137], v238 offset:22528\n"
    "ds_read_b64_tr_b16 a[138:139], v238 offset:23040\n"
    "v_mov_b32 v215, 0x358637bd\n"
    "v_mov_b32 v216, 0x358637bd\n"
    "v_mov_b32 v217, 0x358637bd\n"
    "v_mov_b32 v218, 0x358637bd\n"
    "v_mfma_f32_32x32x16_bf16 v[48:63], a[156:159], a[28:31], v[48:63]\n"
    "ds_read_b64_tr_b16 a[140:141], v239 offset:22528\n"
    "ds_read_b64_tr_b16 a[142:143], v239 offset:23040\n"
    "ds_read_b128 a[176:179], v188 offset:288\n"
    "v_mov_b32 v219, 0x358637bd\n"
    "v_lshlrev_b32 v144, 2, v96\n"
    "v_mov_b32 v155, 0xb5c1a2a0\n"
    "v_mfma_f32_16x16x32_bf16 v[156:159], a[180:183], a[28:31], v[156:159]\n"
    "s_mov_b32 s96, 0x358637bd\n"
    "s_mov_b32 s4, 0x3fb8aa3b\n"
    "s_waitcnt lgkmcnt(10)\n"
    "v_mfma_f32_32x32x16_bf16 v[0:15], a[160:163], a[32:35], v[0:15]\n"
    "ds_read_b128 a[40:43], v182 offset:0\n"
    "ds_read_b64_tr_b16 a[144:145], v238 offset:24576\n"
    "buffer_load_dword v229, v229, s[80:83], 0 offen sc1\n"
    "v_lshl_add_u32 v246, v97, 10, v101\n"
    "v_add_u32 v247, 7168, v246\n"
    "v_mfma_f32_32x32x16_bf16 v[16:31], a[164:167], a[32:35], v[16:31]\n"
    "ds_read_b64_tr_b16 a[146:147], v238 offset:25088\n"
    "ds_read_b64_tr_b16 a[148:149], v239 offset:24576\n"
    "ds_read_b64_tr_b16 a[150:151], v239 offset:25088\n"
    "s_lshl2_add_u32 s16, s21, s22\n"
    "s_lshl2_add_u32 s16, s16, s74\n"
    "s_lshl_b32 s0, s16, 7\n"
    "v_mfma_f32_32x32x16_bf16 v[32:47], a[168:171], a[32:35], v[32:47]\n"
    "ds_read_b64_tr_b16 a[152:153], v238 offset:26624\n"
    "ds_read_b64_tr_b16 a[154:155], v238 offset:27136\n"
    "s_add_u32 s0, s0, 589824\n"
    "s_lshl2_add_u32 s1, s24, s0\n"
    "s_lshl2_add_u32 s2, s20, s74\n"
    "s_lshl_b32 s2, s2, 7\n"
    "v_mfma_f32_32x32x16_bf16 v[48:63], a[172:175], a[32:35], v[48:63]\n"
    "ds_read_b64_tr_b16 a[156:157], v239 offset:26624\n"
    "ds_read_b64_tr_b16 a[158:159], v239 offset:27136\n"
    "ds_read_b128 a[180:183], v188 offset:320\n"
    "s_add_u32 s2, s2, 655360\n"
    "s_mul_i32 s88, s16, 41472\n"
    "s_add_u32 s88, s88, 1048576\n"
    "v_mfma_f32_16x16x32_bf16 v[156:159], a[184:187], a[32:35], v[156:159]\n"
    "s_mul_i32 s16, s24, 4608\n"
    "s_add_u32 s89, s88, s16\n"
    "s_waitcnt lgkmcnt(10)\n"
    "v_mfma_f32_32x32x16_bf16 v[0:15], a[128:131], a[36:39], v[0:15]\n"
    "ds_read_b128 a[44:47], v183 offset:0\n"
    "ds_read_b64_tr_b16 a[160:161], v238 offset:28672\n"
    "s_sub_u32 s90, s88, s16\n"
    "s_add_u32 s90, s90, 4608\n"
    "s_lshl2_add_u32 s16, s18, s74\n"
    "s_lshl_b32 s100, s16, 14\n"
    "v_mfma_f32_32x32x16_bf16 v[16:31], a[132:135], a[36:39], v[16:31]\n"
    "ds_read_b64_tr_b16 a[162:163], v238 offset:29184\n"
    "ds_read_b64_tr_b16 a[164:165], v239 offset:28672\n"
    "ds_read_b64_tr_b16 a[166:167], v239 offset:29184\n"
    "s_add_u32 m0, s75, 16384\n"
    "s_add_u32 s14, s84, 640\n"
    "buffer_load_dwordx4 v172, s[36:39], s14 offen offset:0 lds\n"
    "v_mfma_f32_32x32x16_bf16 v[32:47], a[136:139], a[36:39], v[32:47]\n"
    "ds_read_b64_tr_b16 a[168:169], v238 offset:30720\n"
    "ds_read_b64_tr_b16 a[170:171], v238 offset:31232\n"
    "s_add_u32 s100, s100, 65536\n"
    "s_lshl_b32 s3, s16, 7\n"
    "s_add_u32 s3, s3, 622592\n"
    "s_lshl_b32 s16, s74, 14\n"
    "v_mfma_f32_32x32x16_bf16 v[48:63], a[140:143], a[36:39], v[48:63]\n"
    "ds_read_b64_tr_b16 a[172:173], v239 offset:30720\n"
    "ds_read_b64_tr_b16 a[174:175], v239 offset:31232\n"
    "ds_read_b128 a[184:187], v188 offset:352\n"
    "buffer_load_dwordx4 v173, s[36:39], s14 offen offset:1024 lds\n"
    "v_mfma_f32_16x16x32_bf16 v[156:159], a[176:179], a[36:39], v[156:159]\n"
    "s_lshl_b32 s17, s23, 8\n"
    "s_add_u32 s16, s16, s17\n"
    "s_waitcnt vmcnt(12) lgkmcnt(10)\n"
    "s_barrier\n"
    "v_mfma_f32_32x32x16_bf16 v[0:15], a[144:147], a[40:43], v[0:15]\n"
    "ds_read_b128 a[48:51], v180 offset:32768\n"
    "ds_read_b64_tr_b16 a[128:129], v238 offset:49152\n"
    "buffer_load_dwordx4 v174, s[36:39], s14 offen offset:2048 lds\n"
    "buffer_load_dwordx4 v175, s[36:39], s14 offen offset:3072 lds\n"
    "v_mfma_f32_32x32x16_bf16 v[16:31], a[148:151], a[40:43], v[16:31]\n"
    "ds_read_b64_tr_b16 a[130:131], v238 offset:49664\n"
    "ds_read_b64_tr_b16 a[132:133], v239 offset:49152\n"
    "ds_read_b64_tr_b16 a[134:135], v239 offset:49664\n"
    "s_add_u32 m0, s75, 32768\n"
    "s_add_u32 s15, s85, 1146880\n"
    "buffer_load_dwordx4 v242, s[40:43], s15 offen offset:0 lds\n"
    "v_mfma_f32_32x32x16_bf16 v[32:47], a[152:155], a[40:43], v[32:47]\n"
    "ds_read_b64_tr_b16 a[136:137], v238 offset:51200\n"
    "ds_read_b64_tr_b16 a[138:139], v238 offset:51712\n"
    "buffer_load_dwordx4 v243, s[40:43], s15 offen offset:1024 lds\n"
    "s_add_u32 m0, s75, 34816\n"
    "v_mfma_f32_32x32x16_bf16 v[48:63], a[156:159], a[40:43], v[48:63]\n"
    "ds_read_b64_tr_b16 a[140:141], v239 offset:51200\n"
    "ds_read_b64_tr_b16 a[142:143], v239 offset:51712\n"
    "ds_read_b128 a[176:179], v188 offset:384\n"
    "buffer_load_dwordx4 v242, s[44:47], s15 offen offset:0 lds\n"
    "v_mfma_f32_16x16x32_bf16 v[156:159], a[180:183], a[40:43], v[156:159]\n"
    "buffer_load_dwordx4 v243, s[44:47], s15 offen offset:1024 lds\n"
    "s_waitcnt lgkmcnt(10)\n"
    "v_mfma_f32_32x32x16_bf16 v[0:15], a[160:163], a[44:47], v[0:15]\n"
    "ds_read_b128 a[52:55], v181 offset:32768\n"
    "ds_read_b64_tr_b16 a[144:145], v238 offset:53248\n"
    "s_lshr_b32 s19, s22, 1\n"
    "s_lshl_b32 s19, s19, 7\n"
    "s_add_u32 s16, s16, s19\n"
    "s_lshl_b32 s19, s22, 18\n"
    "v_mfma_f32_32x32x16_bf16 v[16:31], a[164:167], a[44:47], v[16:31]\n"
    "ds_read_b64_tr_b16 a[146:147], v238 offset:53760\n"
    "ds_read_b64_tr_b16 a[148:149], v239 offset:53248\n"
    "ds_read_b64_tr_b16 a[150:151], v239 offset:53760\n"
    "s_add_u32 s25, s16, s19\n"
    "s_lshl_b32 s19, s22, 17\n"
    "s_add_u32 s27, s16, s19\n"
    "v_mfma_f32_32x32x16_bf16 v[32:47], a[168:171], a[44:47], v[32:47]\n"
    "ds_read_b64_tr_b16 a[152:153], v238 offset:55296\n"
    "ds_read_b64_tr_b16 a[154:155], v238 offset:55808\n"
    "s_mul_i32 s19, s22, 196608\n"
    "s_add_u32 s26, s16, s19\n"
    "s_add_u32 s26, s26, 1048576\n"
    "s_lshl_b32 s19, s22, 6\n"
    "v_mfma_f32_32x32x16_bf16 v[48:63], a[172:175], a[44:47], v[48:63]\n"
    "ds_read_b64_tr_b16 a[156:157], v239 offset:55296\n"
    "ds_read_b64_tr_b16 a[158:159], v239 offset:55808\n"
    "ds_read_b128 a[180:183], v188 offset:416\n"
    "s_add_u32 s101, s84, s17\n"
    "s_add_u32 s101, s101, s19\n"
    "s_add_u32 s95, s101, 16384\n"
    "v_mfma_f32_16x16x32_bf16 v[156:159], a[184:187], a[44:47], v[156:159]\n"
    "s_lshl2_add_u32 s16, s21, s23\n"
    "s_mul_i32 s16, s16, 48\n"
    "s_waitcnt lgkmcnt(10)\n"
    "v_mfma_f32_32x32x16_bf16 v[0:15], a[128:131], a[48:51], v[0:15]\n"
    "ds_read_b128 a[56:59], v182 offset:32768\n"
    "ds_read_b64_tr_b16 a[160:161], v238 offset:57344\n"
    "buffer_load_dword v141, off, s[60:63], s0 sc1\n"
    "buffer_load_dword v142, off, s[60:63], s2 sc1\n"
    "v_mfma_f32_32x32x16_bf16 v[16:31], a[132:135], a[48:51], v[16:31]\n"
    "ds_read_b64_tr_b16 a[162:163], v238 offset:57856\n"
    "ds_read_b64_tr_b16 a[164:165], v239 offset:57344\n"
    "ds_read_b64_tr_b16 a[166:167], v239 offset:57856\n"
    "s_add_u32 m0, s75, 49152\n"
    "s_add_u32 s12, s84, 768\n"
    "buffer_load_dwordx4 v172, s[36:39], s12 offen offset:0 lds\n"
    "v_mfma_f32_32x32x16_bf16 v[32:47], a[136:139], a[48:51], v[32:47]\n"
    "ds_read_b64_tr_b16 a[168:169], v238 offset:59392\n"
    "ds_read_b64_tr_b16 a[170:171], v238 offset:59904\n"
    "buffer_load_dword v143, off, s[60:63], s3 sc1\n"
    "s_add_u32 s16, s16, s74\n"
    "s_mul_i32 s16, s16, 2304\n"
    "v_mfma_f32_32x32x16_bf16 v[48:63], a[140:143], a[48:51], v[48:63]\n"
    "ds_read_b64_tr_b16 a[172:173], v239 offset:59392\n"
    "ds_read_b64_tr_b16 a[174:175], v239 offset:59904\n"
    "ds_read_b128 a[184:187], v188 offset:448\n"
    "s_add_u32 s16, s16, 12582912\n"
    "s_mul_i32 s17, s22, 27648\n"
    "s_add_u32 s94, s16, s17\n"
    "v_mfma_f32_16x16x32_bf16 v[156:159], a[176:179], a[48:51], v[156:159]\n"
    "buffer_load_dwordx4 v173, s[36:39], s12 offen offset:1024 lds\n"
    "s_waitcnt lgkmcnt(10)\n"
    "v_mfma_f32_32x32x16_bf16 v[0:15], a[144:147], a[52:55], v[0:15]\n"
    "ds_read_b128 a[60:63], v183 offset:32768\n"
    "ds_read_b64_tr_b16 a[128:129], v238 offset:61440\n"
    "s_xor_b32 s17, s22, 1\n"
    "s_mul_i32 s17, s17, 27648\n"
    "s_add_u32 s17, s17, s16\n"
    "s_add_u32 s91, s17, 0\n"
    "v_mfma_f32_32x32x16_bf16 v[16:31], a[148:151], a[52:55], v[16:31]\n"
    "ds_read_b64_tr_b16 a[130:131], v238 offset:61952\n"
    "ds_read_b64_tr_b16 a[132:133], v239 offset:61440\n"
    "ds_read_b64_tr_b16 a[134:135], v239 offset:61952\n"
    "buffer_load_dwordx4 v174, s[36:39], s12 offen offset:2048 lds\n"
    "v_mfma_f32_32x32x16_bf16 v[32:47], a[152:155], a[52:55], v[32:47]\n"
    "ds_read_b64_tr_b16 a[136:137], v238 offset:63488\n"
    "ds_read_b64_tr_b16 a[138:139], v238 offset:64000\n"
    "s_xor_b32 s17, s22, 2\n"
    "s_mul_i32 s17, s17, 27648\n"
    "s_add_u32 s17, s17, s16\n"
    "s_add_u32 s92, s17, 9216\n"
    "v_mfma_f32_32x32x16_bf16 v[48:63], a[156:159], a[52:55], v[48:63]\n"
    "ds_read_b64_tr_b16 a[140:141], v239 offset:63488\n"
    "ds_read_b64_tr_b16 a[142:143], v239 offset:64000\n"
    "ds_read_b128 a[176:179], v188 offset:480\n"
    "buffer_load_dwordx4 v175, s[36:39], s12 offen offset:3072 lds\n"
    "v_mfma_f32_16x16x32_bf16 v[156:159], a[180:183], a[52:55], v[156:159]\n"
    "s_xor_b32 s17, s22, 3\n"
    "s_mul_i32 s17, s17, 27648\n"
    "s_waitcnt vmcnt(16) lgkmcnt(10)\n"
    "s_barrier\n"
    "v_mfma_f32_32x32x16_bf16 v[0:15], a[160:163], a[56:59], v[0:15]\n"
    "ds_read_b128 a[64:67], v184 offset:0\n"
    "ds_read_b64_tr_b16 a[144:145], v236 offset:0\n"
    "s_add_u32 m0, s75, 65536\n"
    "s_add_u32 s13, s85, 1376256\n"
    "buffer_load_dwordx4 v242, s[40:43], s13 offen offset:0 lds\n"
    "v_mfma_f32_32x32x16_bf16 v[16:31], a[164:167], a[56:59], v[16:31]\n"
    "ds_read_b64_tr_b16 a[146:147], v236 offset:512\n"
    "ds_read_b64_tr_b16 a[148:149], v237 offset:0\n"
    "ds_read_b64_tr_b16 a[150:151], v237 offset:512\n"
    "buffer_load_dwordx4 v243, s[40:43], s13 offen offset:1024 lds\n"
    "v_mfma_f32_32x32x16_bf16 v[32:47], a[168:171], a[56:59], v[32:47]\n"
    "ds_read_b64_tr_b16 a[152:153], v236 offset:2048\n"
    "ds_read_b64_tr_b16 a[154:155], v236 offset:2560\n"
    "s_add_u32 m0, s75, 67584\n"
    "s_nop 0\n"
    "buffer_load_dwordx4 v242, s[44:47], s13 offen offset:0 lds\n"
    "v_mfma_f32_32x32x16_bf16 v[48:63], a[172:175], a[56:59], v[48:63]\n"
    "ds_read_b64_tr_b16 a[156:157], v237 offset:2048\n"
    "ds_read_b64_tr_b16 a[158:159], v237 offset:2560\n"
    "ds_read_b128 a[180:183], v188 offset:512\n"
    "buffer_load_dwordx4 v243, s[44:47], s13 offen offset:1024 lds\n"
    "v_mfma_f32_16x16x32_bf16 v[156:159], a[184:187], a[56:59], v[156:159]\n"
    "s_add_u32 s17, s17, s16\n"
    "s_add_u32 s93, s17, 18432\n"
    "s_waitcnt lgkmcnt(10)\n"
    "v_mfma_f32_32x32x16_bf16 v[0:15], a[128:131], a[60:63], v[0:15]\n"
    "ds_read_b128 a[68:71], v185 offset:0\n"
    "ds_read_b64_tr_b16 a[160:161], v236 offset:4096\n"
    "v_lshl_add_u32 v112, v105, 2, v107\n"
    "v_and_b32 v113, 1, v96\n"
    "v_bfe_u32 v114, v96, 4, 1\n"
    "v_lshl_add_u32 v113, v113, 1, v114\n"
    "v_mfma_f32_32x32x16_bf16 v[16:31], a[132:135], a[60:63], v[16:31]\n"
    "ds_read_b64_tr_b16 a[162:163], v236 offset:4608\n"
    "ds_read_b64_tr_b16 a[164:165], v237 offset:4096\n"
    "ds_read_b64_tr_b16 a[166:167], v237 offset:4608\n"
    "v_bfe_u32 v114, v96, 3, 1\n"
    "v_lshl_or_b32 v113, v114, 2, v113\n"
    "v_lshlrev_b32 v113, 4, v113\n"
    "v_mfma_f32_32x32x16_bf16 v[32:47], a[136:139], a[60:63], v[32:47]\n"
    "ds_read_b64_tr_b16 a[168:169], v236 offset:6144\n"
    "ds_read_b64_tr_b16 a[170:171], v236 offset:6656\n"
    "v_bfe_u32 v114, v96, 1, 1\n"
    "v_lshl_or_b32 v113, v114, 3, v113\n"
    "v_lshl_add_u32 v112, v112, 7, v113\n"
    "s_and_b32 s16, s22, 1\n"
    "v_mfma_f32_32x32x16_bf16 v[48:63], a[140:143], a[60:63], v[48:63]\n"
    "ds_read_b64_tr_b16 a[172:173], v237 offset:6144\n"
    "ds_read_b64_tr_b16 a[174:175], v237 offset:6656\n"
    "ds_read_b128 a[184:187], v188 offset:544\n"
    "s_lshl_b32 s16, s16, 6\n"
    "v_xor_b32 v145, s16, v112\n"
    "v_xor_b32 v146, 64, v145\n"
    "v_mfma_f32_16x16x32_bf16 v[156:159], a[176:179], a[60:63], v[156:159]\n"
    "v_add_u32 v147, 0x10000, v145\n"
    "v_add_u32 v148, 0x10000, v146\n"
    "s_waitcnt lgkmcnt(10)\n"
    "v_mfma_f32_32x32x16_bf16 v[0:15], a[144:147], a[64:67], v[0:15]\n"
    "ds_read_b128 a[72:75], v186 offset:0\n"
    "ds_read_b64_tr_b16 a[128:129], v236 offset:8192\n"
    "buffer_load_dword v230, v230, s[76:79], 0 offen sc1\n"
    "v_add_u32 v149, 0x20000, v145\n"
    "v_add_u32 v150, 0x20000, v146\n"
    "v_mfma_f32_32x32x16_bf16 v[16:31], a[148:151], a[64:67], v[16:31]\n"
    "ds_read_b64_tr_b16 a[130:131], v236 offset:8704\n"
    "ds_read_b64_tr_b16 a[132:133], v237 offset:8192\n"
    "ds_read_b64_tr_b16 a[134:135], v237 offset:8704\n"
    "v_bfe_u32 v112, v96, 2, 1\n"
    "v_and_b32 v113, 1, v96\n"
    "v_lshlrev_b32 v113, 4, v113\n"
    "v_mfma_f32_32x32x16_bf16 v[32:47], a[152:155], a[64:67], v[32:47]\n"
    "ds_read_b64_tr_b16 a[136:137], v236 offset:10240\n"
    "ds_read_b64_tr_b16 a[138:139], v236 offset:10752\n"
    "v_mov_b32 v114, 0x3f80\n"
    "v_lshlrev_b32 v113, v113, v114\n"
    "v_xor_b32 v112, v112, v105\n"
    "v_bfe_u32 v114, v96, 3, 2\n"
    "v_mfma_f32_32x32x16_bf16 v[48:63], a[156:159], a[64:67], v[48:63]\n"
    "ds_read_b64_tr_b16 a[140:141], v237 offset:10240\n"
    "ds_read_b64_tr_b16 a[142:143], v237 offset:10752\n"
    "ds_read_b128 a[176:179], v188 offset:576\n"
    "v_bfe_u32 v115, v96, 1, 1\n"
    "v_lshl_or_b32 v114, v114, 1, v115\n"
    "v_lshl_or_b32 v114, v112, 3, v114\n"
    "v_mfma_f32_16x16x32_bf16 v[156:159], a[180:183], a[64:67], v[156:159]\n"
    "v_cmp_eq_u32 vcc, 0, v114\n"
    "s_nop 1\n"
    "v_cndmask_b32 v116, 0, v113, vcc\n"
    "s_waitcnt lgkmcnt(10)\n"
    "v_mfma_f32_32x32x16_bf16 v[0:15], a[160:163], a[68:71], v[0:15]\n"
    "ds_read_b128 a[76:79], v187 offset:0\n"
    "ds_read_b64_tr_b16 a[144:145], v236 offset:12288\n"
    "v_cmp_eq_u32 vcc, 1, v114\n"
    "s_nop 1\n"
    "v_cndmask_b32 v117, 0, v113, vcc\n"
    "v_cmp_eq_u32 vcc, 2, v114\n"
    "s_nop 1\n"
    "v_cndmask_b32 v118, 0, v113, vcc\n"
    "v_mfma_f32_32x32x16_bf16 v[16:31], a[164:167], a[68:71], v[16:31]\n"
    "ds_read_b64_tr_b16 a[146:147], v236 offset:12800\n"
    "ds_read_b64_tr_b16 a[148:149], v237 offset:12288\n"
    "ds_read_b64_tr_b16 a[150:151], v237 offset:12800\n"
    "v_cmp_eq_u32 vcc, 3, v114\n"
    "s_nop 1\n"
    "v_cndmask_b32 v119, 0, v113, vcc\n"
    "v_mfma_f32_32x32x16_bf16 v[32:47], a[168:171], a[68:71], v[32:47]\n"
    "ds_read_b64_tr_b16 a[152:153], v236 offset:14336\n"
    "ds_read_b64_tr_b16 a[154:155], v236 offset:14848\n"
    "s_add_u32 m0, s75, 81920\n"
    "s_add_u32 s14, s84, 896\n"
    "buffer_load_dwordx4 v172, s[36:39], s14 offen offset:0 lds\n"
    "v_mfma_f32_32x32x16_bf16 v[48:63], a[172:175], a[68:71], v[48:63]\n"
    "ds_read_b64_tr_b16 a[156:157], v237 offset:14336\n"
    "ds_read_b64_tr_b16 a[158:159], v237 offset:14848\n"
    "ds_read_b128 a[180:183], v188 offset:608\n"
    "v_cmp_eq_u32 vcc, 4, v114\n"
    "s_nop 1\n"
    "v_cndmask_b32 v120, 0, v113, vcc\n"
    "v_mfma_f32_16x16x32_bf16 v[156:159], a[184:187], a[68:71], v[156:159]\n"
    "buffer_load_dwordx4 v173, s[36:39], s14 offen offset:1024 lds\n"
    "s_waitcnt vmcnt(14) lgkmcnt(10)\n"
    "s_barrier\n"
    "v_mfma_f32_32x32x16_bf16 v[0:15], a[128:131], a[72:75], v[0:15]\n"
    "ds_read_b128 a[80:83], v176 offset:16384\n"
    "ds_read_b64_tr_b16 a[160:161], v236 offset:32768\n"
    "buffer_load_dwordx4 v174, s[36:39], s14 offen offset:2048 lds\n"
    "buffer_load_dwordx4 v175, s[36:39], s14 offen offset:3072 lds\n"
    "v_mfma_f32_32x32x16_bf16 v[16:31], a[132:135], a[72:75], v[16:31]\n"
    "ds_read_b64_tr_b16 a[162:163], v236 offset:33280\n"
    "ds_read_b64_tr_b16 a[164:165], v237 offset:32768\n"
    "ds_read_b64_tr_b16 a[166:167], v237 offset:33280\n"
    "s_add_u32 m0, s75, 98304\n"
    "s_add_u32 s15, s85, 1605632\n"
    "buffer_load_dwordx4 v242, s[40:43], s15 offen offset:0 lds\n"
    "v_mfma_f32_32x32x16_bf16 v[32:47], a[136:139], a[72:75], v[32:47]\n"
    "ds_read_b64_tr_b16 a[168:169], v236 offset:34816\n"
    "ds_read_b64_tr_b16 a[170:171], v236 offset:35328\n"
    "buffer_load_dwordx4 v243, s[40:43], s15 offen offset:1024 lds\n"
    "s_add_u32 m0, s75, 100352\n"
    "v_mfma_f32_32x32x16_bf16 v[48:63], a[140:143], a[72:75], v[48:63]\n"
    "ds_read_b64_tr_b16 a[172:173], v237 offset:34816\n"
    "ds_read_b64_tr_b16 a[174:175], v237 offset:35328\n"
    "ds_read_b128 a[184:187], v188 offset:640\n"
    "buffer_load_dwordx4 v242, s[44:47], s15 offen offset:0 lds\n"
    "v_mfma_f32_16x16x32_bf16 v[156:159], a[176:179], a[72:75], v[156:159]\n"
    "buffer_load_dwordx4 v243, s[44:47], s15 offen offset:1024 lds\n"
    "s_waitcnt lgkmcnt(10)\n"
    "v_mfma_f32_32x32x16_bf16 v[0:15], a[144:147], a[76:79], v[0:15]\n"
    "ds_read_b128 a[84:87], v177 offset:16384\n"
    "ds_read_b64_tr_b16 a[128:129], v236 offset:36864\n"
    "v_cmp_eq_u32 vcc, 5, v114\n"
    "s_nop 1\n"
    "v_cndmask_b32 v121, 0, v113, vcc\n"
    "v_cmp_eq_u32 vcc, 6, v114\n"
    "s_nop 1\n"
    "v_cndmask_b32 v122, 0, v113, vcc\n"
    "v_mfma_f32_32x32x16_bf16 v[16:31], a[148:151], a[76:79], v[16:31]\n"
    "ds_read_b64_tr_b16 a[130:131], v236 offset:37376\n"
    "ds_read_b64_tr_b16 a[132:133], v237 offset:36864\n"
    "ds_read_b64_tr_b16 a[134:135], v237 offset:37376\n"
    "v_cmp_eq_u32 vcc, 7, v114\n"
    "s_nop 1\n"
    "v_cndmask_b32 v123, 0, v113, vcc\n"
    "v_mfma_f32_32x32x16_bf16 v[32:47], a[152:155], a[76:79], v[32:47]\n"
    "ds_read_b64_tr_b16 a[136:137], v236 offset:38912\n"
    "ds_read_b64_tr_b16 a[138:139], v236 offset:39424\n"
    "v_accvgpr_write_b32 a208, v116\n"
    "v_accvgpr_write_b32 a209, v117\n"
    "v_accvgpr_write_b32 a210, v118\n"
    "v_accvgpr_write_b32 a211, v119\n"
    "v_mfma_f32_32x32x16_bf16 v[48:63], a[156:159], a[76:79], v[48:63]\n"
    "ds_read_b64_tr_b16 a[140:141], v237 offset:38912\n"
    "ds_read_b64_tr_b16 a[142:143], v237 offset:39424\n"
    "ds_read_b128 a[176:179], v188 offset:672\n"
    "v_accvgpr_write_b32 a212, v120\n"
    "v_accvgpr_write_b32 a213, v121\n"
    "v_accvgpr_write_b32 a214, v122\n"
    "v_mfma_f32_16x16x32_bf16 v[156:159], a[180:183], a[76:79], v[156:159]\n"
    "v_accvgpr_write_b32 a215, v123\n"
    "v_bfe_u32 v112, v96, 2, 2\n"
    "s_waitcnt lgkmcnt(10)\n"
    "v_mfma_f32_32x32x16_bf16 v[0:15], a[160:163], a[80:83], v[0:15]\n"
    "ds_read_b128 a[88:91], v178 offset:16384\n"
    "ds_read_b64_tr_b16 a[144:145], v236 offset:40960\n"
    "v_xor_b32 v112, v112, v99\n"
    "v_lshl_or_b32 v112, v112, 1, v115\n"
    "v_cmp_eq_u32 vcc, 0, v112\n"
    "s_nop 1\n"
    "v_cndmask_b32 v116, 0, v113, vcc\n"
    "v_mfma_f32_32x32x16_bf16 v[16:31], a[164:167], a[80:83], v[16:31]\n"
    "ds_read_b64_tr_b16 a[146:147], v236 offset:41472\n"
    "ds_read_b64_tr_b16 a[148:149], v237 offset:40960\n"
    "ds_read_b64_tr_b16 a[150:151], v237 offset:41472\n"
    "s_add_u32 m0, s75, 114688\n"
    "s_nop 0\n"
    "buffer_load_dwordx4 v244, s[48:51], s86 offen offset:0 lds\n"
    "v_mfma_f32_32x32x16_bf16 v[32:47], a[168:171], a[80:83], v[32:47]\n"
    "ds_read_b64_tr_b16 a[152:153], v236 offset:43008\n"
    "ds_read_b64_tr_b16 a[154:155], v236 offset:43520\n"
    "v_cmp_eq_u32 vcc, 1, v112\n"
    "s_nop 1\n"
    "v_cndmask_b32 v117, 0, v113, vcc\n"
    "v_accvgpr_write_b32 a216, v116\n"
    "v_mfma_f32_32x32x16_bf16 v[48:63], a[172:175], a[80:83], v[48:63]\n"
    "ds_read_b64_tr_b16 a[156:157], v237 offset:43008\n"
    "ds_read_b64_tr_b16 a[158:159], v237 offset:43520\n"
    "ds_read_b128 a[180:183], v188 offset:704\n"
    "buffer_load_dwordx4 v245, s[48:51], s86 offen offset:1024 lds\n"
    "v_mfma_f32_16x16x32_bf16 v[156:159], a[184:187], a[80:83], v[156:159]\n"
    "v_accvgpr_write_b32 a217, v117\n"
    "v_add_u32 v112, 147456, v248\n"
    "s_waitcnt lgkmcnt(10)\n"
    "v_mfma_f32_32x32x16_bf16 v[0:15], a[128:131], a[84:87], v[0:15]\n"
    "ds_read_b128 a[92:95], v179 offset:16384\n"
    "ds_read_b64_tr_b16 a[160:161], v236 offset:45056\n"
    "s_add_u32 s13, s86, 47104\n"
    "buffer_load_dwordx4 v244, s[48:51], s13 offen offset:2048 lds\n"
    "v_mfma_f32_32x32x16_bf16 v[16:31], a[132:135], a[84:87], v[16:31]\n"
    "ds_read_b64_tr_b16 a[162:163], v236 offset:45568\n"
    "ds_read_b64_tr_b16 a[164:165], v237 offset:45056\n"
    "ds_read_b64_tr_b16 a[166:167], v237 offset:45568\n"
    "v_and_b32 v113, 3, v96\n"
    "v_lshl_add_u32 v114, v113, 5, v111\n"
    "v_add_u32 v114, 148480, v114\n"
    "v_mfma_f32_32x32x16_bf16 v[32:47], a[136:139], a[84:87], v[32:47]\n"
    "ds_read_b64_tr_b16 a[168:169], v236 offset:47104\n"
    "ds_read_b64_tr_b16 a[170:171], v236 offset:47616\n"
    "buffer_load_dwordx4 v245, s[48:51], s13 offen offset:3072 lds\n"
    "v_add_u32 v115, 128, v114\n"
    "v_mfma_f32_32x32x16_bf16 v[48:63], a[140:143], a[84:87], v[48:63]\n"
    "ds_read_b64_tr_b16 a[172:173], v237 offset:47104\n"
    "ds_read_b64_tr_b16 a[174:175], v237 offset:47616\n"
    "ds_read_b128 a[184:187], v188 offset:736\n"
    "v_cndmask_b32 v252, v114, v112, s[98:99]\n"
    "v_cndmask_b32 v253, v112, v115, s[98:99]\n"
    "v_bfe_u32 v114, v96, 2, 1\n"
    "v_mfma_f32_16x16x32_bf16 v[156:159], a[176:179], a[84:87], v[156:159]\n"
    "v_lshlrev_b32 v114, 4, v114\n"
    "v_bfe_u32 v115, v96, 3, 1\n"
    "s_waitcnt vmcnt(13) lgkmcnt(10)\n"
    "s_barrier\n"
    "v_mfma_f32_32x32x16_bf16 v[0:15], a[144:147], a[88:91], v[0:15]\n"
    "ds_read_b128 a[96:99], v176 offset:49152\n"
    "ds_read_b64_tr_b16 a[128:129], v238 offset:0\n"
    "s_add_u32 m0, s75, 131072\n"
    "s_add_u32 s14, s86, 393216\n"
    "buffer_load_dwordx4 v244, s[48:51], s14 offen offset:0 lds\n"
    "v_mfma_f32_32x32x16_bf16 v[16:31], a[148:151], a[88:91], v[16:31]\n"
    "ds_read_b64_tr_b16 a[130:131], v238 offset:512\n"
    "ds_read_b64_tr_b16 a[132:133], v239 offset:0\n"
    "ds_read_b64_tr_b16 a[134:135], v239 offset:512\n"
    "v_lshl_or_b32 v114, v115, 3, v114\n"
    "v_lshl_or_b32 v114, v113, 1, v114\n"
    "v_bfe_u32 v115, v96, 4, 1\n"
    "v_mfma_f32_32x32x16_bf16 v[32:47], a[152:155], a[88:91], v[32:47]\n"
    "ds_read_b64_tr_b16 a[136:137], v238 offset:2048\n"
    "ds_read_b64_tr_b16 a[138:139], v238 offset:2560\n"
    "buffer_load_dwordx4 v245, s[48:51], s14 offen offset:1024 lds\n"
    "v_lshl_or_b32 v114, v115, 7, v114\n"
    "v_mfma_f32_32x32x16_bf16 v[48:63], a[156:159], a[88:91], v[48:63]\n"
    "ds_read_b64_tr_b16 a[140:141], v239 offset:2048\n"
    "ds_read_b64_tr_b16 a[142:143], v239 offset:2560\n"
    "ds_read_b128 a[176:179], v188 offset:768\n"
    "v_mov_b32 v115, 1280\n"
    "v_mad_u32_u24 v114, v105, v115, v114\n"
    "s_lshl_b32 s16, s74, 5\n"
    "v_mfma_f32_16x16x32_bf16 v[156:159], a[180:183], a[88:91], v[156:159]\n"
    "s_add_u32 s15, s14, 47104\n"
    "buffer_load_dwordx4 v244, s[48:51], s15 offen offset:2048 lds\n"
    "s_waitcnt lgkmcnt(10)\n"
    "v_mfma_f32_32x32x16_bf16 v[0:15], a[160:163], a[92:95], v[0:15]\n"
    "ds_read_b128 a[100:103], v177 offset:49152\n"
    "ds_read_b64_tr_b16 a[144:145], v238 offset:4096\n"
    "s_add_u32 s16, s16, 148480\n"
    "v_add_u32 v251, s16, v114\n"
    "s_lshr_b32 s16, s74, 1\n"
    "s_mul_i32 s16, s16, 1280\n"
    "v_mfma_f32_32x32x16_bf16 v[16:31], a[164:167], a[92:95], v[16:31]\n"
    "ds_read_b64_tr_b16 a[146:147], v238 offset:4608\n"
    "ds_read_b64_tr_b16 a[148:149], v239 offset:4096\n"
    "ds_read_b64_tr_b16 a[150:151], v239 offset:4608\n"
    "buffer_load_dwordx4 v245, s[48:51], s15 offen offset:3072 lds\n"
    "v_mfma_f32_32x32x16_bf16 v[32:47], a[168:171], a[92:95], v[32:47]\n"
    "ds_read_b64_tr_b16 a[152:153], v238 offset:6144\n"
    "ds_read_b64_tr_b16 a[154:155], v238 offset:6656\n"
    "s_and_b32 s17, s74, 1\n"
    "s_lshl3_add_u32 s16, s17, s16\n"
    "v_add_u32 v254, s16, v112\n"
    "v_mfma_f32_32x32x16_bf16 v[48:63], a[172:175], a[92:95], v[48:63]\n"
    "ds_read_b64_tr_b16 a[156:157], v239 offset:6144\n"
    "ds_read_b64_tr_b16 a[158:159], v239 offset:6656\n"
    "ds_read_b128 a[180:183], v188 offset:800\n"
    "s_add_u32 m0, s75, 0\n"
    "s_add_u32 s12, s86, 786432\n"
    "buffer_load_dwordx4 v244, s[48:51], s12 offen offset:0 lds\n"
    "v_mfma_f32_16x16x32_bf16 v[156:159], a[184:187], a[92:95], v[156:159]\n"
    "s_waitcnt lgkmcnt(10)\n"
    "v_mfma_f32_32x32x16_bf16 v[0:15], a[128:131], a[96:99], v[0:15]\n"
    "ds_read_b128 a[104:107], v178 offset:49152\n"
    "ds_read_b64_tr_b16 a[160:161], v238 offset:8192\n"
    "v_mfma_f32_32x32x16_bf16 v[16:31], a[132:135], a[96:99], v[16:31]\n"
    "ds_read_b64_tr_b16 a[162:163], v238 offset:8704\n"
    "ds_read_b64_tr_b16 a[164:165], v239 offset:8192\n"
    "ds_read_b64_tr_b16 a[166:167], v239 offset:8704\n"
    "buffer_load_dwordx4 v245, s[48:51], s12 offen offset:1024 lds\n"
    "v_mfma_f32_32x32x16_bf16 v[32:47], a[136:139], a[96:99], v[32:47]\n"
    "ds_read_b64_tr_b16 a[168:169], v238 offset:10240\n"
    "ds_read_b64_tr_b16 a[170:171], v238 offset:10752\n"
    "v_mfma_f32_32x32x16_bf16 v[48:63], a[140:143], a[96:99], v[48:63]\n"
    "ds_read_b64_tr_b16 a[172:173], v239 offset:10240\n"
    "ds_read_b64_tr_b16 a[174:175], v239 offset:10752\n"
    "ds_read_b128 a[184:187], v188 offset:832\n"
    "s_add_u32 s13, s12, 47104\n"
    "buffer_load_dwordx4 v244, s[48:51], s13 offen offset:2048 lds\n"
    "v_mfma_f32_16x16x32_bf16 v[156:159], a[176:179], a[96:99], v[156:159]\n"
    "s_waitcnt lgkmcnt(10)\n"
    "v_mfma_f32_32x32x16_bf16 v[0:15], a[144:147], a[100:103], v[0:15]\n"
    "ds_read_b128 a[108:111], v179 offset:49152\n"
    "ds_read_b64_tr_b16 a[128:129], v238 offset:12288\n"
    "buffer_load_dwordx4 v245, s[48:51], s13 offen offset:3072 lds\n"
    "v_mfma_f32_32x32x16_bf16 v[16:31], a[148:151], a[100:103], v[16:31]\n"
    "ds_read_b64_tr_b16 a[130:131], v238 offset:12800\n"
    "ds_read_b64_tr_b16 a[132:133], v239 offset:12288\n"
    "ds_read_b64_tr_b16 a[134:135], v239 offset:12800\n"
    "v_mfma_f32_32x32x16_bf16 v[32:47], a[152:155], a[100:103], v[32:47]\n"
    "ds_read_b64_tr_b16 a[136:137], v238 offset:14336\n"
    "ds_read_b64_tr_b16 a[138:139], v238 offset:14848\n"
    "v_mfma_f32_32x32x16_bf16 v[48:63], a[156:159], a[100:103], v[48:63]\n"
    "ds_read_b64_tr_b16 a[140:141], v239 offset:14336\n"
    "ds_read_b64_tr_b16 a[142:143], v239 offset:14848\n"
    "ds_read_b128 a[176:179], v188 offset:864\n"
    "v_mfma_f32_16x16x32_bf16 v[156:159], a[180:183], a[100:103], v[156:159]\n"
    "s_waitcnt vmcnt(12) lgkmcnt(10)\n"
    "s_barrier\n"
    "v_mfma_f32_32x32x16_bf16 v[0:15], a[160:163], a[104:107], v[0:15]\n"
    "ds_read_b128 a[112:115], v180 offset:16384\n"
    "ds_read_b64_tr_b16 a[144:145], v238 offset:32768\n"
    "s_add_u32 m0, s75, 16384\n"
    "s_add_u32 s14, s86, 1179648\n"
    "buffer_load_dwordx4 v244, s[48:51], s14 offen offset:0 lds\n"
    "v_mfma_f32_32x32x16_bf16 v[16:31], a[164:167], a[104:107], v[16:31]\n"
    "ds_read_b64_tr_b16 a[146:147], v238 offset:33280\n"
    "ds_read_b64_tr_b16 a[148:149], v239 offset:32768\n"
    "ds_read_b64_tr_b16 a[150:151], v239 offset:33280\n"
    "buffer_load_dwordx4 v245, s[48:51], s14 offen offset:1024 lds\n"
    "v_mfma_f32_32x32x16_bf16 v[32:47], a[168:171], a[104:107], v[32:47]\n"
    "ds_read_b64_tr_b16 a[152:153], v238 offset:34816\n"
    "ds_read_b64_tr_b16 a[154:155], v238 offset:35328\n"
    "v_mfma_f32_32x32x16_bf16 v[48:63], a[172:175], a[104:107], v[48:63]\n"
    "ds_read_b64_tr_b16 a[156:157], v239 offset:34816\n"
    "ds_read_b64_tr_b16 a[158:159], v239 offset:35328\n"
    "ds_read_b128 a[180:183], v188 offset:896\n"
    "s_add_u32 s15, s14, 47104\n"
    "buffer_load_dwordx4 v244, s[48:51], s15 offen offset:2048 lds\n"
    "v_mfma_f32_16x16x32_bf16 v[156:159], a[184:187], a[104:107], v[156:159]\n"
    "s_waitcnt lgkmcnt(10)\n"
    "v_mfma_f32_32x32x16_bf16 v[0:15], a[128:131], a[108:111], v[0:15]\n"
    "ds_read_b128 a[116:119], v181 offset:16384\n"
    "ds_read_b64_tr_b16 a[160:161], v238 offset:36864\n"
    "v_mfma_f32_32x32x16_bf16 v[16:31], a[132:135], a[108:111], v[16:31]\n"
    "ds_read_b64_tr_b16 a[162:163], v238 offset:37376\n"
    "ds_read_b64_tr_b16 a[164:165], v239 offset:36864\n"
    "ds_read_b64_tr_b16 a[166:167], v239 offset:37376\n"
    "buffer_load_dwordx4 v245, s[48:51], s15 offen offset:3072 lds\n"
    "v_mfma_f32_32x32x16_bf16 v[32:47], a[136:139], a[108:111], v[32:47]\n"
    "ds_read_b64_tr_b16 a[168:169], v238 offset:38912\n"
    "ds_read_b64_tr_b16 a[170:171], v238 offset:39424\n"
    "v_mfma_f32_32x32x16_bf16 v[48:63], a[140:143], a[108:111], v[48:63]\n"
    "ds_read_b64_tr_b16 a[172:173], v239 offset:38912\n"
    "ds_read_b64_tr_b16 a[174:175], v239 offset:39424\n"
    "ds_read_b128 a[184:187], v188 offset:928\n"
    "s_add_u32 m0, s75, 32768\n"
    "s_nop 0\n"
    "buffer_load_dwordx4 v242, s[40:43], s87 offen offset:0 lds\n"
    "v_mfma_f32_16x16x32_bf16 v[156:159], a[176:179], a[108:111], v[156:159]\n"
    "s_waitcnt lgkmcnt(10)\n"
    "v_mfma_f32_32x32x16_bf16 v[0:15], a[144:147], a[112:115], v[0:15]\n"
    "ds_read_b128 a[120:123], v182 offset:16384\n"
    "ds_read_b64_tr_b16 a[128:129], v238 offset:40960\n"
    "buffer_load_dwordx4 v243, s[40:43], s87 offen offset:1024 lds\n"
    "v_mfma_f32_32x32x16_bf16 v[16:31], a[148:151], a[112:115], v[16:31]\n"
    "ds_read_b64_tr_b16 a[130:131], v238 offset:41472\n"
    "ds_read_b64_tr_b16 a[132:133], v239 offset:40960\n"
    "ds_read_b64_tr_b16 a[134:135], v239 offset:41472\n"
    "v_mfma_f32_32x32x16_bf16 v[32:47], a[152:155], a[112:115], v[32:47]\n"
    "ds_read_b64_tr_b16 a[136:137], v238 offset:43008\n"
    "ds_read_b64_tr_b16 a[138:139], v238 offset:43520\n"
    "s_add_u32 m0, s75, 34816\n"
    "s_nop 0\n"
    "buffer_load_dwordx4 v242, s[44:47], s87 offen offset:0 lds\n"
    "v_mfma_f32_32x32x16_bf16 v[48:63], a[156:159], a[112:115], v[48:63]\n"
    "ds_read_b64_tr_b16 a[140:141], v239 offset:43008\n"
    "ds_read_b64_tr_b16 a[142:143], v239 offset:43520\n"
    "ds_read_b128 a[176:179], v188 offset:960\n"
    "v_mfma_f32_16x16x32_bf16 v[156:159], a[180:183], a[112:115], v[156:159]\n"
    "s_waitcnt lgkmcnt(10)\n"
    "v_mfma_f32_32x32x16_bf16 v[0:15], a[160:163], a[116:119], v[0:15]\n"
    "ds_read_b128 a[124:127], v183 offset:16384\n"
    "ds_read_b64_tr_b16 a[144:145], v238 offset:45056\n"
    "buffer_load_dwordx4 v243, s[44:47], s87 offen offset:1024 lds\n"
    "v_mfma_f32_32x32x16_bf16 v[16:31], a[164:167], a[116:119], v[16:31]\n"
    "ds_read_b64_tr_b16 a[146:147], v238 offset:45568\n"
    "ds_read_b64_tr_b16 a[148:149], v239 offset:45056\n"
    "ds_read_b64_tr_b16 a[150:151], v239 offset:45568\n"
    "v_mfma_f32_32x32x16_bf16 v[32:47], a[168:171], a[116:119], v[32:47]\n"
    "ds_read_b64_tr_b16 a[152:153], v238 offset:47104\n"
    "ds_read_b64_tr_b16 a[154:155], v238 offset:47616\n"
    "v_mfma_f32_32x32x16_bf16 v[48:63], a[172:175], a[116:119], v[48:63]\n"
    "ds_read_b64_tr_b16 a[156:157], v239 offset:47104\n"
    "ds_read_b64_tr_b16 a[158:159], v239 offset:47616\n"
    "ds_read_b128 a[180:183], v188 offset:992\n"
    "v_mfma_f32_16x16x32_bf16 v[156:159], a[184:187], a[116:119], v[156:159]\n"
    "s_waitcnt vmcnt(16) lgkmcnt(10)\n"
    "s_barrier\n"
    "v_mfma_f32_32x32x16_bf16 v[0:15], a[128:131], a[120:123], v[0:15]\n"
    "ds_read_b64_tr_b16 a[160:161], v238 offset:49152\n"
    "ds_read_b64_tr_b16 a[162:163], v238 offset:49664\n"
    "s_add_u32 m0, s75, 49152\n"
    "s_add_u32 s13, s87, 229376\n"
    "buffer_load_dwordx4 v242, s[40:43], s13 offen offset:0 lds\n"
    "v_mfma_f32_32x32x16_bf16 v[16:31], a[132:135], a[120:123], v[16:31]\n"
    "ds_read_b64_tr_b16 a[164:165], v239 offset:49152\n"
    "ds_read_b64_tr_b16 a[166:167], v239 offset:49664\n"
    "buffer_load_dwordx4 v243, s[40:43], s13 offen offset:1024 lds\n"
    "v_mfma_f32_32x32x16_bf16 v[32:47], a[136:139], a[120:123], v[32:47]\n"
    "ds_read_b64_tr_b16 a[168:169], v238 offset:51200\n"
    "ds_read_b64_tr_b16 a[170:171], v238 offset:51712\n"
    "v_mfma_f32_32x32x16_bf16 v[48:63], a[140:143], a[120:123], v[48:63]\n"
    "ds_read_b64_tr_b16 a[172:173], v239 offset:51200\n"
    "ds_read_b64_tr_b16 a[174:175], v239 offset:51712\n"
    "s_add_u32 m0, s75, 51200\n"
    "s_nop 0\n"
    "buffer_load_dwordx4 v242, s[44:47], s13 offen offset:0 lds\n"
    "v_mfma_f32_16x16x32_bf16 v[156:159], a[176:179], a[120:123], v[156:159]\n"
    "s_waitcnt lgkmcnt(8)\n"
    "v_mfma_f32_32x32x16_bf16 v[0:15], a[144:147], a[124:127], v[0:15]\n"
    "ds_read_b64_tr_b16 a[128:129], v238 offset:53248\n"
    "ds_read_b64_tr_b16 a[130:131], v238 offset:53760\n"
    "v_mfma_f32_32x32x16_bf16 v[16:31], a[148:151], a[124:127], v[16:31]\n"
    "ds_read_b64_tr_b16 a[132:133], v239 offset:53248\n"
    "ds_read_b64_tr_b16 a[134:135], v239 offset:53760\n"
    "buffer_load_dwordx4 v243, s[44:47], s13 offen offset:1024 lds\n"
    "v_mfma_f32_32x32x16_bf16 v[32:47], a[152:155], a[124:127], v[32:47]\n"
    "ds_read_b64_tr_b16 a[136:137], v238 offset:55296\n"
    "ds_read_b64_tr_b16 a[138:139], v238 offset:55808\n"
    "v_mfma_f32_32x32x16_bf16 v[48:63], a[156:159], a[124:127], v[48:63]\n"
    "ds_read_b64_tr_b16 a[140:141], v239 offset:55296\n"
    "ds_read_b64_tr_b16 a[142:143], v239 offset:55808\n"
    "s_add_u32 m0, s75, 65536\n"
    "s_add_u32 s14, s87, 458752\n"
    "buffer_load_dwordx4 v242, s[40:43], s14 offen offset:0 lds\n"
    "v_mfma_f32_16x16x32_bf16 v[156:159], a[180:183], a[124:127], v[156:159]\n"
    "s_waitcnt lgkmcnt(8)\n"
    "v_mfma_f32_32x32x16_bf16 v[64:79], a[160:163], a[0:3], v[204:219]\n"
    "ds_read_b64_tr_b16 a[144:145], v238 offset:57344\n"
    "ds_read_b64_tr_b16 a[146:147], v238 offset:57856\n"
    "v_mfma_f32_32x32x16_bf16 v[80:95], a[164:167], a[0:3], v[204:219]\n"
    "ds_read_b64_tr_b16 a[148:149], v239 offset:57344\n"
    "ds_read_b64_tr_b16 a[150:151], v239 offset:57856\n"
    "buffer_load_dwordx4 v243, s[40:43], s14 offen offset:1024 lds\n"
    "v_mfma_f32_32x32x16_bf16 v[64:79], a[168:171], a[4:7], v[64:79]\n"
    "ds_read_b64_tr_b16 a[152:153], v238 offset:59392\n"
    "ds_read_b64_tr_b16 a[154:155], v238 offset:59904\n"
    "v_mfma_f32_32x32x16_bf16 v[80:95], a[172:175], a[4:7], v[80:95]\n"
    "ds_read_b64_tr_b16 a[156:157], v239 offset:59392\n"
    "ds_read_b64_tr_b16 a[158:159], v239 offset:59904\n"
    "s_add_u32 m0, s75, 67584\n"
    "s_nop 0\n"
    "buffer_load_dwordx4 v242, s[44:47], s14 offen offset:0 lds\n"
    "s_waitcnt lgkmcnt(8)\n"
    "v_mfma_f32_32x32x16_bf16 v[64:79], a[128:131], a[8:11], v[64:79]\n"
    "ds_read_b64_tr_b16 a[160:161], v238 offset:61440\n"
    "ds_read_b64_tr_b16 a[162:163], v238 offset:61952\n"
    "v_mul_f32 v249, 0xbfb8aa3b, v156\n"
    "v_exp_f32 v249, v249\n"
    "s_nop 0\n"
    "v_add_f32 v249, 1.0, v249\n"
    "v_rcp_f32 v249, v249\n"
    "v_mfma_f32_32x32x16_bf16 v[80:95], a[132:135], a[8:11], v[80:95]\n"
    "ds_read_b64_tr_b16 a[164:165], v239 offset:61440\n"
    "ds_read_b64_tr_b16 a[166:167], v239 offset:61952\n"
    "buffer_load_dwordx4 v243, s[44:47], s14 offen offset:1024 lds\n"
    "s_nop 0\n"
    "v_sub_f32 v250, 1.0, v249\n"
    "s_add_u32 s16, s89, 0\n"
    "v_mfma_f32_32x32x16_bf16 v[64:79], a[136:139], a[12:15], v[64:79]\n"
    "ds_read_b64_tr_b16 a[168:169], v238 offset:63488\n"
    "ds_read_b64_tr_b16 a[170:171], v238 offset:64000\n"
    "v_pk_mul_f32 v[0:1], v[0:1], v[32:33]\n"
    "v_pk_mul_f32 v[2:3], v[2:3], v[34:35]\n"
    "v_pk_mul_f32 v[4:5], v[4:5], v[36:37]\n"
    "v_pk_mul_f32 v[6:7], v[6:7], v[38:39]\n"
    "v_pk_mul_f32 v[0:1], v[0:1], v[250:251] op_sel_hi:[1,0]\n"
    "v_pk_mul_f32 v[2:3], v[2:3], v[250:251] op_sel_hi:[1,0]\n"
    "v_mfma_f32_32x32x16_bf16 v[80:95], a[140:143], a[12:15], v[80:95]\n"
    "ds_read_b64_tr_b16 a[172:173], v239 offset:63488\n"
    "ds_read_b64_tr_b16 a[174:175], v239 offset:64000\n"
    "v_pk_mul_f32 v[4:5], v[4:5], v[250:251] op_sel_hi:[1,0]\n"
    "v_pk_mul_f32 v[6:7], v[6:7], v[250:251] op_sel_hi:[1,0]\n"
    "v_cvt_pk_bf16_f32 v96, v0, v1\n"
    "v_cvt_pk_bf16_f32 v97, v2, v3\n"
    "v_cvt_pk_bf16_f32 v98, v4, v5\n"
    "v_cvt_pk_bf16_f32 v99, v6, v7\n"
    "s_waitcnt vmcnt(20) lgkmcnt(8)\n"
    "s_barrier\n"
    "v_mfma_f32_32x32x16_bf16 v[64:79], a[144:147], a[16:19], v[64:79]\n"
    "ds_read_b64_tr_b16 a[128:129], v240 offset:0\n"
    "ds_read_b64_tr_b16 a[130:131], v240 offset:512\n"
    "buffer_store_dwordx4 v[96:99], v248, s[60:63], s16 offen offset:512 sc1\n"
    "v_add3_u32 v153, v96, v97, s72\n"
    "v_add3_u32 v153, v153, v98, v99\n"
    "v_pk_mul_f32 v[8:9], v[8:9], v[40:41]\n"
    "v_mfma_f32_32x32x16_bf16 v[80:95], a[148:151], a[16:19], v[80:95]\n"
    "ds_read_b64_tr_b16 a[132:133], v241 offset:0\n"
    "ds_read_b64_tr_b16 a[134:135], v241 offset:512\n"
    "v_pk_mul_f32 v[10:11], v[10:11], v[42:43]\n"
    "v_pk_mul_f32 v[12:13], v[12:13], v[44:45]\n"
    "v_pk_mul_f32 v[14:15], v[14:15], v[46:47]\n"
    "v_pk_mul_f32 v[8:9], v[8:9], v[250:251] op_sel_hi:[1,0]\n"
    "v_pk_mul_f32 v[10:11], v[10:11], v[250:251] op_sel_hi:[1,0]\n"
    "v_pk_mul_f32 v[12:13], v[12:13], v[250:251] op_sel_hi:[1,0]\n"
    "v_mfma_f32_32x32x16_bf16 v[64:79], a[152:155], a[20:23], v[64:79]\n"
    "ds_read_b64_tr_b16 a[136:137], v240 offset:2048\n"
    "ds_read_b64_tr_b16 a[138:139], v240 offset:2560\n"
    "s_add_u32 m0, s75, 81920\n"
    "s_add_u32 s15, s87, 688128\n"
    "buffer_load_dwordx4 v242, s[40:43], s15 offen offset:0 lds\n"
    "v_pk_mul_f32 v[14:15], v[14:15], v[250:251] op_sel_hi:[1,0]\n"
    "v_mfma_f32_32x32x16_bf16 v[80:95], a[156:159], a[20:23], v[80:95]\n"
    "ds_read_b64_tr_b16 a[140:141], v241 offset:2048\n"
    "ds_read_b64_tr_b16 a[142:143], v241 offset:2560\n"
    "v_cvt_pk_bf16_f32 v100, v8, v9\n"
    "v_cvt_pk_bf16_f32 v101, v10, v11\n"
    "v_cvt_pk_bf16_f32 v102, v12, v13\n"
    "v_cvt_pk_bf16_f32 v103, v14, v15\n"
    "buffer_store_dwordx4 v[100:103], v248, s[60:63], s16 offen offset:1536 sc1\n"
    "s_waitcnt lgkmcnt(8)\n"
    "v_mfma_f32_32x32x16_bf16 v[64:79], a[160:163], a[24:27], v[64:79]\n"
    "ds_read_b64_tr_b16 a[144:145], v240 offset:4096\n"
    "ds_read_b64_tr_b16 a[146:147], v240 offset:4608\n"
    "v_add3_u32 v153, v153, v100, v101\n"
    "v_add3_u32 v153, v153, v102, v103\n"
    "v_pk_mul_f32 v[16:17], v[16:17], v[48:49]\n"
    "v_pk_mul_f32 v[18:19], v[18:19], v[50:51]\n"
    "v_pk_mul_f32 v[20:21], v[20:21], v[52:53]\n"
    "v_pk_mul_f32 v[22:23], v[22:23], v[54:55]\n"
    "v_mfma_f32_32x32x16_bf16 v[80:95], a[164:167], a[24:27], v[80:95]\n"
    "ds_read_b64_tr_b16 a[148:149], v241 offset:4096\n"
    "ds_read_b64_tr_b16 a[150:151], v241 offset:4608\n"
    "v_pk_mul_f32 v[16:17], v[16:17], v[250:251] op_sel_hi:[1,0]\n"
    "v_pk_mul_f32 v[18:19], v[18:19], v[250:251] op_sel_hi:[1,0]\n"
    "v_pk_mul_f32 v[20:21], v[20:21], v[250:251] op_sel_hi:[1,0]\n"
    "v_pk_mul_f32 v[22:23], v[22:23], v[250:251] op_sel_hi:[1,0]\n"
    "v_cvt_pk_bf16_f32 v104, v16, v17\n"
    "v_cvt_pk_bf16_f32 v105, v18, v19\n"
    "v_mfma_f32_32x32x16_bf16 v[64:79], a[168:171], a[28:31], v[64:79]\n"
    "ds_read_b64_tr_b16 a[152:153], v240 offset:6144\n"
    "ds_read_b64_tr_b16 a[154:155], v240 offset:6656\n"
    "buffer_load_dwordx4 v243, s[40:43], s15 offen offset:1024 lds\n"
    "v_cvt_pk_bf16_f32 v106, v20, v21\n"
    "v_cvt_pk_bf16_f32 v107, v22, v23\n"
    "buffer_store_dwordx4 v[104:107], v248, s[60:63], s16 offen offset:2560 sc1\n"
    "v_mfma_f32_32x32x16_bf16 v[80:95], a[172:175], a[28:31], v[80:95]\n"
    "ds_read_b64_tr_b16 a[156:157], v241 offset:6144\n"
    "ds_read_b64_tr_b16 a[158:159], v241 offset:6656\n"
    "v_add3_u32 v153, v153, v104, v105\n"
    "v_add3_u32 v153, v153, v106, v107\n"
    "v_pk_mul_f32 v[24:25], v[24:25], v[56:57]\n"
    "v_pk_mul_f32 v[26:27], v[26:27], v[58:59]\n"
    "v_pk_mul_f32 v[28:29], v[28:29], v[60:61]\n"
    "v_pk_mul_f32 v[30:31], v[30:31], v[62:63]\n"
    "s_waitcnt lgkmcnt(8)\n"
    "v_mfma_f32_32x32x16_bf16 v[64:79], a[128:131], a[32:35], v[64:79]\n"
    "ds_read_b64_tr_b16 a[160:161], v240 offset:8192\n"
    "ds_read_b64_tr_b16 a[162:163], v240 offset:8704\n"
    "v_pk_mul_f32 v[24:25], v[24:25], v[250:251] op_sel_hi:[1,0]\n"
    "v_pk_mul_f32 v[26:27], v[26:27], v[250:251] op_sel_hi:[1,0]\n"
    "v_pk_mul_f32 v[28:29], v[28:29], v[250:251] op_sel_hi:[1,0]\n"
    "v_pk_mul_f32 v[30:31], v[30:31], v[250:251] op_sel_hi:[1,0]\n"
    "v_cvt_pk_bf16_f32 v108, v24, v25\n"
    "v_cvt_pk_bf16_f32 v109, v26, v27\n"
    "v_mfma_f32_32x32x16_bf16 v[80:95], a[132:135], a[32:35], v[80:95]\n"
    "ds_read_b64_tr_b16 a[164:165], v241 offset:8192\n"
    "ds_read_b64_tr_b16 a[166:167], v241 offset:8704\n"
    "v_cvt_pk_bf16_f32 v110, v28, v29\n"
    "v_cvt_pk_bf16_f32 v111, v30, v31\n"
    "buffer_store_dwordx4 v[108:111], v248, s[60:63], s16 offen offset:3584 sc1\n"
    "v_add3_u32 v153, v153, v108, v109\n"
    "v_mfma_f32_32x32x16_bf16 v[64:79], a[136:139], a[36:39], v[64:79]\n"
    "ds_read_b64_tr_b16 a[168:169], v240 offset:10240\n"
    "ds_read_b64_tr_b16 a[170:171], v240 offset:10752\n"
    "v_add3_u32 v153, v153, v110, v111\n"
    "buffer_store_dword v153, v144, s[60:63], s16 offen offset:0 sc1\n"
    "v_mfma_f32_32x32x16_bf16 v[80:95], a[140:143], a[36:39], v[80:95]\n"
    "ds_read_b64_tr_b16 a[172:173], v241 offset:10240\n"
    "ds_read_b64_tr_b16 a[174:175], v241 offset:10752\n"
    "s_waitcnt lgkmcnt(8)\n"
    "v_mfma_f32_32x32x16_bf16 v[64:79], a[144:147], a[40:43], v[64:79]\n"
    "ds_read_b64_tr_b16 a[128:129], v240 offset:12288\n"
    "ds_read_b64_tr_b16 a[130:131], v240 offset:12800\n"
    "v_mfma_f32_32x32x16_bf16 v[80:95], a[148:151], a[40:43], v[80:95]\n"
    "ds_read_b64_tr_b16 a[132:133], v241 offset:12288\n"
    "ds_read_b64_tr_b16 a[134:135], v241 offset:12800\n"
    "s_add_u32 m0, s75, 83968\n"
    "s_nop 0\n"
    "buffer_load_dwordx4 v242, s[44:47], s15 offen offset:0 lds\n"
    "v_mfma_f32_32x32x16_bf16 v[64:79], a[152:155], a[44:47], v[64:79]\n"
    "ds_read_b64_tr_b16 a[136:137], v240 offset:14336\n"
    "ds_read_b64_tr_b16 a[138:139], v240 offset:14848\n"
    "v_mfma_f32_32x32x16_bf16 v[80:95], a[156:159], a[44:47], v[80:95]\n"
    "ds_read_b64_tr_b16 a[140:141], v241 offset:14336\n"
    "ds_read_b64_tr_b16 a[142:143], v241 offset:14848\n"
    "s_waitcnt vmcnt(24) lgkmcnt(8)\n"
    "s_barrier\n"
    "v_mfma_f32_32x32x16_bf16 v[64:79], a[160:163], a[48:51], v[64:79]\n"
    "ds_read_b64_tr_b16 a[144:145], v236 offset:0\n"
    "ds_read_b64_tr_b16 a[146:147], v236 offset:512\n"
    "buffer_load_dwordx4 v243, s[44:47], s15 offen offset:1024 lds\n"
    "v_mfma_f32_32x32x16_bf16 v[80:95], a[164:167], a[48:51], v[80:95]\n"
    "ds_read_b64_tr_b16 a[148:149], v237 offset:0\n"
    "ds_read_b64_tr_b16 a[150:151], v237 offset:512\n"
    "v_mfma_f32_32x32x16_bf16 v[64:79], a[168:171], a[52:55], v[64:79]\n"
    "ds_read_b64_tr_b16 a[152:153], v236 offset:2048\n"
    "ds_read_b64_tr_b16 a[154:155], v236 offset:2560\n"
    "s_add_u32 m0, s75, 98304\n"
    "s_add_u32 s12, s87, 917504\n"
    "buffer_load_dwordx4 v242, s[40:43], s12 offen offset:0 lds\n"
    "v_mfma_f32_32x32x16_bf16 v[80:95], a[172:175], a[52:55], v[80:95]\n"
    "ds_read_b64_tr_b16 a[156:157], v237 offset:2048\n"
    "ds_read_b64_tr_b16 a[158:159], v237 offset:2560\n"
    "s_waitcnt lgkmcnt(8)\n"
    "v_mfma_f32_32x32x16_bf16 v[64:79], a[128:131], a[56:59], v[64:79]\n"
    "ds_read_b64_tr_b16 a[160:161], v236 offset:4096\n"
    "ds_read_b64_tr_b16 a[162:163], v236 offset:4608\n"
    "buffer_load_dwordx4 v243, s[40:43], s12 offen offset:1024 lds\n"
    "v_mfma_f32_32x32x16_bf16 v[80:95], a[132:135], a[56:59], v[80:95]\n"
    "ds_read_b64_tr_b16 a[164:165], v237 offset:4096\n"
    "ds_read_b64_tr_b16 a[166:167], v237 offset:4608\n"
    "v_mfma_f32_32x32x16_bf16 v[64:79], a[136:139], a[60:63], v[64:79]\n"
    "ds_read_b64_tr_b16 a[168:169], v236 offset:6144\n"
    "ds_read_b64_tr_b16 a[170:171], v236 offset:6656\n"
    "s_add_u32 m0, s75, 100352\n"
    "s_nop 0\n"
    "buffer_load_dwordx4 v242, s[44:47], s12 offen offset:0 lds\n"
    "v_mfma_f32_32x32x16_bf16 v[80:95], a[140:143], a[60:63], v[80:95]\n"
    "ds_read_b64_tr_b16 a[172:173], v237 offset:6144\n"
    "ds_read_b64_tr_b16 a[174:175], v237 offset:6656\n"
    "s_waitcnt lgkmcnt(8)\n"
    "v_mfma_f32_32x32x16_bf16 v[64:79], a[144:147], a[64:67], v[64:79]\n"
    "ds_read_b64_tr_b16 a[128:129], v236 offset:8192\n"
    "ds_read_b64_tr_b16 a[130:131], v236 offset:8704\n"
    "s_xor_b32 s97, s74, 3\n"
    "s_or_b32 s97, s97, s20\n"
    "s_cbranch_scc0 Lep1\n"
    "Lep1r:\n"
    "v_mfma_f32_32x32x16_bf16 v[80:95], a[148:151], a[64:67], v[80:95]\n"
    "ds_read_b64_tr_b16 a[132:133], v237 offset:8192\n"
    "ds_read_b64_tr_b16 a[134:135], v237 offset:8704\n"
    "buffer_load_dwordx4 v243, s[44:47], s12 offen offset:1024 lds\n"
    "v_mfma_f32_32x32x16_bf16 v[64:79], a[152:155], a[68:71], v[64:79]\n"
    "ds_read_b64_tr_b16 a[136:137], v236 offset:10240\n"
    "ds_read_b64_tr_b16 a[138:139], v236 offset:10752\n"
    "v_mfma_f32_32x32x16_bf16 v[80:95], a[156:159], a[68:71], v[80:95]\n"
    "ds_read_b64_tr_b16 a[140:141], v237 offset:10240\n"
    "ds_read_b64_tr_b16 a[142:143], v237 offset:10752\n"
    "s_add_u32 m0, s75, 114688\n"
    "s_add_u32 s13, s87, 1146880\n"
    "buffer_load_dwordx4 v242, s[40:43], s13 offen offset:0 lds\n"
    "s_waitcnt lgkmcnt(8)\n"
    "v_mfma_f32_32x32x16_bf16 v[64:79], a[160:163], a[72:75], v[64:79]\n"
    "ds_read_b64_tr_b16 a[144:145], v236 offset:12288\n"
    "ds_read_b64_tr_b16 a[146:147], v236 offset:12800\n"
    "v_mfma_f32_32x32x16_bf16 v[80:95], a[164:167], a[72:75], v[80:95]\n"
    "ds_read_b64_tr_b16 a[148:149], v237 offset:12288\n"
    "ds_read_b64_tr_b16 a[150:151], v237 offset:12800\n"
    "buffer_load_dwordx4 v243, s[40:43], s13 offen offset:1024 lds\n"
    "v_mfma_f32_32x32x16_bf16 v[64:79], a[168:171], a[76:79], v[64:79]\n"
    "ds_read_b64_tr_b16 a[152:153], v236 offset:14336\n"
    "ds_read_b64_tr_b16 a[154:155], v236 offset:14848\n"
    "v_mfma_f32_32x32x16_bf16 v[80:95], a[172:175], a[76:79], v[80:95]\n"
    "ds_read_b64_tr_b16 a[156:157], v237 offset:14336\n"
    "ds_read_b64_tr_b16 a[158:159], v237 offset:14848\n"
    "s_add_u32 m0, s75, 116736\n"
    "s_nop 0\n"
    "buffer_load_dwordx4 v242, s[44:47], s13 offen offset:0 lds\n"
    "s_waitcnt vmcnt(28) lgkmcnt(8)\n"
    "s_barrier\n"
    "v_mfma_f32_32x32x16_bf16 v[64:79], a[128:131], a[80:83], v[64:79]\n"
    "ds_read_b64_tr_b16 a[160:161], v236 offset:16384\n"
    "ds_read_b64_tr_b16 a[162:163], v236 offset:16896\n"
    "v_mfma_f32_32x32x16_bf16 v[80:95], a[132:135], a[80:83], v[80:95]\n"
    "ds_read_b64_tr_b16 a[164:165], v237 offset:16384\n"
    "ds_read_b64_tr_b16 a[166:167], v237 offset:16896\n"
    "v_mfma_f32_32x32x16_bf16 v[64:79], a[136:139], a[84:87], v[64:79]\n"
    "ds_read_b64_tr_b16 a[168:169], v236 offset:18432\n"
    "ds_read_b64_tr_b16 a[170:171], v236 offset:18944\n"
    "buffer_load_dwordx4 v243, s[44:47], s13 offen offset:1024 lds\n"
    "v_mfma_f32_32x32x16_bf16 v[80:95], a[140:143], a[84:87], v[80:95]\n"
    "ds_read_b64_tr_b16 a[172:173], v237 offset:18432\n"
    "ds_read_b64_tr_b16 a[174:175], v237 offset:18944\n"
    "s_waitcnt lgkmcnt(8)\n"
    "v_mfma_f32_32x32x16_bf16 v[64:79], a[144:147], a[88:91], v[64:79]\n"
    "ds_read_b64_tr_b16 a[128:129], v236 offset:20480\n"
    "ds_read_b64_tr_b16 a[130:131], v236 offset:20992\n"
    "s_add_u32 m0, s75, 131072\n"
    "s_add_u32 s14, s87, 1376256\n"
    "buffer_load_dwordx4 v242, s[40:43], s14 offen offset:0 lds\n"
    "v_mfma_f32_32x32x16_bf16 v[80:95], a[148:151], a[88:91], v[80:95]\n"
    "ds_read_b64_tr_b16 a[132:133], v237 offset:20480\n"
    "ds_read_b64_tr_b16 a[134:135], v237 offset:20992\n"
    "v_mfma_f32_32x32x16_bf16 v[64:79], a[152:155], a[92:95], v[64:79]\n"
    "ds_read_b64_tr_b16 a[136:137], v236 offset:22528\n"
    "ds_read_b64_tr_b16 a[138:139], v236 offset:23040\n"
    "buffer_load_dwordx4 v243, s[40:43], s14 offen offset:1024 lds\n"
    "v_mfma_f32_32x32x16_bf16 v[80:95], a[156:159], a[92:95], v[80:95]\n"
    "ds_read_b64_tr_b16 a[140:141], v237 offset:22528\n"
    "ds_read_b64_tr_b16 a[142:143], v237 offset:23040\n"
    "s_waitcnt lgkmcnt(8)\n"
    "v_mfma_f32_32x32x16_bf16 v[64:79], a[160:163], a[96:99], v[64:79]\n"
    "ds_read_b64_tr_b16 a[144:145], v236 offset:24576\n"
    "ds_read_b64_tr_b16 a[146:147], v236 offset:25088\n"
    "s_add_u32 m0, s75, 133120\n"
    "s_nop 0\n"
    "buffer_load_dwordx4 v242, s[44:47], s14 offen offset:0 lds\n"
    "v_mfma_f32_32x32x16_bf16 v[80:95], a[164:167], a[96:99], v[80:95]\n"
    "ds_read_b64_tr_b16 a[148:149], v237 offset:24576\n"
    "ds_read_b64_tr_b16 a[150:151], v237 offset:25088\n"
    "v_mfma_f32_32x32x16_bf16 v[64:79], a[168:171], a[100:103], v[64:79]\n"
    "ds_read_b64_tr_b16 a[152:153], v236 offset:26624\n"
    "ds_read_b64_tr_b16 a[154:155], v236 offset:27136\n"
    "buffer_load_dwordx4 v243, s[44:47], s14 offen offset:1024 lds\n"
    "v_mfma_f32_32x32x16_bf16 v[80:95], a[172:175], a[100:103], v[80:95]\n"
    "ds_read_b64_tr_b16 a[156:157], v237 offset:26624\n"
    "ds_read_b64_tr_b16 a[158:159], v237 offset:27136\n"
    "s_waitcnt lgkmcnt(8)\n"
    "v_mfma_f32_32x32x16_bf16 v[64:79], a[128:131], a[104:107], v[64:79]\n"
    "ds_read_b64_tr_b16 a[160:161], v236 offset:28672\n"
    "ds_read_b64_tr_b16 a[162:163], v236 offset:29184\n"
    "v_mfma_f32_32x32x16_bf16 v[80:95], a[132:135], a[104:107], v[80:95]\n"
    "ds_read_b64_tr_b16 a[164:165], v237 offset:28672\n"
    "ds_read_b64_tr_b16 a[166:167], v237 offset:29184\n"
    "v_mfma_f32_32x32x16_bf16 v[64:79], a[136:139], a[108:111], v[64:79]\n"
    "ds_read_b64_tr_b16 a[168:169], v236 offset:30720\n"
    "ds_read_b64_tr_b16 a[170:171], v236 offset:31232\n"
    "v_mfma_f32_32x32x16_bf16 v[80:95], a[140:143], a[108:111], v[80:95]\n"
    "ds_read_b64_tr_b16 a[172:173], v237 offset:30720\n"
    "ds_read_b64_tr_b16 a[174:175], v237 offset:31232\n"
    "s_waitcnt vmcnt(29) lgkmcnt(8)\n"
    "s_barrier\n"
    "v_mfma_f32_32x32x16_bf16 v[64:79], a[144:147], a[112:115], v[64:79]\n"
    "ds_read_b64_tr_b16 a[128:129], v236 offset:32768\n"
    "ds_read_b64_tr_b16 a[130:131], v236 offset:33280\n"
    "s_add_u32 m0, s75, 0\n"
    "s_add_u32 s15, s87, 1605632\n"
    "buffer_load_dwordx4 v242, s[40:43], s15 offen offset:0 lds\n"
    "v_mfma_f32_32x32x16_bf16 v[80:95], a[148:151], a[112:115], v[80:95]\n"
    "ds_read_b64_tr_b16 a[132:133], v237 offset:32768\n"
    "ds_read_b64_tr_b16 a[134:135], v237 offset:33280\n"
    "v_mfma_f32_32x32x16_bf16 v[64:79], a[152:155], a[116:119], v[64:79]\n"
    "ds_read_b64_tr_b16 a[136:137], v236 offset:34816\n"
    "ds_read_b64_tr_b16 a[138:139], v236 offset:35328\n"
    "buffer_load_dwordx4 v243, s[40:43], s15 offen offset:1024 lds\n"
    "v_mfma_f32_32x32x16_bf16 v[80:95], a[156:159], a[116:119], v[80:95]\n"
    "ds_read_b64_tr_b16 a[140:141], v237 offset:34816\n"
    "ds_read_b64_tr_b16 a[142:143], v237 offset:35328\n"
    "s_waitcnt lgkmcnt(8)\n"
    "v_mfma_f32_32x32x16_bf16 v[64:79], a[160:163], a[120:123], v[64:79]\n"
    "ds_read_b64_tr_b16 a[144:145], v236 offset:36864\n"
    "ds_read_b64_tr_b16 a[146:147], v236 offset:37376\n"
    "s_add_u32 m0, s75, 2048\n"
    "s_nop 0\n"
    "buffer_load_dwordx4 v242, s[44:47], s15 offen offset:0 lds\n"
    "v_mfma_f32_32x32x16_bf16 v[80:95], a[164:167], a[120:123], v[80:95]\n"
    "ds_read_b64_tr_b16 a[148:149], v237 offset:36864\n"
    "ds_read_b64_tr_b16 a[150:151], v237 offset:37376\n"
    "v_mfma_f32_32x32x16_bf16 v[64:79], a[168:171], a[124:127], v[64:79]\n"
    "ds_read_b64_tr_b16 a[152:153], v236 offset:38912\n"
    "ds_read_b64_tr_b16 a[154:155], v236 offset:39424\n"
    "buffer_load_dwordx4 v243, s[44:47], s15 offen offset:1024 lds\n"
    "v_mfma_f32_32x32x16_bf16 v[80:95], a[172:175], a[124:127], v[80:95]\n"
    "ds_read_b64_tr_b16 a[156:157], v237 offset:38912\n"
    "ds_read_b64_tr_b16 a[158:159], v237 offset:39424\n"
    "s_waitcnt lgkmcnt(8)\n"
    "v_mfma_f32_32x32x16_bf16 v[0:15], a[128:131], a[0:3], 0\n"
    "ds_read_b64_tr_b16 a[160:161], v236 offset:40960\n"
    "ds_read_b64_tr_b16 a[162:163], v236 offset:41472\n"
    "s_waitcnt vmcnt(13)\n"
    "s_xor_b32 s97, s74, 3\n"
    "s_or_b32 s97, s97, s20\n"
    "s_cbranch_scc0 Lep2\n"
    "Lep2r:\n"
    "v_mfma_f32_32x32x16_bf16 v[16:31], a[132:135], a[0:3], 0\n"
    "ds_read_b64_tr_b16 a[164:165], v237 offset:40960\n"
    "ds_read_b64_tr_b16 a[166:167], v237 offset:41472\n"
    "v_mfma_f32_32x32x16_bf16 v[32:47], a[136:139], a[0:3], 0\n"
    "ds_read_b64_tr_b16 a[168:169], v236 offset:43008\n"
    "ds_read_b64_tr_b16 a[170:171], v236 offset:43520\n"
    "v_mfma_f32_32x32x16_bf16 v[48:63], a[140:143], a[0:3], 0\n"
    "ds_read_b64_tr_b16 a[172:173], v237 offset:43008\n"
    "ds_read_b64_tr_b16 a[174:175], v237 offset:43520\n"
    "s_waitcnt lgkmcnt(8)\n"
    "v_mfma_f32_32x32x16_bf16 v[0:15], a[144:147], a[4:7], v[0:15]\n"
    "ds_read_b64_tr_b16 a[128:129], v236 offset:45056\n"
    "ds_read_b64_tr_b16 a[130:131], v236 offset:45568\n"
    "v_pk_fma_f32 v[156:157], v[64:65], s[4:5], v[154:155] op_sel:[0,0,1] op_sel_hi:[1,0,1]\n"
    "v_pk_fma_f32 v[158:159], v[66:67], s[4:5], v[154:155] op_sel:[0,0,1] op_sel_hi:[1,0,1]\n"
    "v_exp_f32_e64 v156, v156 clamp\n"
    "v_mfma_f32_32x32x16_bf16 v[16:31], a[148:151], a[4:7], v[16:31]\n"
    "ds_read_b64_tr_b16 a[132:133], v237 offset:45056\n"
    "ds_read_b64_tr_b16 a[134:135], v237 offset:45568\n"
    "v_exp_f32_e64 v157, v157 clamp\n"
    "v_exp_f32_e64 v158, v158 clamp\n"
    "v_mfma_f32_32x32x16_bf16 v[32:47], a[152:155], a[4:7], v[32:47]\n"
    "ds_read_b64_tr_b16 a[136:137], v236 offset:47104\n"
    "ds_read_b64_tr_b16 a[138:139], v236 offset:47616\n"
    "v_exp_f32_e64 v159, v159 clamp\n"
    "v_max_f32 v64, s96, v64\n"
    "v_max_f32 v65, s96, v65\n"
    "v_mfma_f32_32x32x16_bf16 v[48:63], a[156:159], a[4:7], v[48:63]\n"
    "ds_read_b64_tr_b16 a[140:141], v237 offset:47104\n"
    "ds_read_b64_tr_b16 a[142:143], v237 offset:47616\n"
    "v_max_f32 v66, s96, v66\n"
    "v_max_f32 v67, s96, v67\n"
    "v_pk_add_f32 v[64:65], v[64:65], v[156:157]\n"
    "v_pk_add_f32 v[66:67], v[66:67], v[158:159]\n"
    "s_waitcnt lgkmcnt(8)\n"
    "s_barrier\n"
    "v_mfma_f32_32x32x16_bf16 v[0:15], a[160:163], a[8:11], v[0:15]\n"
    "ds_read_b64_tr_b16 a[144:145], v236 offset:49152\n"
    "ds_read_b64_tr_b16 a[146:147], v236 offset:49664\n"
    "s_add_u32 m0, s75, 16384\n"
    "s_lshl_b32 s14, s24, 17\n"
    "s_add_u32 s12, s25, s14\n"
    "buffer_load_dwordx4 v246, s[52:55], s12 offen offset:0 lds\n"
    "v_mfma_f32_32x32x16_bf16 v[16:31], a[164:167], a[8:11], v[16:31]\n"
    "ds_read_b64_tr_b16 a[148:149], v237 offset:49152\n"
    "ds_read_b64_tr_b16 a[150:151], v237 offset:49664\n"
    "v_pk_fma_f32 v[156:157], v[68:69], s[4:5], v[154:155] op_sel:[0,0,1] op_sel_hi:[1,0,1]\n"
    "v_pk_fma_f32 v[158:159], v[70:71], s[4:5], v[154:155] op_sel:[0,0,1] op_sel_hi:[1,0,1]\n"
    "v_exp_f32_e64 v156, v156 clamp\n"
    "v_mfma_f32_32x32x16_bf16 v[32:47], a[168:171], a[8:11], v[32:47]\n"
    "ds_read_b64_tr_b16 a[152:153], v236 offset:51200\n"
    "ds_read_b64_tr_b16 a[154:155], v236 offset:51712\n"
    "buffer_load_dwordx4 v247, s[52:55], s12 offen offset:1024 lds\n"
    "v_exp_f32_e64 v157, v157 clamp\n"
    "v_mfma_f32_32x32x16_bf16 v[48:63], a[172:175], a[8:11], v[48:63]\n"
    "ds_read_b64_tr_b16 a[156:157], v237 offset:51200\n"
    "ds_read_b64_tr_b16 a[158:159], v237 offset:51712\n"
    "v_exp_f32_e64 v158, v158 clamp\n"
    "v_exp_f32_e64 v159, v159 clamp\n"
    "s_waitcnt lgkmcnt(8)\n"
    "v_mfma_f32_32x32x16_bf16 v[0:15], a[128:131], a[12:15], v[0:15]\n"
    "ds_read_b64_tr_b16 a[160:161], v236 offset:53248\n"
    "ds_read_b64_tr_b16 a[162:163], v236 offset:53760\n"
    "s_xor_b32 s13, s12, 128\n"
    "s_add_u32 m0, s75, 18432\n"
    "s_nop 0\n"
    "buffer_load_dwordx4 v246, s[52:55], s13 offen offset:0 lds\n"
    "v_mfma_f32_32x32x16_bf16 v[16:31], a[132:135], a[12:15], v[16:31]\n"
    "ds_read_b64_tr_b16 a[164:165], v237 offset:53248\n"
    "ds_read_b64_tr_b16 a[166:167], v237 offset:53760\n"
    "v_max_f32 v68, s96, v68\n"
    "v_max_f32 v69, s96, v69\n"
    "v_max_f32 v70, s96, v70\n"
    "v_max_f32 v71, s96, v71\n"
    "v_mfma_f32_32x32x16_bf16 v[32:47], a[136:139], a[12:15], v[32:47]\n"
    "ds_read_b64_tr_b16 a[168:169], v236 offset:55296\n"
    "ds_read_b64_tr_b16 a[170:171], v236 offset:55808\n"
    "buffer_load_dwordx4 v247, s[52:55], s13 offen offset:1024 lds\n"
    "v_pk_add_f32 v[68:69], v[68:69], v[156:157]\n"
    "v_mfma_f32_32x32x16_bf16 v[48:63], a[140:143], a[12:15], v[48:63]\n"
    "ds_read_b64_tr_b16 a[172:173], v237 offset:55296\n"
    "ds_read_b64_tr_b16 a[174:175], v237 offset:55808\n"
    "v_pk_add_f32 v[70:71], v[70:71], v[158:159]\n"
    "v_pk_fma_f32 v[156:157], v[72:73], s[4:5], v[154:155] op_sel:[0,0,1] op_sel_hi:[1,0,1]\n"
    "v_pk_fma_f32 v[158:159], v[74:75], s[4:5], v[154:155] op_sel:[0,0,1] op_sel_hi:[1,0,1]\n"
    "v_exp_f32_e64 v156, v156 clamp\n"
    "s_waitcnt lgkmcnt(8)\n"
    "v_mfma_f32_32x32x16_bf16 v[0:15], a[144:147], a[16:19], v[0:15]\n"
    "ds_read_b64_tr_b16 a[128:129], v236 offset:57344\n"
    "ds_read_b64_tr_b16 a[130:131], v236 offset:57856\n"
    "s_add_u32 s17, s100, 0\n"
    "buffer_load_dwordx4 v[172:175], v248, s[60:63], s17 offen offset:0 sc1\n"
    "buffer_load_dwordx4 v[176:179], v248, s[60:63], s17 offen offset:1024 sc1\n"
    "buffer_load_dwordx4 v[180:183], v248, s[60:63], s17 offen offset:2048 sc1\n"
    "v_mfma_f32_32x32x16_bf16 v[16:31], a[148:151], a[16:19], v[16:31]\n"
    "ds_read_b64_tr_b16 a[132:133], v237 offset:57344\n"
    "ds_read_b64_tr_b16 a[134:135], v237 offset:57856\n"
    "buffer_load_dwordx4 v[184:187], v248, s[60:63], s17 offen offset:3072 sc1\n"
    "s_add_u32 s16, s100, 4096\n"
    "buffer_load_dwordx4 v[188:191], v248, s[60:63], s16 offen offset:0 sc1\n"
    "buffer_load_dwordx4 v[192:195], v248, s[60:63], s16 offen offset:1024 sc1\n"
    "v_mfma_f32_32x32x16_bf16 v[32:47], a[152:155], a[16:19], v[32:47]\n"
    "ds_read_b64_tr_b16 a[136:137], v236 offset:59392\n"
    "ds_read_b64_tr_b16 a[138:139], v236 offset:59904\n"
    "buffer_load_dwordx4 v[196:199], v248, s[60:63], s16 offen offset:2048 sc1\n"
    "buffer_load_dwordx4 v[200:203], v248, s[60:63], s16 offen offset:3072 sc1\n"
    "s_add_u32 s17, s100, 8192\n"
    "buffer_load_dwordx4 v[204:207], v248, s[60:63], s17 offen offset:0 sc1\n"
    "v_mfma_f32_32x32x16_bf16 v[48:63], a[156:159], a[16:19], v[48:63]\n"
    "ds_read_b64_tr_b16 a[140:141], v237 offset:59392\n"
    "ds_read_b64_tr_b16 a[142:143], v237 offset:59904\n"
    "buffer_load_dwordx4 v[208:211], v248, s[60:63], s17 offen offset:1024 sc1\n"
    "buffer_load_dwordx4 v[212:215], v248, s[60:63], s17 offen offset:2048 sc1\n"
    "buffer_load_dwordx4 v[216:219], v248, s[60:63], s17 offen offset:3072 sc1\n"
    "s_waitcnt lgkmcnt(8)\n"
    "v_mfma_f32_32x32x16_bf16 v[0:15], a[160:163], a[20:23], v[0:15]\n"
    "ds_read_b64_tr_b16 a[144:145], v236 offset:61440\n"
    "ds_read_b64_tr_b16 a[146:147], v236 offset:61952\n"
    "s_add_u32 s16, s100, 12288\n"
    "buffer_load_dwordx4 v[220:223], v248, s[60:63], s16 offen offset:0 sc1\n"
    "buffer_load_dwordx4 v[224:227], v248, s[60:63], s16 offen offset:1024 sc1\n"
    "buffer_load_dwordx4 v[228:231], v248, s[60:63], s16 offen offset:2048 sc1\n"
    "v_mfma_f32_32x32x16_bf16 v[16:31], a[164:167], a[20:23], v[16:31]\n"
    "ds_read_b64_tr_b16 a[148:149], v237 offset:61440\n"
    "ds_read_b64_tr_b16 a[150:151], v237 offset:61952\n"
    "buffer_load_dwordx4 v[232:235], v248, s[60:63], s16 offen offset:3072 sc1\n"
    "v_exp_f32_e64 v157, v157 clamp\n"
    "v_exp_f32_e64 v158, v158 clamp\n"
    "v_mfma_f32_32x32x16_bf16 v[32:47], a[168:171], a[20:23], v[32:47]\n"
    "ds_read_b64_tr_b16 a[152:153], v236 offset:63488\n"
    "ds_read_b64_tr_b16 a[154:155], v236 offset:64000\n"
    "v_exp_f32_e64 v159, v159 clamp\n"
    "v_max_f32 v72, s96, v72\n"
    "v_max_f32 v73, s96, v73\n"
    "v_max_f32 v74, s96, v74\n"
    "v_max_f32 v75, s96, v75\n"
    "v_mfma_f32_32x32x16_bf16 v[48:63], a[172:175], a[20:23], v[48:63]\n"
    "ds_read_b64_tr_b16 a[156:157], v237 offset:63488\n"
    "ds_read_b64_tr_b16 a[158:159], v237 offset:64000\n"
    "v_pk_add_f32 v[72:73], v[72:73], v[156:157]\n"
    "v_pk_add_f32 v[74:75], v[74:75], v[158:159]\n"
    "v_pk_fma_f32 v[156:157], v[76:77], s[4:5], v[154:155] op_sel:[0,0,1] op_sel_hi:[1,0,1]\n"
    "v_pk_fma_f32 v[158:159], v[78:79], s[4:5], v[154:155] op_sel:[0,0,1] op_sel_hi:[1,0,1]\n"
    "v_exp_f32_e64 v156, v156 clamp\n"
    "s_waitcnt lgkmcnt(8)\n"
    "s_barrier\n"
    "v_mfma_f32_32x32x16_bf16 v[0:15], a[128:131], a[24:27], v[0:15]\n"
    "ds_read_b64_tr_b16 a[160:161], v238 offset:0\n"
    "ds_read_b64_tr_b16 a[162:163], v238 offset:512\n"
    "v_exp_f32_e64 v157, v157 clamp\n"
    "v_exp_f32_e64 v158, v158 clamp\n"
    "v_exp_f32_e64 v159, v159 clamp\n"
    "v_mfma_f32_32x32x16_bf16 v[16:31], a[132:135], a[24:27], v[16:31]\n"
    "ds_read_b64_tr_b16 a[164:165], v239 offset:0\n"
    "ds_read_b64_tr_b16 a[166:167], v239 offset:512\n"
    "v_max_f32 v76, s96, v76\n"
    "v_max_f32 v77, s96, v77\n"
    "v_max_f32 v78, s96, v78\n"
    "v_max_f32 v79, s96, v79\n"
    "v_pk_add_f32 v[76:77], v[76:77], v[156:157]\n"
    "v_pk_add_f32 v[78:79], v[78:79], v[158:159]\n"
    "v_mfma_f32_32x32x16_bf16 v[32:47], a[136:139], a[24:27], v[32:47]\n"
    "ds_read_b64_tr_b16 a[168:169], v238 offset:2048\n"
    "ds_read_b64_tr_b16 a[170:171], v238 offset:2560\n"
    "v_pk_fma_f32 v[156:157], v[80:81], s[4:5], v[154:155] op_sel:[0,0,1] op_sel_hi:[1,0,1]\n"
    "v_pk_fma_f32 v[158:159], v[82:83], s[4:5], v[154:155] op_sel:[0,0,1] op_sel_hi:[1,0,1]\n"
    "v_exp_f32_e64 v156, v156 clamp\n"
    "v_exp_f32_e64 v157, v157 clamp\n"
    "v_mfma_f32_32x32x16_bf16 v[48:63], a[140:143], a[24:27], v[48:63]\n"
    "ds_read_b64_tr_b16 a[172:173], v239 offset:2048\n"
    "ds_read_b64_tr_b16 a[174:175], v239 offset:2560\n"
    "v_exp_f32_e64 v158, v158 clamp\n"
    "v_exp_f32_e64 v159, v159 clamp\n"
    "v_max_f32 v80, s96, v80\n"
    "v_max_f32 v81, s96, v81\n"
    "s_waitcnt lgkmcnt(8)\n"
    "v_mfma_f32_32x32x16_bf16 v[0:15], a[144:147], a[28:31], v[0:15]\n"
    "ds_read_b64_tr_b16 a[128:129], v238 offset:4096\n"
    "ds_read_b64_tr_b16 a[130:131], v238 offset:4608\n"
    "s_add_u32 m0, s75, 32768\n"
    "s_add_u32 s15, s25, 131072\n"
    "s_lshl_b32 s13, s24, 17\n"
    "s_sub_u32 s15, s15, s13\n"
    "buffer_load_dwordx4 v246, s[52:55], s15 offen offset:0 lds\n"
    "v_mfma_f32_32x32x16_bf16 v[16:31], a[148:151], a[28:31], v[16:31]\n"
    "ds_read_b64_tr_b16 a[132:133], v239 offset:4096\n"
    "ds_read_b64_tr_b16 a[134:135], v239 offset:4608\n"
    "v_max_f32 v82, s96, v82\n"
    "v_max_f32 v83, s96, v83\n"
    "v_pk_add_f32 v[80:81], v[80:81], v[156:157]\n"
    "v_pk_add_f32 v[82:83], v[82:83], v[158:159]\n"
    "v_pk_fma_f32 v[156:157], v[84:85], s[4:5], v[154:155] op_sel:[0,0,1] op_sel_hi:[1,0,1]\n"
    "v_pk_fma_f32 v[158:159], v[86:87], s[4:5], v[154:155] op_sel:[0,0,1] op_sel_hi:[1,0,1]\n"
    "v_mfma_f32_32x32x16_bf16 v[32:47], a[152:155], a[28:31], v[32:47]\n"
    "ds_read_b64_tr_b16 a[136:137], v238 offset:6144\n"
    "ds_read_b64_tr_b16 a[138:139], v238 offset:6656\n"
    "buffer_load_dwordx4 v247, s[52:55], s15 offen offset:1024 lds\n"
    "v_exp_f32_e64 v156, v156 clamp\n"
    "v_exp_f32_e64 v157, v157 clamp\n"
    "v_mfma_f32_32x32x16_bf16 v[48:63], a[156:159], a[28:31], v[48:63]\n"
    "ds_read_b64_tr_b16 a[140:141], v239 offset:6144\n"
    "ds_read_b64_tr_b16 a[142:143], v239 offset:6656\n"
    "v_exp_f32_e64 v158, v158 clamp\n"
    "v_exp_f32_e64 v159, v159 clamp\n"
    "v_max_f32 v84, s96, v84\n"
    "v_max_f32 v85, s96, v85\n"
    "s_waitcnt lgkmcnt(8)\n"
    "v_mfma_f32_32x32x16_bf16 v[0:15], a[160:163], a[32:35], v[0:15]\n"
    "ds_read_b64_tr_b16 a[144:145], v238 offset:8192\n"
    "ds_read_b64_tr_b16 a[146:147], v238 offset:8704\n"
    "v_max_f32 v86, s96, v86\n"
    "v_max_f32 v87, s96, v87\n"
    "v_pk_add_f32 v[84:85], v[84:85], v[156:157]\n"
    "v_pk_add_f32 v[86:87], v[86:87], v[158:159]\n"
    "v_pk_fma_f32 v[156:157], v[88:89], s[4:5], v[154:155] op_sel:[0,0,1] op_sel_hi:[1,0,1]\n"
    "v_pk_fma_f32 v[158:159], v[90:91], s[4:5], v[154:155] op_sel:[0,0,1] op_sel_hi:[1,0,1]\n"
    "v_mfma_f32_32x32x16_bf16 v[16:31], a[164:167], a[32:35], v[16:31]\n"
    "ds_read_b64_tr_b16 a[148:149], v239 offset:8192\n"
    "ds_read_b64_tr_b16 a[150:151], v239 offset:8704\n"
    "s_xor_b32 s12, s15, 128\n"
    "s_add_u32 m0, s75, 34816\n"
    "s_nop 0\n"
    "buffer_load_dwordx4 v246, s[52:55], s12 offen offset:0 lds\n"
    "v_exp_f32_e64 v156, v156 clamp\n"
    "v_mfma_f32_32x32x16_bf16 v[32:47], a[168:171], a[32:35], v[32:47]\n"
    "ds_read_b64_tr_b16 a[152:153], v238 offset:10240\n"
    "ds_read_b64_tr_b16 a[154:155], v238 offset:10752\n"
    "v_exp_f32_e64 v157, v157 clamp\n"
    "v_exp_f32_e64 v158, v158 clamp\n"
    "v_exp_f32_e64 v159, v159 clamp\n"
    "v_mfma_f32_32x32x16_bf16 v[48:63], a[172:175], a[32:35], v[48:63]\n"
    "ds_read_b64_tr_b16 a[156:157], v239 offset:10240\n"
    "ds_read_b64_tr_b16 a[158:159], v239 offset:10752\n"
    "buffer_load_dwordx4 v247, s[52:55], s12 offen offset:1024 lds\n"
    "v_max_f32 v88, s96, v88\n"
    "v_max_f32 v89, s96, v89\n"
    "v_max_f32 v90, s96, v90\n"
    "s_waitcnt lgkmcnt(8)\n"
    "v_mfma_f32_32x32x16_bf16 v[0:15], a[128:131], a[36:39], v[0:15]\n"
    "ds_read_b64_tr_b16 a[160:161], v238 offset:12288\n"
    "ds_read_b64_tr_b16 a[162:163], v238 offset:12800\n"
    "v_max_f32 v91, s96, v91\n"
    "v_pk_add_f32 v[88:89], v[88:89], v[156:157]\n"
    "v_pk_add_f32 v[90:91], v[90:91], v[158:159]\n"
    "v_pk_fma_f32 v[156:157], v[92:93], s[4:5], v[154:155] op_sel:[0,0,1] op_sel_hi:[1,0,1]\n"
    "v_pk_fma_f32 v[158:159], v[94:95], s[4:5], v[154:155] op_sel:[0,0,1] op_sel_hi:[1,0,1]\n"
    "v_exp_f32_e64 v156, v156 clamp\n"
    "v_mfma_f32_32x32x16_bf16 v[16:31], a[132:135], a[36:39], v[16:31]\n"
    "ds_read_b64_tr_b16 a[164:165], v239 offset:12288\n"
    "ds_read_b64_tr_b16 a[166:167], v239 offset:12800\n"
    "s_add_u32 s18, s90, 0\n"
    "buffer_load_dwordx4 v[112:115], v248, s[60:63], s18 offen offset:512 sc1\n"
    "v_exp_f32_e64 v157, v157 clamp\n"
    "v_exp_f32_e64 v158, v158 clamp\n"
    "v_mfma_f32_32x32x16_bf16 v[32:47], a[136:139], a[36:39], v[32:47]\n"
    "ds_read_b64_tr_b16 a[168:169], v238 offset:14336\n"
    "ds_read_b64_tr_b16 a[170:171], v238 offset:14848\n"
    "v_exp_f32_e64 v159, v159 clamp\n"
    "v_max_f32 v92, s96, v92\n"
    "v_max_f32 v93, s96, v93\n"
    "v_max_f32 v94, s96, v94\n"
    "v_max_f32 v95, s96, v95\n"
    "v_mfma_f32_32x32x16_bf16 v[48:63], a[140:143], a[36:39], v[48:63]\n"
    "ds_read_b64_tr_b16 a[172:173], v239 offset:14336\n"
    "ds_read_b64_tr_b16 a[174:175], v239 offset:14848\n"
    "buffer_load_dwordx4 v[116:119], v248, s[60:63], s18 offen offset:1536 sc1\n"
    "v_pk_add_f32 v[92:93], v[92:93], v[156:157]\n"
    "v_pk_add_f32 v[94:95], v[94:95], v[158:159]\n"
    "v_cvt_pk_bf16_f32 v64, v64, v65\n"
    "v_cvt_pk_bf16_f32 v65, v66, v67\n"
    "s_waitcnt lgkmcnt(8)\n"
    "s_barrier\n"
    "v_mfma_f32_32x32x16_bf16 v[0:15], a[144:147], a[40:43], v[0:15]\n"
    "ds_read_b64_tr_b16 a[128:129], v238 offset:16384\n"
    "ds_read_b64_tr_b16 a[130:131], v238 offset:16896\n"
    "v_cvt_pk_bf16_f32 v66, v68, v69\n"
    "v_cvt_pk_bf16_f32 v67, v70, v71\n"
    "v_cvt_pk_bf16_f32 v68, v72, v73\n"
    "v_cvt_pk_bf16_f32 v69, v74, v75\n"
    "v_cvt_pk_bf16_f32 v70, v76, v77\n"
    "v_cvt_pk_bf16_f32 v71, v78, v79\n"
    "v_mfma_f32_32x32x16_bf16 v[16:31], a[148:151], a[40:43], v[16:31]\n"
    "ds_read_b64_tr_b16 a[132:133], v239 offset:16384\n"
    "ds_read_b64_tr_b16 a[134:135], v239 offset:16896\n"
    "v_cvt_pk_bf16_f32 v72, v80, v81\n"
    "v_cvt_pk_bf16_f32 v73, v82, v83\n"
    "v_cvt_pk_bf16_f32 v74, v84, v85\n"
    "v_cvt_pk_bf16_f32 v75, v86, v87\n"
    "v_cvt_pk_bf16_f32 v76, v88, v89\n"
    "v_cvt_pk_bf16_f32 v77, v90, v91\n"
    "v_mfma_f32_32x32x16_bf16 v[32:47], a[152:155], a[40:43], v[32:47]\n"
    "ds_read_b64_tr_b16 a[136:137], v238 offset:18432\n"
    "ds_read_b64_tr_b16 a[138:139], v238 offset:18944\n"
    "s_add_u32 m0, s75, 49152\n"
    "s_add_u32 s14, s26, 131072\n"
    "buffer_load_dwordx4 v246, s[52:55], s14 offen offset:0 lds\n"
    "v_cvt_pk_bf16_f32 v78, v92, v93\n"
    "v_mfma_f32_32x32x16_bf16 v[48:63], a[156:159], a[40:43], v[48:63]\n"
    "ds_read_b64_tr_b16 a[140:141], v239 offset:18432\n"
    "ds_read_b64_tr_b16 a[142:143], v239 offset:18944\n"
    "v_cvt_pk_bf16_f32 v79, v94, v95\n"
    "s_waitcnt lgkmcnt(8)\n"
    "v_mfma_f32_32x32x16_bf16 v[0:15], a[160:163], a[44:47], v[0:15]\n"
    "ds_read_b64_tr_b16 a[144:145], v238 offset:20480\n"
    "ds_read_b64_tr_b16 a[146:147], v238 offset:20992\n"
    "buffer_load_dwordx4 v[120:123], v248, s[60:63], s18 offen offset:2560 sc1\n"
    "buffer_load_dwordx4 v[124:127], v248, s[60:63], s18 offen offset:3584 sc1\n"
    "buffer_load_dword v151, v144, s[60:63], s18 offen offset:0 sc1\n"
    "v_mfma_f32_32x32x16_bf16 v[16:31], a[164:167], a[44:47], v[16:31]\n"
    "ds_read_b64_tr_b16 a[148:149], v239 offset:20480\n"
    "ds_read_b64_tr_b16 a[150:151], v239 offset:20992\n"
    "s_waitcnt vmcnt(25)\n"
    "v_add_u32 v160, v172, v173\n"
    "v_add3_u32 v160, v160, v174, v175\n"
    "s_waitcnt vmcnt(24)\n"
    "v_add3_u32 v160, v160, v176, v177\n"
    "v_add3_u32 v160, v160, v178, v179\n"
    "s_waitcnt vmcnt(23)\n"
    "v_add3_u32 v160, v160, v180, v181\n"
    "v_add3_u32 v160, v160, v182, v183\n"
    "v_mfma_f32_32x32x16_bf16 v[32:47], a[168:171], a[44:47], v[32:47]\n"
    "ds_read_b64_tr_b16 a[152:153], v238 offset:22528\n"
    "ds_read_b64_tr_b16 a[154:155], v238 offset:23040\n"
    "s_waitcnt vmcnt(22)\n"
    "v_add3_u32 v160, v160, v184, v185\n"
    "v_add3_u32 v160, v160, v186, v187\n"
    "s_waitcnt vmcnt(21)\n"
    "v_add3_u32 v160, v160, v188, v189\n"
    "v_add3_u32 v160, v160, v190, v191\n"
    "s_waitcnt vmcnt(20)\n"
    "v_add3_u32 v160, v160, v192, v193\n"
    "v_add3_u32 v160, v160, v194, v195\n"
    "v_mfma_f32_32x32x16_bf16 v[48:63], a[172:175], a[44:47], v[48:63]\n"
    "ds_read_b64_tr_b16 a[156:157], v239 offset:22528\n"
    "ds_read_b64_tr_b16 a[158:159], v239 offset:23040\n"
    "s_waitcnt vmcnt(19)\n"
    "v_add3_u32 v160, v160, v196, v197\n"
    "v_add3_u32 v160, v160, v198, v199\n"
    "s_waitcnt vmcnt(18)\n"
    "v_add3_u32 v160, v160, v200, v201\n"
    "v_add3_u32 v160, v160, v202, v203\n"
    "s_waitcnt vmcnt(17)\n"
    "v_add3_u32 v160, v160, v204, v205\n"
    "v_add3_u32 v160, v160, v206, v207\n"
    "s_waitcnt lgkmcnt(8)\n"
    "v_mfma_f32_32x32x16_bf16 v[0:15], a[128:131], a[48:51], v[0:15]\n"
    "ds_read_b64_tr_b16 a[160:161], v238 offset:24576\n"
    "ds_read_b64_tr_b16 a[162:163], v238 offset:25088\n"
    "s_waitcnt vmcnt(16)\n"
    "v_add3_u32 v160, v160, v208, v209\n"
    "v_add3_u32 v160, v160, v210, v211\n"
    "s_waitcnt vmcnt(15)\n"
    "v_add3_u32 v160, v160, v212, v213\n"
    "v_add3_u32 v160, v160, v214, v215\n"
    "s_waitcnt vmcnt(14)\n"
    "v_add3_u32 v160, v160, v216, v217\n"
    "v_add3_u32 v160, v160, v218, v219\n"
    "v_mfma_f32_32x32x16_bf16 v[16:31], a[132:135], a[48:51], v[16:31]\n"
    "ds_read_b64_tr_b16 a[164:165], v239 offset:24576\n"
    "ds_read_b64_tr_b16 a[166:167], v239 offset:25088\n"
    "buffer_load_dwordx4 v247, s[52:55], s14 offen offset:1024 lds\n"
    "s_waitcnt vmcnt(14)\n"
    "v_add3_u32 v160, v160, v220, v221\n"
    "v_add3_u32 v160, v160, v222, v223\n"
    "s_waitcnt vmcnt(13)\n"
    "v_add3_u32 v160, v160, v224, v225\n"
    "v_mfma_f32_32x32x16_bf16 v[32:47], a[136:139], a[48:51], v[32:47]\n"
    "ds_read_b64_tr_b16 a[168:169], v238 offset:26624\n"
    "ds_read_b64_tr_b16 a[170:171], v238 offset:27136\n"
    "v_add3_u32 v160, v160, v226, v227\n"
    "s_waitcnt vmcnt(12)\n"
    "v_add3_u32 v160, v160, v228, v229\n"
    "v_add3_u32 v160, v160, v230, v231\n"
    "s_waitcnt vmcnt(11)\n"
    "v_add3_u32 v160, v160, v232, v233\n"
    "v_add3_u32 v160, v160, v234, v235\n"
    "v_cmp_ne_u32 vcc, s70, v160\n"
    "s_nop 0\n"
    "s_cbranch_vccnz LreA1\n"
    "LokA1:\n"
    "v_mfma_f32_32x32x16_bf16 v[48:63], a[140:143], a[48:51], v[48:63]\n"
    "ds_read_b64_tr_b16 a[172:173], v239 offset:26624\n"
    "ds_read_b64_tr_b16 a[174:175], v239 offset:27136\n"
    "s_xor_b32 s15, s14, 128\n"
    "s_add_u32 m0, s75, 51200\n"
    "s_nop 0\n"
    "buffer_load_dwordx4 v246, s[52:55], s15 offen offset:0 lds\n"
    "v_mfma_f32_16x16x16_bf16 v[162:165], a[216:217], v[172:173], 0\n"
    "v_mfma_f32_16x16x16_bf16 v[166:169], a[216:217], v[176:177], 0\n"
    "v_mfma_f32_16x16x16_bf16 v[162:165], a[216:217], v[180:181], v[162:165]\n"
    "v_add_f32 v174, v174, v182\n"
    "v_mfma_f32_16x16x16_bf16 v[166:169], a[216:217], v[184:185], v[166:169]\n"
    "buffer_load_dwordx4 v247, s[52:55], s15 offen offset:1024 lds\n"
    "v_add_f32 v178, v178, v186\n"
    "s_waitcnt lgkmcnt(8)\n"
    "v_mfma_f32_32x32x16_bf16 v[0:15], a[144:147], a[52:55], v[0:15]\n"
    "ds_read_b64_tr_b16 a[128:129], v238 offset:28672\n"
    "ds_read_b64_tr_b16 a[130:131], v238 offset:29184\n"
    "v_mfma_f32_16x16x16_bf16 v[162:165], a[216:217], v[188:189], v[162:165]\n"
    "v_add_f32 v174, v174, v190\n"
    "v_mfma_f32_16x16x16_bf16 v[166:169], a[216:217], v[192:193], v[166:169]\n"
    "v_add_f32 v178, v178, v194\n"
    "v_mfma_f32_16x16x16_bf16 v[162:165], a[216:217], v[196:197], v[162:165]\n"
    "v_add_f32 v174, v174, v198\n"
    "v_mfma_f32_16x16x16_bf16 v[166:169], a[216:217], v[200:201], v[166:169]\n"
    "v_add_f32 v178, v178, v202\n"
    "v_mfma_f32_16x16x16_bf16 v[162:165], a[216:217], v[204:205], v[162:165]\n"
    "v_add_f32 v174, v174, v206\n"
    "v_mfma_f32_16x16x16_bf16 v[166:169], a[216:217], v[208:209], v[166:169]\n"
    "v_add_f32 v178, v178, v210\n"
    "v_mfma_f32_16x16x16_bf16 v[162:165], a[216:217], v[212:213], v[162:165]\n"
    "v_add_f32 v174, v174, v214\n"
    "v_mfma_f32_16x16x16_bf16 v[166:169], a[216:217], v[216:217], v[166:169]\n"
    "v_add_f32 v178, v178, v218\n"
    "v_mfma_f32_16x16x16_bf16 v[162:165], a[216:217], v[220:221], v[162:165]\n"
    "v_add_f32 v174, v174, v222\n"
    "v_mfma_f32_16x16x16_bf16 v[166:169], a[216:217], v[224:225], v[166:169]\n"
    "v_add_f32 v178, v178, v226\n"
    "v_mfma_f32_16x16x16_bf16 v[162:165], a[216:217], v[228:229], v[162:165]\n"
    "v_add_f32 v174, v174, v230\n"
    "v_mfma_f32_16x16x16_bf16 v[166:169], a[216:217], v[232:233], v[166:169]\n"
    "v_add_f32 v178, v178, v234\n"
    "v_add_f32 v174, v174, v178\n"
    "v_cvt_pk_bf16_f32 v174, v174, v174\n"
    "ds_write_b16 v251, v174\n"
    "v_mfma_f32_32x32x16_bf16 v[16:31], a[148:151], a[52:55], v[16:31]\n"
    "ds_read_b64_tr_b16 a[132:133], v239 offset:28672\n"
    "ds_read_b64_tr_b16 a[134:135], v239 offset:29184\n"
    "s_nop 4\n"
    "v_pk_add_f32 v[162:163], v[162:163], v[166:167]\n"
    "v_pk_add_f32 v[164:165], v[164:165], v[168:169]\n"
    "v_cvt_pk_bf16_f32 v176, v162, v163\n"
    "v_cvt_pk_bf16_f32 v177, v164, v165\n"
    "ds_write_b64 v254, v[176:177]\n"
    "v_mfma_f32_32x32x16_bf16 v[32:47], a[152:155], a[52:55], v[32:47]\n"
    "ds_read_b64_tr_b16 a[136:137], v238 offset:30720\n"
    "ds_read_b64_tr_b16 a[138:139], v238 offset:31232\n"
    "v_mfma_f32_32x32x16_bf16 v[48:63], a[156:159], a[52:55], v[48:63]\n"
    "ds_read_b64_tr_b16 a[140:141], v239 offset:30720\n"
    "ds_read_b64_tr_b16 a[142:143], v239 offset:31232\n"
    "s_waitcnt lgkmcnt(4)\n"
    "s_barrier\n"
    "v_mfma_f32_32x32x16_bf16 v[0:15], a[160:163], a[56:59], v[0:15]\n"
    "ds_read_b64_tr_b16 a[144:145], v238 offset:32768\n"
    "ds_read_b64_tr_b16 a[146:147], v238 offset:33280\n"
    "s_add_u32 m0, s75, 65536\n"
    "s_add_u32 s13, s25, 65536\n"
    "s_lshl_b32 s15, s24, 17\n"
    "s_add_u32 s13, s13, s15\n"
    "buffer_load_dwordx4 v246, s[52:55], s13 offen offset:0 lds\n"
    "v_mfma_f32_32x32x16_bf16 v[16:31], a[164:167], a[56:59], v[16:31]\n"
    "ds_read_b64_tr_b16 a[148:149], v239 offset:32768\n"
    "ds_read_b64_tr_b16 a[150:151], v239 offset:33280\n"
    "ds_read_b128 a[192:195], v252 offset:0\n"
    "ds_read_b128 a[196:199], v253 offset:0\n"
    "ds_read_b128 a[200:203], v252 offset:1280\n"
    "ds_read_b128 a[204:207], v253 offset:1280\n"
    "s_add_u32 s77, s89, 13824\n"
    "v_mfma_f32_32x32x16_bf16 v[32:47], a[168:171], a[56:59], v[32:47]\n"
    "ds_read_b64_tr_b16 a[152:153], v238 offset:34816\n"
    "ds_read_b64_tr_b16 a[154:155], v238 offset:35328\n"
    "buffer_load_dwordx4 v247, s[52:55], s13 offen offset:1024 lds\n"
    "v_mfma_f32_32x32x16_bf16 v[48:63], a[172:175], a[56:59], v[48:63]\n"
    "ds_read_b64_tr_b16 a[156:157], v239 offset:34816\n"
    "ds_read_b64_tr_b16 a[158:159], v239 offset:35328\n"
    "s_waitcnt lgkmcnt(12)\n"
    "v_mfma_f32_32x32x16_bf16 v[0:15], a[128:131], a[60:63], v[0:15]\n"
    "ds_read_b64_tr_b16 a[160:161], v238 offset:36864\n"
    "ds_read_b64_tr_b16 a[162:163], v238 offset:37376\n"
    "s_xor_b32 s14, s13, 128\n"
    "s_add_u32 m0, s75, 67584\n"
    "s_nop 0\n"
    "buffer_load_dwordx4 v246, s[52:55], s14 offen offset:0 lds\n"
    "s_waitcnt lgkmcnt(9)\n"
    "v_mfma_f32_32x32x16_bf16 v[172:187], a[192:195], v[64:67], 0\n"
    "s_waitcnt lgkmcnt(8)\n"
    "v_mfma_f32_32x32x16_bf16 v[188:203], a[196:199], v[68:71], 0\n"
    "buffer_load_dwordx4 v247, s[52:55], s14 offen offset:1024 lds\n"
    "s_waitcnt lgkmcnt(7)\n"
    "v_mfma_f32_32x32x16_bf16 v[204:219], a[200:203], v[72:75], 0\n"
    "s_waitcnt lgkmcnt(6)\n"
    "v_mfma_f32_32x32x16_bf16 v[220:235], a[204:207], v[76:79], 0\n"
    "v_mfma_f32_32x32x16_bf16 v[16:31], a[132:135], a[60:63], v[16:31]\n"
    "ds_read_b64_tr_b16 a[164:165], v239 offset:36864\n"
    "ds_read_b64_tr_b16 a[166:167], v239 offset:37376\n"
    "s_nop 1\n"
    "v_pk_add_f32 v[180:181], v[180:181], v[182:183]\n"
    "s_nop 0\n"
    "v_pk_add_f32 v[188:189], v[188:189], v[190:191]\n"
    "s_nop 1\n"
    "v_pk_add_f32 v[212:213], v[212:213], v[214:215]\n"
    "s_nop 0\n"
    "v_pk_add_f32 v[220:221], v[220:221], v[222:223]\n"
    "v_add_f32 v180, v180, v181\n"
    "v_add_f32 v188, v188, v189\n"
    "v_mfma_f32_32x32x16_bf16 v[32:47], a[136:139], a[60:63], v[32:47]\n"
    "ds_read_b64_tr_b16 a[168:169], v238 offset:38912\n"
    "ds_read_b64_tr_b16 a[170:171], v238 offset:39424\n"
    "v_add_f32 v212, v212, v213\n"
    "v_add_f32 v220, v220, v221\n"
    "v_add_f32 v180, s96, v180\n"
    "v_add_f32 v188, s96, v188\n"
    "v_add_f32 v212, s96, v212\n"
    "v_add_f32 v220, s96, v220\n"
    "v_mfma_f32_32x32x16_bf16 v[48:63], a[140:143], a[60:63], v[48:63]\n"
    "ds_read_b64_tr_b16 a[172:173], v239 offset:38912\n"
    "ds_read_b64_tr_b16 a[174:175], v239 offset:39424\n"
    "v_rcp_f32 v180, v180\n"
    "v_rcp_f32 v188, v188\n"
    "v_rcp_f32 v212, v212\n"
    "s_waitcnt lgkmcnt(8)\n"
    "v_mfma_f32_32x32x16_bf16 v[0:15], a[144:147], a[64:67], v[0:15]\n"
    "ds_read_b64_tr_b16 a[128:129], v238 offset:40960\n"
    "ds_read_b64_tr_b16 a[130:131], v238 offset:41472\n"
    "v_rcp_f32 v220, v220\n"
    "v_mul_f32 v180, v180, v249\n"
    "v_mul_f32 v188, v188, v249\n"
    "v_mul_f32 v212, v212, v249\n"
    "v_mul_f32 v220, v220, v249\n"
    "v_mfma_f32_32x32x16_bf16 v[16:31], a[148:151], a[64:67], v[16:31]\n"
    "ds_read_b64_tr_b16 a[132:133], v239 offset:40960\n"
    "ds_read_b64_tr_b16 a[134:135], v239 offset:41472\n"
    "v_pk_mul_f32 v[172:173], v[172:173], v[180:181] op_sel_hi:[1,0]\n"
    "v_pk_mul_f32 v[174:175], v[174:175], v[180:181] op_sel_hi:[1,0]\n"
    "v_pk_mul_f32 v[176:177], v[176:177], v[180:181] op_sel_hi:[1,0]\n"
    "v_pk_mul_f32 v[178:179], v[178:179], v[180:181] op_sel_hi:[1,0]\n"
    "v_cvt_pk_bf16_f32 v64, v172, v173\n"
    "v_cvt_pk_bf16_f32 v65, v174, v175\n"
    "v_mfma_f32_32x32x16_bf16 v[32:47], a[152:155], a[64:67], v[32:47]\n"
    "ds_read_b64_tr_b16 a[136:137], v238 offset:43008\n"
    "ds_read_b64_tr_b16 a[138:139], v238 offset:43520\n"
    "v_cvt_pk_bf16_f32 v66, v176, v177\n"
    "v_cvt_pk_bf16_f32 v67, v178, v179\n"
    "buffer_store_dwordx4 v[64:67], v248, s[60:63], s77 offen offset:512 sc1\n"
    "v_add3_u32 v181, v64, v65, s72\n"
    "v_mfma_f32_32x32x16_bf16 v[48:63], a[156:159], a[64:67], v[48:63]\n"
    "ds_read_b64_tr_b16 a[140:141], v239 offset:43008\n"
    "ds_read_b64_tr_b16 a[142:143], v239 offset:43520\n"
    "v_add3_u32 v181, v181, v66, v67\n"
    "v_pk_mul_f32 v[196:197], v[196:197], v[188:189] op_sel_hi:[1,0]\n"
    "v_pk_mul_f32 v[198:199], v[198:199], v[188:189] op_sel_hi:[1,0]\n"
    "v_pk_mul_f32 v[200:201], v[200:201], v[188:189] op_sel_hi:[1,0]\n"
    "v_pk_mul_f32 v[202:203], v[202:203], v[188:189] op_sel_hi:[1,0]\n"
    "v_cvt_pk_bf16_f32 v68, v196, v197\n"
    "s_waitcnt lgkmcnt(8)\n"
    "v_mfma_f32_32x32x16_bf16 v[0:15], a[160:163], a[68:71], v[0:15]\n"
    "ds_read_b64_tr_b16 a[144:145], v238 offset:45056\n"
    "ds_read_b64_tr_b16 a[146:147], v238 offset:45568\n"
    "v_cvt_pk_bf16_f32 v69, v198, v199\n"
    "v_cvt_pk_bf16_f32 v70, v200, v201\n"
    "v_cvt_pk_bf16_f32 v71, v202, v203\n"
    "buffer_store_dwordx4 v[68:71], v248, s[60:63], s77 offen offset:1536 sc1\n"
    "v_mfma_f32_32x32x16_bf16 v[16:31], a[164:167], a[68:71], v[16:31]\n"
    "ds_read_b64_tr_b16 a[148:149], v239 offset:45056\n"
    "ds_read_b64_tr_b16 a[150:151], v239 offset:45568\n"
    "v_add3_u32 v181, v181, v68, v69\n"
    "v_add3_u32 v181, v181, v70, v71\n"
    "v_pk_mul_f32 v[204:205], v[204:205], v[212:213] op_sel_hi:[1,0]\n"
    "v_pk_mul_f32 v[206:207], v[206:207], v[212:213] op_sel_hi:[1,0]\n"
    "v_pk_mul_f32 v[208:209], v[208:209], v[212:213] op_sel_hi:[1,0]\n"
    "v_pk_mul_f32 v[210:211], v[210:211], v[212:213] op_sel_hi:[1,0]\n"
    "v_mfma_f32_32x32x16_bf16 v[32:47], a[168:171], a[68:71], v[32:47]\n"
    "ds_read_b64_tr_b16 a[152:153], v238 offset:47104\n"
    "ds_read_b64_tr_b16 a[154:155], v238 offset:47616\n"
    "v_cvt_pk_bf16_f32 v72, v204, v205\n"
    "v_cvt_pk_bf16_f32 v73, v206, v207\n"
    "v_cvt_pk_bf16_f32 v74, v208, v209\n"
    "v_cvt_pk_bf16_f32 v75, v210, v211\n"
    "buffer_store_dwordx4 v[72:75], v248, s[60:63], s77 offen offset:2560 sc1\n"
    "v_mfma_f32_32x32x16_bf16 v[48:63], a[172:175], a[68:71], v[48:63]\n"
    "ds_read_b64_tr_b16 a[156:157], v239 offset:47104\n"
    "ds_read_b64_tr_b16 a[158:159], v239 offset:47616\n"
    "v_add3_u32 v181, v181, v72, v73\n"
    "v_add3_u32 v181, v181, v74, v75\n"
    "v_pk_mul_f32 v[228:229], v[228:229], v[220:221] op_sel_hi:[1,0]\n"
    "v_pk_mul_f32 v[230:231], v[230:231], v[220:221] op_sel_hi:[1,0]\n"
    "v_pk_mul_f32 v[232:233], v[232:233], v[220:221] op_sel_hi:[1,0]\n"
    "v_pk_mul_f32 v[234:235], v[234:235], v[220:221] op_sel_hi:[1,0]\n"
    "s_waitcnt lgkmcnt(8)\n"
    "s_barrier\n"
    "v_mfma_f32_32x32x16_bf16 v[0:15], a[128:131], a[72:75], v[0:15]\n"
    "ds_read_b64_tr_b16 a[160:161], v238 offset:49152\n"
    "ds_read_b64_tr_b16 a[162:163], v238 offset:49664\n"
    "s_add_u32 m0, s75, 81920\n"
    "s_lshl_b32 s14, s24, 16\n"
    "s_add_u32 s12, s27, s14\n"
    "buffer_load_dwordx4 v246, s[56:59], s12 offen offset:0 lds\n"
    "v_mfma_f32_32x32x16_bf16 v[16:31], a[132:135], a[72:75], v[16:31]\n"
    "ds_read_b64_tr_b16 a[164:165], v239 offset:49152\n"
    "ds_read_b64_tr_b16 a[166:167], v239 offset:49664\n"
    "v_cvt_pk_bf16_f32 v76, v228, v229\n"
    "v_cvt_pk_bf16_f32 v77, v230, v231\n"
    "v_cvt_pk_bf16_f32 v78, v232, v233\n"
    "v_cvt_pk_bf16_f32 v79, v234, v235\n"
    "buffer_store_dwordx4 v[76:79], v248, s[60:63], s77 offen offset:3584 sc1\n"
    "v_mfma_f32_32x32x16_bf16 v[32:47], a[136:139], a[72:75], v[32:47]\n"
    "ds_read_b64_tr_b16 a[168:169], v238 offset:51200\n"
    "ds_read_b64_tr_b16 a[170:171], v238 offset:51712\n"
    "v_add3_u32 v181, v181, v76, v77\n"
    "v_add3_u32 v181, v181, v78, v79\n"
    "buffer_store_dword v181, v144, s[60:63], s77 offen sc1\n"
    "v_mov_b32 v182, s72\n"
    "v_mfma_f32_32x32x16_bf16 v[48:63], a[140:143], a[72:75], v[48:63]\n"
    "ds_read_b64_tr_b16 a[172:173], v239 offset:51200\n"
    "ds_read_b64_tr_b16 a[174:175], v239 offset:51712\n"
    "buffer_store_dword v182, off, s[60:63], s1 offset:16 sc1\n"
    "s_waitcnt vmcnt(19)\n"
    "v_add3_u32 v154, v112, v113, s72\n"
    "v_add3_u32 v154, v154, v114, v115\n"
    "s_waitcnt vmcnt(18)\n"
    "v_add3_u32 v154, v154, v116, v117\n"
    "s_waitcnt lgkmcnt(8)\n"
    "v_mfma_f32_32x32x16_bf16 v[0:15], a[144:147], a[76:79], v[0:15]\n"
    "ds_read_b64_tr_b16 a[128:129], v238 offset:53248\n"
    "ds_read_b64_tr_b16 a[130:131], v238 offset:53760\n"
    "v_add3_u32 v154, v154, v118, v119\n"
    "s_waitcnt vmcnt(16)\n"
    "v_add3_u32 v154, v154, v120, v121\n"
    "v_add3_u32 v154, v154, v122, v123\n"
    "s_waitcnt vmcnt(15)\n"
    "v_add3_u32 v154, v154, v124, v125\n"
    "v_add3_u32 v154, v154, v126, v127\n"
    "s_waitcnt vmcnt(14)\n"
    "v_cmp_ne_u32 vcc, v154, v151\n"
    "s_nop 0\n"
    "s_cbranch_vccnz LreA2\n"
    "LokA2:\n"
    "v_mfma_f32_32x32x16_bf16 v[16:31], a[148:151], a[76:79], v[16:31]\n"
    "ds_read_b64_tr_b16 a[132:133], v239 offset:53248\n"
    "ds_read_b64_tr_b16 a[134:135], v239 offset:53760\n"
    "v_mfma_f32_32x32x16_bf16 v[32:47], a[152:155], a[76:79], v[32:47]\n"
    "ds_read_b64_tr_b16 a[136:137], v238 offset:55296\n"
    "ds_read_b64_tr_b16 a[138:139], v238 offset:55808\n"
    "buffer_load_dwordx4 v247, s[56:59], s12 offen offset:1024 lds\n"
    "v_mfma_f32_32x32x16_bf16 v[48:63], a[156:159], a[76:79], v[48:63]\n"
    "ds_read_b64_tr_b16 a[140:141], v239 offset:55296\n"
    "ds_read_b64_tr_b16 a[142:143], v239 offset:55808\n"
    "s_waitcnt lgkmcnt(8)\n"
    "v_mfma_f32_32x32x16_bf16 v[0:15], a[160:163], a[80:83], v[0:15]\n"
    "ds_read_b64_tr_b16 a[144:145], v238 offset:57344\n"
    "ds_read_b64_tr_b16 a[146:147], v238 offset:57856\n"
    "s_xor_b32 s13, s12, 128\n"
    "s_add_u32 m0, s75, 83968\n"
    "s_nop 0\n"
    "buffer_load_dwordx4 v246, s[56:59], s13 offen offset:0 lds\n"
    "v_mfma_f32_32x32x16_bf16 v[16:31], a[164:167], a[80:83], v[16:31]\n"
    "ds_read_b64_tr_b16 a[148:149], v239 offset:57344\n"
    "ds_read_b64_tr_b16 a[150:151], v239 offset:57856\n"
    "v_mfma_f32_32x32x16_bf16 v[32:47], a[168:171], a[80:83], v[32:47]\n"
    "ds_read_b64_tr_b16 a[152:153], v238 offset:59392\n"
    "ds_read_b64_tr_b16 a[154:155], v238 offset:59904\n"
    "buffer_load_dwordx4 v247, s[56:59], s13 offen offset:1024 lds\n"
    "v_mfma_f32_32x32x16_bf16 v[48:63], a[172:175], a[80:83], v[48:63]\n"
    "ds_read_b64_tr_b16 a[156:157], v239 offset:59392\n"
    "ds_read_b64_tr_b16 a[158:159], v239 offset:59904\n"
    "s_waitcnt lgkmcnt(8)\n"
    "v_mfma_f32_32x32x16_bf16 v[0:15], a[128:131], a[84:87], v[0:15]\n"
    "ds_read_b64_tr_b16 a[160:161], v238 offset:61440\n"
    "ds_read_b64_tr_b16 a[162:163], v238 offset:61952\n"
    "s_add_u32 s19, s88, 9216\n"
    "buffer_load_dwordx4 v[128:131], v248, s[60:63], s19 offen offset:512 sc1\n"
    "v_mfma_f32_32x32x16_bf16 v[16:31], a[132:135], a[84:87], v[16:31]\n"
    "ds_read_b64_tr_b16 a[164:165], v239 offset:61440\n"
    "ds_read_b64_tr_b16 a[166:167], v239 offset:61952\n"
    "v_mfma_f32_32x32x16_bf16 v[32:47], a[136:139], a[84:87], v[32:47]\n"
    "ds_read_b64_tr_b16 a[168:169], v238 offset:63488\n"
    "ds_read_b64_tr_b16 a[170:171], v238 offset:64000\n"
    "buffer_load_dwordx4 v[132:135], v248, s[60:63], s19 offen offset:1536 sc1\n"
    "v_mfma_f32_32x32x16_bf16 v[48:63], a[140:143], a[84:87], v[48:63]\n"
    "ds_read_b64_tr_b16 a[172:173], v239 offset:63488\n"
    "ds_read_b64_tr_b16 a[174:175], v239 offset:64000\n"
    "buffer_load_dword v151, v144, s[60:63], s19 offen offset:0 sc1\n"
    "s_waitcnt lgkmcnt(8)\n"
    "s_barrier\n"
    "v_mfma_f32_32x32x16_bf16 v[0:15], a[144:147], a[88:91], v[0:15]\n"
    "ds_read_b64_tr_b16 a[128:129], v240 offset:0\n"
    "ds_read_b64_tr_b16 a[130:131], v240 offset:512\n"
    "s_add_u32 s18, s88, 9216\n"
    "buffer_load_dwordx4 v[136:139], v248, s[60:63], s18 offen offset:2560 sc1\n"
    "buffer_load_dwordx4 v[140:143], v248, s[60:63], s18 offen offset:3584 sc1\n"
    "buffer_load_dword v152, v144, s[60:63], s18 offen offset:256 sc1\n"
    "v_mfma_f32_32x32x16_bf16 v[16:31], a[148:151], a[88:91], v[16:31]\n"
    "ds_read_b64_tr_b16 a[132:133], v241 offset:0\n"
    "ds_read_b64_tr_b16 a[134:135], v241 offset:512\n"
    "v_mfma_f32_32x32x16_bf16 v[32:47], a[152:155], a[88:91], v[32:47]\n"
    "ds_read_b64_tr_b16 a[136:137], v240 offset:2048\n"
    "ds_read_b64_tr_b16 a[138:139], v240 offset:2560\n"
    "v_mfma_f32_32x32x16_bf16 v[48:63], a[156:159], a[88:91], v[48:63]\n"
    "ds_read_b64_tr_b16 a[140:141], v241 offset:2048\n"
    "ds_read_b64_tr_b16 a[142:143], v241 offset:2560\n"
    "s_waitcnt lgkmcnt(8)\n"
    "v_mfma_f32_32x32x16_bf16 v[0:15], a[160:163], a[92:95], v[0:15]\n"
    "ds_read_b64_tr_b16 a[144:145], v240 offset:4096\n"
    "ds_read_b64_tr_b16 a[146:147], v240 offset:4608\n"
    "v_mfma_f32_32x32x16_bf16 v[16:31], a[164:167], a[92:95], v[16:31]\n"
    "ds_read_b64_tr_b16 a[148:149], v241 offset:4096\n"
    "ds_read_b64_tr_b16 a[150:151], v241 offset:4608\n"
    "v_mfma_f32_32x32x16_bf16 v[32:47], a[168:171], a[92:95], v[32:47]\n"
    "ds_read_b64_tr_b16 a[152:153], v240 offset:6144\n"
    "ds_read_b64_tr_b16 a[154:155], v240 offset:6656\n"
    "s_add_u32 m0, s75, 98304\n"
    "s_add_u32 s15, s27, 65536\n"
    "s_lshl_b32 s13, s24, 16\n"
    "s_sub_u32 s15, s15, s13\n"
    "buffer_load_dwordx4 v246, s[56:59], s15 offen offset:0 lds\n"
    "v_mfma_f32_32x32x16_bf16 v[48:63], a[172:175], a[92:95], v[48:63]\n"
    "ds_read_b64_tr_b16 a[156:157], v241 offset:6144\n"
    "ds_read_b64_tr_b16 a[158:159], v241 offset:6656\n"
    "s_waitcnt lgkmcnt(8)\n"
    "v_mfma_f32_32x32x16_bf16 v[0:15], a[128:131], a[96:99], v[0:15]\n"
    "ds_read_b64_tr_b16 a[160:161], v240 offset:8192\n"
    "ds_read_b64_tr_b16 a[162:163], v240 offset:8704\n"
    "buffer_load_dwordx4 v247, s[56:59], s15 offen offset:1024 lds\n"
    "v_mfma_f32_32x32x16_bf16 v[16:31], a[132:135], a[96:99], v[16:31]\n"
    "ds_read_b64_tr_b16 a[164:165], v241 offset:8192\n"
    "ds_read_b64_tr_b16 a[166:167], v241 offset:8704\n"
    "v_mfma_f32_32x32x16_bf16 v[32:47], a[136:139], a[96:99], v[32:47]\n"
    "ds_read_b64_tr_b16 a[168:169], v240 offset:10240\n"
    "ds_read_b64_tr_b16 a[170:171], v240 offset:10752\n"
    "v_mfma_f32_32x32x16_bf16 v[48:63], a[140:143], a[96:99], v[48:63]\n"
    "ds_read_b64_tr_b16 a[172:173], v241 offset:10240\n"
    "ds_read_b64_tr_b16 a[174:175], v241 offset:10752\n"
    "s_xor_b32 s12, s15, 128\n"
    "s_add_u32 m0, s75, 100352\n"
    "s_nop 0\n"
    "buffer_load_dwordx4 v246, s[56:59], s12 offen offset:0 lds\n"
    "s_waitcnt lgkmcnt(8)\n"
    "v_mfma_f32_32x32x16_bf16 v[0:15], a[144:147], a[100:103], v[0:15]\n"
    "ds_read_b64_tr_b16 a[128:129], v240 offset:12288\n"
    "ds_read_b64_tr_b16 a[130:131], v240 offset:12800\n"
    "v_mfma_f32_32x32x16_bf16 v[16:31], a[148:151], a[100:103], v[16:31]\n"
    "ds_read_b64_tr_b16 a[132:133], v241 offset:12288\n"
    "ds_read_b64_tr_b16 a[134:135], v241 offset:12800\n"
    "buffer_load_dwordx4 v247, s[56:59], s12 offen offset:1024 lds\n"
    "v_mfma_f32_32x32x16_bf16 v[32:47], a[152:155], a[100:103], v[32:47]\n"
    "ds_read_b64_tr_b16 a[136:137], v240 offset:14336\n"
    "ds_read_b64_tr_b16 a[138:139], v240 offset:14848\n"
    "v_mfma_f32_32x32x16_bf16 v[48:63], a[156:159], a[100:103], v[48:63]\n"
    "ds_read_b64_tr_b16 a[140:141], v241 offset:14336\n"
    "ds_read_b64_tr_b16 a[142:143], v241 offset:14848\n"
    "s_waitcnt lgkmcnt(8)\n"
    "s_barrier\n"
    "v_mfma_f32_32x32x16_bf16 v[0:15], a[160:163], a[104:107], v[0:15]\n"
    "ds_read_b64_tr_b16 a[144:145], v236 offset:0\n"
    "ds_read_b64_tr_b16 a[146:147], v236 offset:512\n"
    "s_add_u32 m0, s75, 114688\n"
    "s_add_u32 s14, s25, 196608\n"
    "s_lshl_b32 s12, s24, 17\n"
    "s_sub_u32 s14, s14, s12\n"
    "buffer_load_dwordx4 v246, s[52:55], s14 offen offset:0 lds\n"
    "v_mfma_f32_32x32x16_bf16 v[16:31], a[164:167], a[104:107], v[16:31]\n"
    "ds_read_b64_tr_b16 a[148:149], v237 offset:0\n"
    "ds_read_b64_tr_b16 a[150:151], v237 offset:512\n"
    "v_mfma_f32_32x32x16_bf16 v[32:47], a[168:171], a[104:107], v[32:47]\n"
    "ds_read_b64_tr_b16 a[152:153], v236 offset:2048\n"
    "ds_read_b64_tr_b16 a[154:155], v236 offset:2560\n"
    "buffer_load_dwordx4 v247, s[52:55], s14 offen offset:1024 lds\n"
    "v_mfma_f32_32x32x16_bf16 v[48:63], a[172:175], a[104:107], v[48:63]\n"
    "ds_read_b64_tr_b16 a[156:157], v237 offset:2048\n"
    "ds_read_b64_tr_b16 a[158:159], v237 offset:2560\n"
    "s_waitcnt lgkmcnt(8)\n"
    "v_mfma_f32_32x32x16_bf16 v[0:15], a[128:131], a[108:111], v[0:15]\n"
    "ds_read_b64_tr_b16 a[160:161], v236 offset:4096\n"
    "ds_read_b64_tr_b16 a[162:163], v236 offset:4608\n"
    "s_xor_b32 s15, s14, 128\n"
    "s_add_u32 m0, s75, 116736\n"
    "s_nop 0\n"
    "buffer_load_dwordx4 v246, s[52:55], s15 offen offset:0 lds\n"
    "v_mfma_f32_32x32x16_bf16 v[16:31], a[132:135], a[108:111], v[16:31]\n"
    "ds_read_b64_tr_b16 a[164:165], v237 offset:4096\n"
    "ds_read_b64_tr_b16 a[166:167], v237 offset:4608\n"
    "v_mfma_f32_32x32x16_bf16 v[32:47], a[136:139], a[108:111], v[32:47]\n"
    "ds_read_b64_tr_b16 a[168:169], v236 offset:6144\n"
    "ds_read_b64_tr_b16 a[170:171], v236 offset:6656\n"
    "buffer_load_dwordx4 v247, s[52:55], s15 offen offset:1024 lds\n"
    "v_mfma_f32_32x32x16_bf16 v[48:63], a[140:143], a[108:111], v[48:63]\n"
    "ds_read_b64_tr_b16 a[172:173], v237 offset:6144\n"
    "ds_read_b64_tr_b16 a[174:175], v237 offset:6656\n"
    "s_waitcnt lgkmcnt(8)\n"
    "v_mfma_f32_32x32x16_bf16 v[0:15], a[144:147], a[112:115], v[0:15]\n"
    "ds_read_b64_tr_b16 a[128:129], v236 offset:8192\n"
    "ds_read_b64_tr_b16 a[130:131], v236 offset:8704\n"
    "v_mfma_f32_32x32x16_bf16 v[16:31], a[148:151], a[112:115], v[16:31]\n"
    "ds_read_b64_tr_b16 a[132:133], v237 offset:8192\n"
    "ds_read_b64_tr_b16 a[134:135], v237 offset:8704\n"
    "v_mfma_f32_32x32x16_bf16 v[32:47], a[152:155], a[112:115], v[32:47]\n"
    "ds_read_b64_tr_b16 a[136:137], v236 offset:10240\n"
    "ds_read_b64_tr_b16 a[138:139], v236 offset:10752\n"
    "v_mfma_f32_32x32x16_bf16 v[48:63], a[156:159], a[112:115], v[48:63]\n"
    "ds_read_b64_tr_b16 a[140:141], v237 offset:10240\n"
    "ds_read_b64_tr_b16 a[142:143], v237 offset:10752\n"
    "s_waitcnt lgkmcnt(8)\n"
    "v_mfma_f32_32x32x16_bf16 v[0:15], a[160:163], a[116:119], v[0:15]\n"
    "ds_read_b64_tr_b16 a[144:145], v236 offset:12288\n"
    "ds_read_b64_tr_b16 a[146:147], v236 offset:12800\n"
    "v_mfma_f32_32x32x16_bf16 v[16:31], a[164:167], a[116:119], v[16:31]\n"
    "ds_read_b64_tr_b16 a[148:149], v237 offset:12288\n"
    "ds_read_b64_tr_b16 a[150:151], v237 offset:12800\n"
    "s_waitcnt vmcnt(13)\n"
    "v_add3_u32 v154, v128, v129, s68\n"
    "v_add3_u32 v154, v154, v130, v131\n"
    "s_waitcnt vmcnt(12)\n"
    "v_add3_u32 v154, v154, v132, v133\n"
    "v_add3_u32 v154, v154, v134, v135\n"
    "s_waitcnt vmcnt(10)\n"
    "v_add3_u32 v154, v154, v136, v137\n"
    "v_add3_u32 v154, v154, v138, v139\n"
    "v_mfma_f32_32x32x16_bf16 v[32:47], a[168:171], a[116:119], v[32:47]\n"
    "ds_read_b64_tr_b16 a[152:153], v236 offset:14336\n"
    "ds_read_b64_tr_b16 a[154:155], v236 offset:14848\n"
    "s_waitcnt vmcnt(9)\n"
    "v_add3_u32 v154, v154, v140, v141\n"
    "v_add3_u32 v154, v154, v142, v143\n"
    "s_waitcnt vmcnt(8)\n"
    "v_add_u32 v151, v151, v152\n"
    "v_cmp_ne_u32 vcc, v154, v151\n"
    "s_nop 0\n"
    "s_cbranch_vccnz LreA3\n"
    "LokA3:\n"
    "v_mfma_f32_32x32x16_bf16 v[48:63], a[172:175], a[116:119], v[48:63]\n"
    "ds_read_b64_tr_b16 a[156:157], v237 offset:14336\n"
    "ds_read_b64_tr_b16 a[158:159], v237 offset:14848\n"
    "s_waitcnt lgkmcnt(8)\n"
    "s_barrier\n"
    "v_mfma_f32_32x32x16_bf16 v[0:15], a[128:131], a[120:123], v[0:15]\n"
    "ds_read_b64_tr_b16 a[160:161], v146 offset:16384\n"
    "ds_read_b64_tr_b16 a[162:163], v146 offset:17408\n"
    "s_add_u32 m0, s75, 131072\n"
    "s_nop 0\n"
    "buffer_load_dwordx4 v246, s[52:55], s26 offen offset:0 lds\n"
    "v_mfma_f32_32x32x16_bf16 v[16:31], a[132:135], a[120:123], v[16:31]\n"
    "ds_read_b64_tr_b16 a[164:165], v145 offset:18432\n"
    "ds_read_b64_tr_b16 a[166:167], v145 offset:19456\n"
    "v_mfma_f32_32x32x16_bf16 v[32:47], a[136:139], a[120:123], v[32:47]\n"
    "ds_read_b64_tr_b16 a[168:169], v146 offset:20480\n"
    "ds_read_b64_tr_b16 a[170:171], v146 offset:21504\n"
    "buffer_load_dwordx4 v247, s[52:55], s26 offen offset:1024 lds\n"
    "v_mfma_f32_32x32x16_bf16 v[48:63], a[140:143], a[120:123], v[48:63]\n"
    "ds_read_b64_tr_b16 a[172:173], v145 offset:22528\n"
    "ds_read_b64_tr_b16 a[174:175], v145 offset:23552\n"
    "s_waitcnt lgkmcnt(8)\n"
    "v_mfma_f32_32x32x16_bf16 v[0:15], a[144:147], a[124:127], v[0:15]\n"
    "ds_read_b64_tr_b16 a[128:129], v146 offset:24576\n"
    "ds_read_b64_tr_b16 a[130:131], v146 offset:25600\n"
    "s_xor_b32 s14, s26, 128\n"
    "s_add_u32 m0, s75, 133120\n"
    "s_nop 0\n"
    "buffer_load_dwordx4 v246, s[52:55], s14 offen offset:0 lds\n"
    "v_and_b32 v245, 0x7c, v144\n"
    "v_mfma_f32_32x32x16_bf16 v[16:31], a[148:151], a[124:127], v[16:31]\n"
    "ds_read_b64_tr_b16 a[132:133], v145 offset:26624\n"
    "ds_read_b64_tr_b16 a[134:135], v145 offset:27648\n"
    "buffer_load_dword v245, v245, s[60:63], s0 offen sc1\n"
    "v_mfma_f32_32x32x16_bf16 v[32:47], a[152:155], a[124:127], v[32:47]\n"
    "ds_read_b64_tr_b16 a[136:137], v146 offset:28672\n"
    "ds_read_b64_tr_b16 a[138:139], v146 offset:29696\n"
    "buffer_load_dwordx4 v247, s[52:55], s14 offen offset:1024 lds\n"
    "v_mfma_f32_32x32x16_bf16 v[48:63], a[156:159], a[124:127], v[48:63]\n"
    "ds_read_b64_tr_b16 a[140:141], v145 offset:30720\n"
    "ds_read_b64_tr_b16 a[142:143], v145 offset:31744\n"
    "s_waitcnt lgkmcnt(8)\n"
    "s_barrier\n"
    "v_mfma_f32_32x32x16_bf16 v[156:171], a[160:163], v[96:99], 0\n"
    "ds_read_b64_tr_b16 a[144:145], v146 offset:32768\n"
    "ds_read_b64_tr_b16 a[146:147], v146 offset:33792\n"
    "s_add_u32 s76, s89, 23040\n"
    "v_pk_mul_f32 v[0:1], v[0:1], v[32:33]\n"
    "v_pk_mul_f32 v[2:3], v[2:3], v[34:35]\n"
    "v_pk_mul_f32 v[4:5], v[4:5], v[36:37]\n"
    "v_pk_mul_f32 v[6:7], v[6:7], v[38:39]\n"
    "v_pk_mul_f32 v[8:9], v[8:9], v[40:41]\n"
    "v_pk_mul_f32 v[10:11], v[10:11], v[42:43]\n"
    "v_pk_mul_f32 v[12:13], v[12:13], v[44:45]\n"
    "v_pk_mul_f32 v[14:15], v[14:15], v[46:47]\n"
    "v_mfma_f32_32x32x16_bf16 v[220:235], a[164:167], v[96:99], 0\n"
    "ds_read_b64_tr_b16 a[148:149], v145 offset:34816\n"
    "ds_read_b64_tr_b16 a[150:151], v145 offset:35840\n"
    "s_add_u32 m0, s75, 0\n"
    "s_add_u32 s12, s26, 65536\n"
    "buffer_load_dwordx4 v246, s[52:55], s12 offen offset:0 lds\n"
    "v_pk_mul_f32 v[16:17], v[16:17], v[48:49]\n"
    "v_pk_mul_f32 v[18:19], v[18:19], v[50:51]\n"
    "v_pk_mul_f32 v[20:21], v[20:21], v[52:53]\n"
    "v_pk_mul_f32 v[22:23], v[22:23], v[54:55]\n"
    "v_mfma_f32_32x32x16_bf16 v[156:171], a[168:171], v[100:103], v[156:171]\n"
    "ds_read_b64_tr_b16 a[152:153], v146 offset:36864\n"
    "ds_read_b64_tr_b16 a[154:155], v146 offset:37888\n"
    "v_pk_mul_f32 v[24:25], v[24:25], v[56:57]\n"
    "v_pk_mul_f32 v[26:27], v[26:27], v[58:59]\n"
    "v_pk_mul_f32 v[28:29], v[28:29], v[60:61]\n"
    "v_pk_mul_f32 v[30:31], v[30:31], v[62:63]\n"
    "v_pk_mul_f32 v[0:1], v[0:1], v[250:251] op_sel_hi:[1,0]\n"
    "v_pk_mul_f32 v[2:3], v[2:3], v[250:251] op_sel_hi:[1,0]\n"
    "v_pk_mul_f32 v[4:5], v[4:5], v[250:251] op_sel_hi:[1,0]\n"
    "v_pk_mul_f32 v[6:7], v[6:7], v[250:251] op_sel_hi:[1,0]\n"
    "v_cvt_pk_bf16_f32 v0, v0, v1\n"
    "v_mfma_f32_32x32x16_bf16 v[220:235], a[172:175], v[100:103], v[220:235]\n"
    "ds_read_b64_tr_b16 a[156:157], v145 offset:38912\n"
    "ds_read_b64_tr_b16 a[158:159], v145 offset:39936\n"
    "buffer_load_dwordx4 v247, s[52:55], s12 offen offset:1024 lds\n"
    "v_cvt_pk_bf16_f32 v1, v2, v3\n"
    "v_cvt_pk_bf16_f32 v2, v4, v5\n"
    "v_cvt_pk_bf16_f32 v3, v6, v7\n"
    "v_mov_b32 v5, s72\n"
    "buffer_store_dword v5, off, s[60:63], s1 offset:24 sc1\n"
    "s_waitcnt lgkmcnt(8)\n"
    "v_mfma_f32_32x32x16_bf16 v[156:171], a[128:131], v[104:107], v[156:171]\n"
    "ds_read_b64_tr_b16 a[160:161], v146 offset:40960\n"
    "ds_read_b64_tr_b16 a[162:163], v146 offset:41984\n"
    "buffer_store_dwordx4 v[0:3], v248, s[60:63], s76 offen offset:512 sc1\n"
    "v_add3_u32 v4, v0, v1, s72\n"
    "v_add3_u32 v4, v4, v2, v3\n"
    "v_pk_mul_f32 v[8:9], v[8:9], v[250:251] op_sel_hi:[1,0]\n"
    "v_pk_mul_f32 v[10:11], v[10:11], v[250:251] op_sel_hi:[1,0]\n"
    "v_pk_mul_f32 v[12:13], v[12:13], v[250:251] op_sel_hi:[1,0]\n"
    "v_pk_mul_f32 v[14:15], v[14:15], v[250:251] op_sel_hi:[1,0]\n"
    "v_mfma_f32_32x32x16_bf16 v[220:235], a[132:135], v[104:107], v[220:235]\n"
    "ds_read_b64_tr_b16 a[164:165], v145 offset:43008\n"
    "ds_read_b64_tr_b16 a[166:167], v145 offset:44032\n"
    "v_cvt_pk_bf16_f32 v8, v8, v9\n"
    "v_cvt_pk_bf16_f32 v9, v10, v11\n"
    "v_cvt_pk_bf16_f32 v10, v12, v13\n"
    "v_cvt_pk_bf16_f32 v11, v14, v15\n"
    "buffer_store_dwordx4 v[8:11], v248, s[60:63], s76 offen offset:1536 sc1\n"
    "v_add3_u32 v4, v4, v8, v9\n"
    "v_add3_u32 v4, v4, v10, v11\n"
    "v_mfma_f32_32x32x16_bf16 v[156:171], a[136:139], v[108:111], v[156:171]\n"
    "ds_read_b64_tr_b16 a[168:169], v146 offset:45056\n"
    "ds_read_b64_tr_b16 a[170:171], v146 offset:46080\n"
    "v_pk_mul_f32 v[16:17], v[16:17], v[250:251] op_sel_hi:[1,0]\n"
    "v_pk_mul_f32 v[18:19], v[18:19], v[250:251] op_sel_hi:[1,0]\n"
    "v_pk_mul_f32 v[20:21], v[20:21], v[250:251] op_sel_hi:[1,0]\n"
    "v_pk_mul_f32 v[22:23], v[22:23], v[250:251] op_sel_hi:[1,0]\n"
    "v_cvt_pk_bf16_f32 v16, v16, v17\n"
    "v_cvt_pk_bf16_f32 v17, v18, v19\n"
    "v_cvt_pk_bf16_f32 v18, v20, v21\n"
    "v_cvt_pk_bf16_f32 v19, v22, v23\n"
    "buffer_store_dwordx4 v[16:19], v248, s[60:63], s76 offen offset:2560 sc1\n"
    "v_mfma_f32_32x32x16_bf16 v[220:235], a[140:143], v[108:111], v[220:235]\n"
    "ds_read_b64_tr_b16 a[172:173], v145 offset:47104\n"
    "ds_read_b64_tr_b16 a[174:175], v145 offset:48128\n"
    "v_add3_u32 v4, v4, v16, v17\n"
    "v_add3_u32 v4, v4, v18, v19\n"
    "v_pk_mul_f32 v[24:25], v[24:25], v[250:251] op_sel_hi:[1,0]\n"
    "v_pk_mul_f32 v[26:27], v[26:27], v[250:251] op_sel_hi:[1,0]\n"
    "v_pk_mul_f32 v[28:29], v[28:29], v[250:251] op_sel_hi:[1,0]\n"
    "v_pk_mul_f32 v[30:31], v[30:31], v[250:251] op_sel_hi:[1,0]\n"
    "v_cvt_pk_bf16_f32 v24, v24, v25\n"
    "v_cvt_pk_bf16_f32 v25, v26, v27\n"
    "v_cvt_pk_bf16_f32 v26, v28, v29\n"
    "s_waitcnt lgkmcnt(8)\n"
    "v_mfma_f32_32x32x16_bf16 v[156:171], a[144:147], v[112:115], v[156:171]\n"
    "ds_read_b64_tr_b16 a[128:129], v146 offset:49152\n"
    "ds_read_b64_tr_b16 a[130:131], v146 offset:50176\n"
    "v_cvt_pk_bf16_f32 v27, v30, v31\n"
    "buffer_store_dwordx4 v[24:27], v248, s[60:63], s76 offen offset:3584 sc1\n"
    "v_add3_u32 v4, v4, v24, v25\n"
    "v_add3_u32 v4, v4, v26, v27\n"
    "buffer_store_dword v4, v144, s[60:63], s76 offen offset:0 sc1\n"
    "v_mfma_f32_32x32x16_bf16 v[220:235], a[148:151], v[112:115], v[220:235]\n"
    "ds_read_b64_tr_b16 a[132:133], v145 offset:51200\n"
    "ds_read_b64_tr_b16 a[134:135], v145 offset:52224\n"
    "v_and_b32 v242, 0xf0, v248\n"
    "v_lshlrev_b32 v242, 6, v242\n"
    "v_and_b32 v243, 0x300, v248\n"
    "v_lshrrev_b32 v243, 4, v243\n"
    "v_or_b32 v242, v242, v243\n"
    "v_mfma_f32_32x32x16_bf16 v[156:171], a[152:155], v[116:119], v[156:171]\n"
    "ds_read_b64_tr_b16 a[136:137], v146 offset:53248\n"
    "ds_read_b64_tr_b16 a[138:139], v146 offset:54272\n"
    "v_mfma_f32_32x32x16_bf16 v[220:235], a[156:159], v[116:119], v[220:235]\n"
    "ds_read_b64_tr_b16 a[140:141], v145 offset:55296\n"
    "ds_read_b64_tr_b16 a[142:143], v145 offset:56320\n"
    "s_waitcnt lgkmcnt(8)\n"
    "v_mfma_f32_32x32x16_bf16 v[156:171], a[160:163], v[120:123], v[156:171]\n"
    "ds_read_b64_tr_b16 a[144:145], v146 offset:57344\n"
    "ds_read_b64_tr_b16 a[146:147], v146 offset:58368\n"
    "v_mfma_f32_32x32x16_bf16 v[220:235], a[164:167], v[120:123], v[220:235]\n"
    "ds_read_b64_tr_b16 a[148:149], v145 offset:59392\n"
    "ds_read_b64_tr_b16 a[150:151], v145 offset:60416\n"
    "v_mfma_f32_32x32x16_bf16 v[156:171], a[168:171], v[124:127], v[156:171]\n"
    "ds_read_b64_tr_b16 a[152:153], v146 offset:61440\n"
    "ds_read_b64_tr_b16 a[154:155], v146 offset:62464\n"
    "v_mfma_f32_32x32x16_bf16 v[220:235], a[172:175], v[124:127], v[220:235]\n"
    "ds_read_b64_tr_b16 a[156:157], v145 offset:63488\n"
    "ds_read_b64_tr_b16 a[158:159], v145 offset:64512\n"
    "s_xor_b32 s13, s12, 128\n"
    "s_add_u32 m0, s75, 2048\n"
    "s_nop 0\n"
    "buffer_load_dwordx4 v246, s[52:55], s13 offen offset:0 lds\n"
    "s_waitcnt lgkmcnt(8)\n"
    "v_mfma_f32_32x32x16_bf16 v[156:171], a[128:131], v[128:131], v[156:171]\n"
    "ds_read_b64_tr_b16 a[160:161], v146 offset:18432\n"
    "ds_read_b64_tr_b16 a[162:163], v146 offset:19456\n"
    "v_mfma_f32_32x32x16_bf16 v[220:235], a[132:135], v[128:131], v[220:235]\n"
    "ds_read_b64_tr_b16 a[164:165], v146 offset:22528\n"
    "ds_read_b64_tr_b16 a[166:167], v146 offset:23552\n"
    "buffer_load_dwordx4 v247, s[52:55], s13 offen offset:1024 lds\n"
    "s_waitcnt vmcnt(11)\n"
    "v_cmp_eq_u32 vcc, s72, v245\n"
    "s_nop 0\n"
    "s_andn2_b32 s5, 0x30, vcc_lo\n"
    "s_cbranch_scc1 LfsA4\n"
    "LfoA4:\n"
    "v_mfma_f32_32x32x16_bf16 v[156:171], a[136:139], v[132:135], v[156:171]\n"
    "ds_read_b64_tr_b16 a[168:169], v146 offset:26624\n"
    "ds_read_b64_tr_b16 a[170:171], v146 offset:27648\n"
    "v_mfma_f32_32x32x16_bf16 v[220:235], a[140:143], v[132:135], v[220:235]\n"
    "ds_read_b64_tr_b16 a[172:173], v146 offset:30720\n"
    "ds_read_b64_tr_b16 a[174:175], v146 offset:31744\n"
    "s_add_u32 s19, s90, 13824\n"
    "buffer_load_dwordx4 v[80:83], v248, s[60:63], s19 offen offset:512 sc1\n"
    "s_waitcnt lgkmcnt(8)\n"
    "v_mfma_f32_32x32x16_bf16 v[156:171], a[144:147], v[136:139], v[156:171]\n"
    "ds_read_b64_tr_b16 a[128:129], v146 offset:34816\n"
    "ds_read_b64_tr_b16 a[130:131], v146 offset:35840\n"
    "v_mfma_f32_32x32x16_bf16 v[220:235], a[148:151], v[136:139], v[220:235]\n"
    "ds_read_b64_tr_b16 a[132:133], v146 offset:38912\n"
    "ds_read_b64_tr_b16 a[134:135], v146 offset:39936\n"
    "v_mfma_f32_32x32x16_bf16 v[156:171], a[152:155], v[140:143], v[156:171]\n"
    "ds_read_b64_tr_b16 a[136:137], v146 offset:43008\n"
    "ds_read_b64_tr_b16 a[138:139], v146 offset:44032\n"
    "buffer_load_dwordx4 v[84:87], v248, s[60:63], s19 offen offset:1536 sc1\n"
    "v_mfma_f32_32x32x16_bf16 v[220:235], a[156:159], v[140:143], v[220:235]\n"
    "ds_read_b64_tr_b16 a[140:141], v146 offset:47104\n"
    "ds_read_b64_tr_b16 a[142:143], v146 offset:48128\n"
    "s_waitcnt lgkmcnt(8)\n"
    "s_barrier\n"
    "v_mfma_f32_32x32x16_bf16 v[32:47], a[160:163], v[96:99], 0\n"
    "ds_read_b64_tr_b16 a[144:145], v146 offset:51200\n"
    "ds_read_b64_tr_b16 a[146:147], v146 offset:52224\n"
    "buffer_load_dwordx4 v[88:91], v248, s[60:63], s19 offen offset:2560 sc1\n"
    "s_add_u32 s79, s88, 32256\n"
    "buffer_load_dwordx4 v[188:191], v248, s[60:63], s79 offen offset:512 sc1\n"
    "v_mfma_f32_32x32x16_bf16 v[32:47], a[164:167], v[100:103], v[32:47]\n"
    "ds_read_b64_tr_b16 a[148:149], v146 offset:55296\n"
    "ds_read_b64_tr_b16 a[150:151], v146 offset:56320\n"
    "v_mfma_f32_32x32x16_bf16 v[32:47], a[168:171], v[104:107], v[32:47]\n"
    "ds_read_b64_tr_b16 a[152:153], v146 offset:59392\n"
    "ds_read_b64_tr_b16 a[154:155], v146 offset:60416\n"
    "buffer_load_dwordx4 v[192:195], v248, s[60:63], s79 offen offset:1536 sc1\n"
    "v_mfma_f32_32x32x16_bf16 v[32:47], a[172:175], v[108:111], v[32:47]\n"
    "ds_read_b64_tr_b16 a[156:157], v146 offset:63488\n"
    "ds_read_b64_tr_b16 a[158:159], v146 offset:64512\n"
    "s_waitcnt lgkmcnt(8)\n"
    "v_mfma_f32_32x32x16_bf16 v[32:47], a[128:131], v[112:115], v[32:47]\n"
    "ds_read_b64_tr_b16 a[160:161], v148 offset:0\n"
    "ds_read_b64_tr_b16 a[162:163], v148 offset:1024\n"
    "buffer_load_dwordx4 v[92:95], v248, s[60:63], s19 offen offset:3584 sc1\n"
    "buffer_load_dword v151, v144, s[60:63], s19 offen offset:0 sc1\n"
    "v_mfma_f32_32x32x16_bf16 v[32:47], a[132:135], v[116:119], v[32:47]\n"
    "ds_read_b64_tr_b16 a[164:165], v147 offset:2048\n"
    "ds_read_b64_tr_b16 a[166:167], v147 offset:3072\n"
    "v_mfma_f32_32x32x16_bf16 v[32:47], a[136:139], v[120:123], v[32:47]\n"
    "ds_read_b64_tr_b16 a[168:169], v148 offset:2048\n"
    "ds_read_b64_tr_b16 a[170:171], v148 offset:3072\n"
    "v_mfma_f32_32x32x16_bf16 v[32:47], a[140:143], v[124:127], v[32:47]\n"
    "ds_read_b64_tr_b16 a[172:173], v148 offset:4096\n"
    "ds_read_b64_tr_b16 a[174:175], v148 offset:5120\n"
    "buffer_load_dwordx4 v[196:199], v248, s[60:63], s79 offen offset:2560 sc1\n"
    "s_waitcnt lgkmcnt(8)\n"
    "v_mfma_f32_32x32x16_bf16 v[32:47], a[144:147], v[128:131], v[32:47]\n"
    "ds_read_b64_tr_b16 a[128:129], v147 offset:6144\n"
    "ds_read_b64_tr_b16 a[130:131], v147 offset:7168\n"
    "v_mfma_f32_32x32x16_bf16 v[32:47], a[148:151], v[132:135], v[32:47]\n"
    "ds_read_b64_tr_b16 a[132:133], v148 offset:6144\n"
    "ds_read_b64_tr_b16 a[134:135], v148 offset:7168\n"
    "buffer_load_dwordx4 v[200:203], v248, s[60:63], s79 offen offset:3584 sc1\n"
    "v_mfma_f32_32x32x16_bf16 v[32:47], a[152:155], v[136:139], v[32:47]\n"
    "ds_read_b64_tr_b16 a[136:137], v148 offset:8192\n"
    "ds_read_b64_tr_b16 a[138:139], v148 offset:9216\n"
    "v_mfma_f32_32x32x16_bf16 v[32:47], a[156:159], v[140:143], v[32:47]\n"
    "ds_read_b64_tr_b16 a[140:141], v147 offset:10240\n"
    "ds_read_b64_tr_b16 a[142:143], v147 offset:11264\n"
    "buffer_load_dword v20, v144, s[60:63], s79 offen offset:0 sc1\n"
    "s_waitcnt lgkmcnt(8)\n"
    "v_mfma_f32_32x32x16_bf16 v[156:171], a[160:163], v[0:3], v[156:171]\n"
    "ds_read_b64_tr_b16 a[144:145], v148 offset:10240\n"
    "ds_read_b64_tr_b16 a[146:147], v148 offset:11264\n"
    "s_add_u32 s80, s88, 36864\n"
    "buffer_load_dwordx4 v[204:207], v248, s[60:63], s80 offen offset:512 sc1\n"
    "v_mfma_f32_32x32x16_bf16 v[220:235], a[164:167], v[0:3], v[220:235]\n"
    "ds_read_b64_tr_b16 a[148:149], v148 offset:12288\n"
    "ds_read_b64_tr_b16 a[150:151], v148 offset:13312\n"
    "v_mfma_f32_32x32x16_bf16 v[32:47], a[168:171], v[0:3], v[32:47]\n"
    "ds_read_b64_tr_b16 a[152:153], v147 offset:14336\n"
    "ds_read_b64_tr_b16 a[154:155], v147 offset:15360\n"
    "buffer_load_dwordx4 v[208:211], v248, s[60:63], s80 offen offset:1536 sc1\n"
    "v_mfma_f32_32x32x16_bf16 v[156:171], a[172:175], v[8:11], v[156:171]\n"
    "ds_read_b64_tr_b16 a[156:157], v148 offset:14336\n"
    "ds_read_b64_tr_b16 a[158:159], v148 offset:15360\n"
    "s_waitcnt lgkmcnt(8)\n"
    "v_mfma_f32_32x32x16_bf16 v[220:235], a[128:131], v[8:11], v[220:235]\n"
    "ds_read_b64_tr_b16 a[160:161], v148 offset:16384\n"
    "ds_read_b64_tr_b16 a[162:163], v148 offset:17408\n"
    "buffer_load_dwordx4 v[212:215], v248, s[60:63], s80 offen offset:2560 sc1\n"
    "v_mfma_f32_32x32x16_bf16 v[32:47], a[132:135], v[8:11], v[32:47]\n"
    "ds_read_b64_tr_b16 a[164:165], v147 offset:18432\n"
    "ds_read_b64_tr_b16 a[166:167], v147 offset:19456\n"
    "v_mfma_f32_32x32x16_bf16 v[156:171], a[136:139], v[16:19], v[156:171]\n"
    "ds_read_b64_tr_b16 a[168:169], v148 offset:18432\n"
    "ds_read_b64_tr_b16 a[170:171], v148 offset:19456\n"
    "v_mfma_f32_32x32x16_bf16 v[220:235], a[140:143], v[16:19], v[220:235]\n"
    "ds_read_b64_tr_b16 a[172:173], v148 offset:20480\n"
    "ds_read_b64_tr_b16 a[174:175], v148 offset:21504\n"
    "buffer_load_dwordx4 v[216:219], v248, s[60:63], s80 offen offset:3584 sc1\n"
    "s_waitcnt lgkmcnt(8)\n"
    "v_mfma_f32_32x32x16_bf16 v[32:47], a[144:147], v[16:19], v[32:47]\n"
    "ds_read_b64_tr_b16 a[128:129], v147 offset:22528\n"
    "ds_read_b64_tr_b16 a[130:131], v147 offset:23552\n"
    "v_mfma_f32_32x32x16_bf16 v[156:171], a[148:151], v[24:27], v[156:171]\n"
    "ds_read_b64_tr_b16 a[132:133], v148 offset:22528\n"
    "ds_read_b64_tr_b16 a[134:135], v148 offset:23552\n"
    "buffer_load_dword v21, v144, s[60:63], s80 offen offset:0 sc1\n"
    "v_mfma_f32_32x32x16_bf16 v[220:235], a[152:155], v[24:27], v[220:235]\n"
    "ds_read_b64_tr_b16 a[136:137], v148 offset:24576\n"
    "ds_read_b64_tr_b16 a[138:139], v148 offset:25600\n"
    "v_mfma_f32_32x32x16_bf16 v[32:47], a[156:159], v[24:27], v[32:47]\n"
    "ds_read_b64_tr_b16 a[140:141], v147 offset:26624\n"
    "ds_read_b64_tr_b16 a[142:143], v147 offset:27648\n"
    "s_waitcnt lgkmcnt(8)\n"
    "v_mfma_f32_32x32x16_bf16 v[156:171], a[160:163], v[64:67], v[156:171]\n"
    "ds_read_b64_tr_b16 a[144:145], v148 offset:26624\n"
    "ds_read_b64_tr_b16 a[146:147], v148 offset:27648\n"
    "v_mfma_f32_32x32x16_bf16 v[220:235], a[164:167], v[64:67], v[220:235]\n"
    "ds_read_b64_tr_b16 a[148:149], v148 offset:28672\n"
    "ds_read_b64_tr_b16 a[150:151], v148 offset:29696\n"
    "s_add_u32 s7, s90, 23040\n"
    "buffer_load_dwordx4 v[172:175], v248, s[60:63], s7 offen offset:512 sc1\n"
    "buffer_load_dwordx4 v[176:179], v248, s[60:63], s7 offen offset:1536 sc1\n"
    "v_mfma_f32_32x32x16_bf16 v[32:47], a[168:171], v[64:67], v[32:47]\n"
    "ds_read_b64_tr_b16 a[152:153], v147 offset:30720\n"
    "ds_read_b64_tr_b16 a[154:155], v147 offset:31744\n"
    "v_mfma_f32_32x32x16_bf16 v[156:171], a[172:175], v[68:71], v[156:171]\n"
    "ds_read_b64_tr_b16 a[156:157], v148 offset:30720\n"
    "ds_read_b64_tr_b16 a[158:159], v148 offset:31744\n"
    "buffer_load_dwordx4 v[180:183], v248, s[60:63], s7 offen offset:2560 sc1\n"
    "s_waitcnt lgkmcnt(8)\n"
    "v_mfma_f32_32x32x16_bf16 v[220:235], a[128:131], v[68:71], v[220:235]\n"
    "ds_read_b64_tr_b16 a[160:161], v148 offset:32768\n"
    "ds_read_b64_tr_b16 a[162:163], v148 offset:33792\n"
    "v_mfma_f32_32x32x16_bf16 v[32:47], a[132:135], v[68:71], v[32:47]\n"
    "ds_read_b64_tr_b16 a[164:165], v147 offset:34816\n"
    "ds_read_b64_tr_b16 a[166:167], v147 offset:35840\n"
    "buffer_load_dwordx4 v[184:187], v248, s[60:63], s7 offen offset:3584 sc1\n"
    "v_mfma_f32_32x32x16_bf16 v[156:171], a[136:139], v[72:75], v[156:171]\n"
    "ds_read_b64_tr_b16 a[168:169], v148 offset:34816\n"
    "ds_read_b64_tr_b16 a[170:171], v148 offset:35840\n"
    "v_mfma_f32_32x32x16_bf16 v[220:235], a[140:143], v[72:75], v[220:235]\n"
    "ds_read_b64_tr_b16 a[172:173], v148 offset:36864\n"
    "ds_read_b64_tr_b16 a[174:175], v148 offset:37888\n"
    "buffer_load_dword v22, v144, s[60:63], s7 offen offset:0 sc1\n"
    "s_waitcnt lgkmcnt(8)\n"
    "v_mfma_f32_32x32x16_bf16 v[32:47], a[144:147], v[72:75], v[32:47]\n"
    "ds_read_b64_tr_b16 a[128:129], v147 offset:38912\n"
    "ds_read_b64_tr_b16 a[130:131], v147 offset:39936\n"
    "v_mfma_f32_32x32x16_bf16 v[156:171], a[148:151], v[76:79], v[156:171]\n"
    "ds_read_b64_tr_b16 a[132:133], v148 offset:38912\n"
    "ds_read_b64_tr_b16 a[134:135], v148 offset:39936\n"
    "s_waitcnt vmcnt(19)\n"
    "v_add3_u32 v154, v80, v81, s72\n"
    "v_add3_u32 v154, v154, v82, v83\n"
    "s_waitcnt vmcnt(18)\n"
    "v_add3_u32 v154, v154, v84, v85\n"
    "v_add3_u32 v154, v154, v86, v87\n"
    "v_mfma_f32_32x32x16_bf16 v[220:235], a[152:155], v[76:79], v[220:235]\n"
    "ds_read_b64_tr_b16 a[136:137], v148 offset:40960\n"
    "ds_read_b64_tr_b16 a[138:139], v148 offset:41984\n"
    "s_waitcnt vmcnt(17)\n"
    "v_add3_u32 v154, v154, v88, v89\n"
    "v_add3_u32 v154, v154, v90, v91\n"
    "s_waitcnt vmcnt(14)\n"
    "v_add3_u32 v154, v154, v92, v93\n"
    "v_add3_u32 v154, v154, v94, v95\n"
    "v_mfma_f32_32x32x16_bf16 v[32:47], a[156:159], v[76:79], v[32:47]\n"
    "ds_read_b64_tr_b16 a[140:141], v147 offset:43008\n"
    "ds_read_b64_tr_b16 a[142:143], v147 offset:44032\n"
    "s_waitcnt vmcnt(13)\n"
    "v_cmp_ne_u32 vcc, v154, v151\n"
    "s_nop 0\n"
    "s_cbranch_vccnz LreA5\n"
    "LokA5:\n"
    "s_waitcnt lgkmcnt(8)\n"
    "v_mfma_f32_32x32x16_bf16 v[156:171], a[160:163], v[80:83], v[156:171]\n"
    "ds_read_b64_tr_b16 a[144:145], v148 offset:43008\n"
    "ds_read_b64_tr_b16 a[146:147], v148 offset:44032\n"
    "v_mfma_f32_32x32x16_bf16 v[220:235], a[164:167], v[80:83], v[220:235]\n"
    "ds_read_b64_tr_b16 a[148:149], v148 offset:45056\n"
    "ds_read_b64_tr_b16 a[150:151], v148 offset:46080\n"
    "v_add3_u32 v23, v188, v189, s68\n"
    "v_add3_u32 v23, v23, v190, v191\n"
    "v_add3_u32 v23, v23, v192, v193\n"
    "v_add3_u32 v23, v23, v194, v195\n"
    "v_mfma_f32_32x32x16_bf16 v[32:47], a[168:171], v[80:83], v[32:47]\n"
    "ds_read_b64_tr_b16 a[152:153], v147 offset:47104\n"
    "ds_read_b64_tr_b16 a[154:155], v147 offset:48128\n"
    "s_waitcnt vmcnt(12)\n"
    "v_add3_u32 v23, v23, v196, v197\n"
    "v_add3_u32 v23, v23, v198, v199\n"
    "s_waitcnt vmcnt(11)\n"
    "v_add3_u32 v23, v23, v200, v201\n"
    "v_add3_u32 v23, v23, v202, v203\n"
    "v_mfma_f32_32x32x16_bf16 v[156:171], a[172:175], v[84:87], v[156:171]\n"
    "ds_read_b64_tr_b16 a[156:157], v148 offset:47104\n"
    "ds_read_b64_tr_b16 a[158:159], v148 offset:48128\n"
    "s_waitcnt vmcnt(9)\n"
    "v_add3_u32 v23, v23, v204, v205\n"
    "v_add3_u32 v23, v23, v206, v207\n"
    "s_waitcnt vmcnt(8)\n"
    "v_add3_u32 v23, v23, v208, v209\n"
    "v_add3_u32 v23, v23, v210, v211\n"
    "s_waitcnt lgkmcnt(8)\n"
    "s_barrier\n"
    "v_mfma_f32_32x32x16_bf16 v[220:235], a[128:131], v[84:87], v[220:235]\n"
    "ds_read_b64_tr_b16 a[160:161], v150 offset:0\n"
    "ds_read_b64_tr_b16 a[162:163], v150 offset:1024\n"
    "s_waitcnt vmcnt(7)\n"
    "v_add3_u32 v23, v23, v212, v213\n"
    "v_add3_u32 v23, v23, v214, v215\n"
    "s_waitcnt vmcnt(6)\n"
    "v_add3_u32 v23, v23, v216, v217\n"
    "v_add3_u32 v23, v23, v218, v219\n"
    "v_mfma_f32_32x32x16_bf16 v[32:47], a[132:135], v[84:87], v[32:47]\n"
    "ds_read_b64_tr_b16 a[164:165], v149 offset:2048\n"
    "ds_read_b64_tr_b16 a[166:167], v149 offset:3072\n"
    "s_waitcnt vmcnt(5)\n"
    "v_add_u32 v20, v20, v21\n"
    "v_cmp_ne_u32 vcc, v23, v20\n"
    "s_nop 0\n"
    "s_cbranch_vccnz LreA6\n"
    "LokA6:\n"
    "v_mfma_f32_32x32x16_bf16 v[156:171], a[136:139], v[88:91], v[156:171]\n"
    "ds_read_b64_tr_b16 a[168:169], v150 offset:2048\n"
    "ds_read_b64_tr_b16 a[170:171], v150 offset:3072\n"
    "v_mfma_f32_32x32x16_bf16 v[220:235], a[140:143], v[88:91], v[220:235]\n"
    "ds_read_b64_tr_b16 a[172:173], v150 offset:4096\n"
    "ds_read_b64_tr_b16 a[174:175], v150 offset:5120\n"
    "s_waitcnt lgkmcnt(8)\n"
    "v_mfma_f32_32x32x16_bf16 v[32:47], a[144:147], v[88:91], v[32:47]\n"
    "ds_read_b64_tr_b16 a[128:129], v149 offset:6144\n"
    "ds_read_b64_tr_b16 a[130:131], v149 offset:7168\n"
    "v_mfma_f32_32x32x16_bf16 v[156:171], a[148:151], v[92:95], v[156:171]\n"
    "ds_read_b64_tr_b16 a[132:133], v150 offset:6144\n"
    "ds_read_b64_tr_b16 a[134:135], v150 offset:7168\n"
    "v_mfma_f32_32x32x16_bf16 v[220:235], a[152:155], v[92:95], v[220:235]\n"
    "ds_read_b64_tr_b16 a[136:137], v150 offset:8192\n"
    "ds_read_b64_tr_b16 a[138:139], v150 offset:9216\n"
    "v_mfma_f32_32x32x16_bf16 v[32:47], a[156:159], v[92:95], v[32:47]\n"
    "ds_read_b64_tr_b16 a[140:141], v149 offset:10240\n"
    "ds_read_b64_tr_b16 a[142:143], v149 offset:11264\n"
    "s_waitcnt lgkmcnt(8)\n"
    "v_mfma_f32_32x32x16_bf16 v[156:171], a[160:163], v[188:191], v[156:171]\n"
    "ds_read_b64_tr_b16 a[144:145], v150 offset:10240\n"
    "ds_read_b64_tr_b16 a[146:147], v150 offset:11264\n"
    "v_mfma_f32_32x32x16_bf16 v[220:235], a[164:167], v[188:191], v[220:235]\n"
    "ds_read_b64_tr_b16 a[148:149], v150 offset:12288\n"
    "ds_read_b64_tr_b16 a[150:151], v150 offset:13312\n"
    "v_mfma_f32_32x32x16_bf16 v[32:47], a[168:171], v[188:191], v[32:47]\n"
    "ds_read_b64_tr_b16 a[152:153], v149 offset:14336\n"
    "ds_read_b64_tr_b16 a[154:155], v149 offset:15360\n"
    "v_mfma_f32_32x32x16_bf16 v[156:171], a[172:175], v[192:195], v[156:171]\n"
    "ds_read_b64_tr_b16 a[156:157], v150 offset:14336\n"
    "ds_read_b64_tr_b16 a[158:159], v150 offset:15360\n"
    "s_waitcnt lgkmcnt(8)\n"
    "v_mfma_f32_32x32x16_bf16 v[220:235], a[128:131], v[192:195], v[220:235]\n"
    "ds_read_b64_tr_b16 a[160:161], v146 offset:0\n"
    "ds_read_b64_tr_b16 a[162:163], v146 offset:1024\n"
    "v_mfma_f32_32x32x16_bf16 v[32:47], a[132:135], v[192:195], v[32:47]\n"
    "ds_read_b64_tr_b16 a[164:165], v145 offset:2048\n"
    "ds_read_b64_tr_b16 a[166:167], v145 offset:3072\n"
    "v_mfma_f32_32x32x16_bf16 v[156:171], a[136:139], v[196:199], v[156:171]\n"
    "ds_read_b64_tr_b16 a[168:169], v146 offset:4096\n"
    "ds_read_b64_tr_b16 a[170:171], v146 offset:5120\n"
    "v_mfma_f32_32x32x16_bf16 v[220:235], a[140:143], v[196:199], v[220:235]\n"
    "ds_read_b64_tr_b16 a[172:173], v145 offset:6144\n"
    "ds_read_b64_tr_b16 a[174:175], v145 offset:7168\n"
    "s_waitcnt vmcnt(4)\n"
    "v_add3_u32 v31, v172, v173, s72\n"
    "v_add3_u32 v31, v31, v174, v175\n"
    "s_waitcnt vmcnt(3)\n"
    "v_add3_u32 v31, v31, v176, v177\n"
    "v_add3_u32 v31, v31, v178, v179\n"
    "s_waitcnt lgkmcnt(8)\n"
    "v_mfma_f32_32x32x16_bf16 v[32:47], a[144:147], v[196:199], v[32:47]\n"
    "ds_read_b64_tr_b16 a[128:129], v146 offset:8192\n"
    "ds_read_b64_tr_b16 a[130:131], v146 offset:9216\n"
    "s_waitcnt vmcnt(2)\n"
    "v_add3_u32 v31, v31, v180, v181\n"
    "v_add3_u32 v31, v31, v182, v183\n"
    "s_waitcnt vmcnt(1)\n"
    "v_add3_u32 v31, v31, v184, v185\n"
    "v_add3_u32 v31, v31, v186, v187\n"
    "v_mfma_f32_32x32x16_bf16 v[156:171], a[148:151], v[200:203], v[156:171]\n"
    "ds_read_b64_tr_b16 a[132:133], v146 offset:12288\n"
    "ds_read_b64_tr_b16 a[134:135], v146 offset:13312\n"
    "s_waitcnt vmcnt(0)\n"
    "v_cmp_ne_u32 vcc, v31, v22\n"
    "s_nop 0\n"
    "s_cbranch_vccnz LreA7\n"
    "LokA7:\n"
    "v_mfma_f32_32x32x16_bf16 v[220:235], a[152:155], v[200:203], v[220:235]\n"
    "ds_read_b64_tr_b16 a[136:137], v148 offset:49152\n"
    "ds_read_b64_tr_b16 a[138:139], v148 offset:50176\n"
    "v_mfma_f32_32x32x16_bf16 v[32:47], a[156:159], v[200:203], v[32:47]\n"
    "ds_read_b64_tr_b16 a[140:141], v148 offset:53248\n"
    "ds_read_b64_tr_b16 a[142:143], v148 offset:54272\n"
    "s_waitcnt lgkmcnt(8)\n"
    "v_mfma_f32_32x32x16_bf16 v[156:171], a[160:163], v[204:207], v[156:171]\n"
    "ds_read_b64_tr_b16 a[144:145], v148 offset:57344\n"
    "ds_read_b64_tr_b16 a[146:147], v148 offset:58368\n"
    "v_mfma_f32_32x32x16_bf16 v[220:235], a[164:167], v[204:207], v[220:235]\n"
    "ds_read_b64_tr_b16 a[148:149], v148 offset:61440\n"
    "ds_read_b64_tr_b16 a[150:151], v148 offset:62464\n"
    "v_mfma_f32_32x32x16_bf16 v[156:171], a[168:171], v[208:211], v[156:171]\n"
    "ds_read_b64_tr_b16 a[152:153], v145 offset:10240\n"
    "ds_read_b64_tr_b16 a[154:155], v145 offset:11264\n"
    "v_mfma_f32_32x32x16_bf16 v[220:235], a[172:175], v[208:211], v[220:235]\n"
    "ds_read_b64_tr_b16 a[156:157], v145 offset:14336\n"
    "ds_read_b64_tr_b16 a[158:159], v145 offset:15360\n"
    "s_waitcnt lgkmcnt(8)\n"
    "v_mfma_f32_32x32x16_bf16 v[156:171], a[128:131], v[212:215], v[156:171]\n"
    "ds_read_b64_tr_b16 a[160:161], v147 offset:51200\n"
    "ds_read_b64_tr_b16 a[162:163], v147 offset:52224\n"
    "v_mfma_f32_32x32x16_bf16 v[156:171], a[132:135], v[216:219], v[156:171]\n"
    "ds_read_b64_tr_b16 a[164:165], v147 offset:55296\n"
    "ds_read_b64_tr_b16 a[166:167], v147 offset:56320\n"
    "v_mfma_f32_32x32x16_bf16 v[156:171], a[136:139], v[172:175], v[156:171]\n"
    "ds_read_b64_tr_b16 a[168:169], v147 offset:59392\n"
    "ds_read_b64_tr_b16 a[170:171], v147 offset:60416\n"
    "v_mfma_f32_32x32x16_bf16 v[156:171], a[140:143], v[176:179], v[156:171]\n"
    "ds_read_b64_tr_b16 a[172:173], v147 offset:63488\n"
    "ds_read_b64_tr_b16 a[174:175], v147 offset:64512\n"
    "s_waitcnt lgkmcnt(8)\n"
    "v_mfma_f32_32x32x16_bf16 v[156:171], a[144:147], v[180:183], v[156:171]\n"
    "ds_read_b64_tr_b16 a[128:129], v146 offset:2048\n"
    "ds_read_b64_tr_b16 a[130:131], v146 offset:3072\n"
    "v_mfma_f32_32x32x16_bf16 v[156:171], a[148:151], v[184:187], v[156:171]\n"
    "ds_read_b64_tr_b16 a[132:133], v146 offset:6144\n"
    "ds_read_b64_tr_b16 a[134:135], v146 offset:7168\n"
    "v_mfma_f32_32x32x16_bf16 v[220:235], a[152:155], v[212:215], v[220:235]\n"
    "ds_read_b64_tr_b16 a[136:137], v146 offset:10240\n"
    "ds_read_b64_tr_b16 a[138:139], v146 offset:11264\n"
    "s_nop 6\n"
    "v_cvt_pk_bf16_f32 v156, v156, v157\n"
    "v_cvt_pk_bf16_f32 v157, v158, v159\n"
    "v_cvt_pk_bf16_f32 v158, v160, v161\n"
    "v_cvt_pk_bf16_f32 v159, v162, v163\n"
    "buffer_store_dwordx4 v[156:159], v248, s[60:63], s91 offen offset:256 sc1\n"
    "v_add3_u32 v5, v156, v157, s72\n"
    "v_mfma_f32_32x32x16_bf16 v[220:235], a[156:159], v[216:219], v[220:235]\n"
    "ds_read_b64_tr_b16 a[140:141], v146 offset:14336\n"
    "ds_read_b64_tr_b16 a[142:143], v146 offset:15360\n"
    "v_add3_u32 v5, v5, v158, v159\n"
    "v_cvt_pk_bf16_f32 v160, v164, v165\n"
    "v_cvt_pk_bf16_f32 v161, v166, v167\n"
    "v_cvt_pk_bf16_f32 v162, v168, v169\n"
    "v_cvt_pk_bf16_f32 v163, v170, v171\n"
    "buffer_store_dwordx4 v[160:163], v248, s[60:63], s91 offen offset:1280 sc1\n"
    "s_waitcnt lgkmcnt(8)\n"
    "v_mfma_f32_32x32x16_bf16 v[220:235], a[160:163], v[172:175], v[220:235]\n"
    "ds_read_b64_tr_b16 a[144:145], v148 offset:51200\n"
    "ds_read_b64_tr_b16 a[146:147], v148 offset:52224\n"
    "v_add3_u32 v5, v5, v160, v161\n"
    "v_add3_u32 v5, v5, v162, v163\n"
    "buffer_store_dword v5, v144, s[60:63], s91 offen sc1\n"
    "s_xor_b32 s6, s2, 1024\n"
    "v_mov_b32 v12, s72\n"
    "buffer_store_dword v12, off, s[60:63], s6 offset:4 sc1\n"
    "v_mfma_f32_32x32x16_bf16 v[220:235], a[164:167], v[176:179], v[220:235]\n"
    "ds_read_b64_tr_b16 a[148:149], v148 offset:55296\n"
    "ds_read_b64_tr_b16 a[150:151], v148 offset:56320\n"
    "s_xor_b32 s7, s2, 2048\n"
    "v_mov_b32 v12, s72\n"
    "buffer_store_dword v12, off, s[60:63], s7 offset:8 sc1\n"
    "s_xor_b32 s6, s2, 3072\n"
    "v_mov_b32 v12, s72\n"
    "buffer_store_dword v12, off, s[60:63], s6 offset:12 sc1\n"
    "v_mfma_f32_32x32x16_bf16 v[220:235], a[168:171], v[180:183], v[220:235]\n"
    "ds_read_b64_tr_b16 a[152:153], v148 offset:59392\n"
    "ds_read_b64_tr_b16 a[154:155], v148 offset:60416\n"
    "v_mfma_f32_32x32x16_bf16 v[220:235], a[172:175], v[184:187], v[220:235]\n"
    "ds_read_b64_tr_b16 a[156:157], v148 offset:63488\n"
    "ds_read_b64_tr_b16 a[158:159], v148 offset:64512\n"
    "s_waitcnt lgkmcnt(8)\n"
    "v_mfma_f32_32x32x16_bf16 v[32:47], a[128:131], v[204:207], v[32:47]\n"
    "ds_read_b64_tr_b16 a[160:161], v145 offset:16384\n"
    "ds_read_b64_tr_b16 a[162:163], v145 offset:17408\n"
    "s_nop 5\n"
    "v_cvt_pk_bf16_f32 v220, v220, v221\n"
    "v_cvt_pk_bf16_f32 v221, v222, v223\n"
    "v_cvt_pk_bf16_f32 v222, v224, v225\n"
    "v_cvt_pk_bf16_f32 v223, v226, v227\n"
    "buffer_store_dwordx4 v[220:223], v248, s[60:63], s92 offen offset:256 sc1\n"
    "v_add3_u32 v6, v220, v221, s72\n"
    "v_mfma_f32_32x32x16_bf16 v[32:47], a[132:135], v[208:211], v[32:47]\n"
    "ds_read_b64_tr_b16 a[164:165], v145 offset:20480\n"
    "ds_read_b64_tr_b16 a[166:167], v145 offset:21504\n"
    "v_add3_u32 v6, v6, v222, v223\n"
    "v_cvt_pk_bf16_f32 v224, v228, v229\n"
    "v_cvt_pk_bf16_f32 v225, v230, v231\n"
    "v_cvt_pk_bf16_f32 v226, v232, v233\n"
    "v_cvt_pk_bf16_f32 v227, v234, v235\n"
    "buffer_store_dwordx4 v[224:227], v248, s[60:63], s92 offen offset:1280 sc1\n"
    "v_mfma_f32_32x32x16_bf16 v[32:47], a[136:139], v[212:215], v[32:47]\n"
    "ds_read_b64_tr_b16 a[168:169], v145 offset:24576\n"
    "ds_read_b64_tr_b16 a[170:171], v145 offset:25600\n"
    "v_add3_u32 v6, v6, v224, v225\n"
    "v_add3_u32 v6, v6, v226, v227\n"
    "buffer_store_dword v6, v144, s[60:63], s92 offen sc1\n"
    "v_mfma_f32_32x32x16_bf16 v[32:47], a[140:143], v[216:219], v[32:47]\n"
    "ds_read_b64_tr_b16 a[172:173], v145 offset:28672\n"
    "ds_read_b64_tr_b16 a[174:175], v145 offset:29696\n"
    "s_waitcnt lgkmcnt(8)\n"
    "v_mfma_f32_32x32x16_bf16 v[32:47], a[144:147], v[172:175], v[32:47]\n"
    "ds_read_b64_tr_b16 a[128:129], v145 offset:32768\n"
    "ds_read_b64_tr_b16 a[130:131], v145 offset:33792\n"
    "v_mfma_f32_32x32x16_bf16 v[32:47], a[148:151], v[176:179], v[32:47]\n"
    "ds_read_b64_tr_b16 a[132:133], v145 offset:36864\n"
    "ds_read_b64_tr_b16 a[134:135], v145 offset:37888\n"
    "v_mfma_f32_32x32x16_bf16 v[32:47], a[152:155], v[180:183], v[32:47]\n"
    "ds_read_b64_tr_b16 a[136:137], v145 offset:40960\n"
    "ds_read_b64_tr_b16 a[138:139], v145 offset:41984\n"
    "v_mfma_f32_32x32x16_bf16 v[32:47], a[156:159], v[184:187], v[32:47]\n"
    "ds_read_b64_tr_b16 a[140:141], v145 offset:45056\n"
    "ds_read_b64_tr_b16 a[142:143], v145 offset:46080\n"
    "s_waitcnt lgkmcnt(8)\n"
    "v_mfma_f32_32x32x16_bf16 v[48:63], a[160:163], v[96:99], 0\n"
    "ds_read_b64_tr_b16 a[144:145], v145 offset:49152\n"
    "ds_read_b64_tr_b16 a[146:147], v145 offset:50176\n"
    "s_nop 5\n"
    "v_cvt_pk_bf16_f32 v32, v32, v33\n"
    "v_cvt_pk_bf16_f32 v33, v34, v35\n"
    "v_cvt_pk_bf16_f32 v34, v36, v37\n"
    "v_cvt_pk_bf16_f32 v35, v38, v39\n"
    "buffer_store_dwordx4 v[32:35], v248, s[60:63], s93 offen offset:256 sc1\n"
    "v_add3_u32 v7, v32, v33, s72\n"
    "v_mfma_f32_32x32x16_bf16 v[48:63], a[164:167], v[100:103], v[48:63]\n"
    "ds_read_b64_tr_b16 a[148:149], v145 offset:53248\n"
    "ds_read_b64_tr_b16 a[150:151], v145 offset:54272\n"
    "v_add3_u32 v7, v7, v34, v35\n"
    "v_cvt_pk_bf16_f32 v36, v40, v41\n"
    "v_cvt_pk_bf16_f32 v37, v42, v43\n"
    "v_cvt_pk_bf16_f32 v38, v44, v45\n"
    "v_cvt_pk_bf16_f32 v39, v46, v47\n"
    "buffer_store_dwordx4 v[36:39], v248, s[60:63], s93 offen offset:1280 sc1\n"
    "v_mfma_f32_32x32x16_bf16 v[48:63], a[168:171], v[104:107], v[48:63]\n"
    "ds_read_b64_tr_b16 a[152:153], v145 offset:57344\n"
    "ds_read_b64_tr_b16 a[154:155], v145 offset:58368\n"
    "v_add3_u32 v7, v7, v36, v37\n"
    "v_add3_u32 v7, v7, v38, v39\n"
    "buffer_store_dword v7, v144, s[60:63], s93 offen sc1\n"
    "v_mfma_f32_32x32x16_bf16 v[48:63], a[172:175], v[108:111], v[48:63]\n"
    "ds_read_b64_tr_b16 a[156:157], v145 offset:61440\n"
    "ds_read_b64_tr_b16 a[158:159], v145 offset:62464\n"
    "s_waitcnt lgkmcnt(8)\n"
    "v_mfma_f32_32x32x16_bf16 v[48:63], a[128:131], v[112:115], v[48:63]\n"
    "ds_read_b64_tr_b16 a[160:161], v147 offset:0\n"
    "ds_read_b64_tr_b16 a[162:163], v147 offset:1024\n"
    "v_mfma_f32_32x32x16_bf16 v[48:63], a[132:135], v[116:119], v[48:63]\n"
    "ds_read_b64_tr_b16 a[164:165], v147 offset:4096\n"
    "ds_read_b64_tr_b16 a[166:167], v147 offset:5120\n"
    "v_and_b32 v13, 0x7c, v144\n"
    "buffer_load_dword v13, v13, s[60:63], s2 offen sc1\n"
    "v_mfma_f32_32x32x16_bf16 v[48:63], a[136:139], v[120:123], v[48:63]\n"
    "ds_read_b64_tr_b16 a[168:169], v147 offset:8192\n"
    "ds_read_b64_tr_b16 a[170:171], v147 offset:9216\n"
    "v_mfma_f32_32x32x16_bf16 v[48:63], a[140:143], v[124:127], v[48:63]\n"
    "ds_read_b64_tr_b16 a[172:173], v147 offset:12288\n"
    "ds_read_b64_tr_b16 a[174:175], v147 offset:13312\n"
    "s_waitcnt lgkmcnt(8)\n"
    "v_mfma_f32_32x32x16_bf16 v[48:63], a[144:147], v[128:131], v[48:63]\n"
    "ds_read_b64_tr_b16 a[128:129], v147 offset:16384\n"
    "ds_read_b64_tr_b16 a[130:131], v147 offset:17408\n"
    "v_mfma_f32_32x32x16_bf16 v[48:63], a[148:151], v[132:135], v[48:63]\n"
    "ds_read_b64_tr_b16 a[132:133], v147 offset:20480\n"
    "ds_read_b64_tr_b16 a[134:135], v147 offset:21504\n"
    "v_mfma_f32_32x32x16_bf16 v[48:63], a[152:155], v[136:139], v[48:63]\n"
    "ds_read_b64_tr_b16 a[136:137], v147 offset:24576\n"
    "ds_read_b64_tr_b16 a[138:139], v147 offset:25600\n"
    "v_mfma_f32_32x32x16_bf16 v[48:63], a[156:159], v[140:143], v[48:63]\n"
    "ds_read_b64_tr_b16 a[140:141], v147 offset:28672\n"
    "ds_read_b64_tr_b16 a[142:143], v147 offset:29696\n"
    "s_waitcnt lgkmcnt(8)\n"
    "v_mfma_f32_32x32x16_bf16 v[48:63], a[160:163], v[0:3], v[48:63]\n"
    "ds_read_b64_tr_b16 a[144:145], v147 offset:32768\n"
    "ds_read_b64_tr_b16 a[146:147], v147 offset:33792\n"
    "v_mfma_f32_32x32x16_bf16 v[48:63], a[164:167], v[8:11], v[48:63]\n"
    "ds_read_b64_tr_b16 a[148:149], v147 offset:36864\n"
    "ds_read_b64_tr_b16 a[150:151], v147 offset:37888\n"
    "v_mfma_f32_32x32x16_bf16 v[48:63], a[168:171], v[16:19], v[48:63]\n"
    "ds_read_b64_tr_b16 a[152:153], v147 offset:40960\n"
    "ds_read_b64_tr_b16 a[154:155], v147 offset:41984\n"
    "v_mfma_f32_32x32x16_bf16 v[48:63], a[172:175], v[24:27], v[48:63]\n"
    "ds_read_b64_tr_b16 a[156:157], v147 offset:45056\n"
    "ds_read_b64_tr_b16 a[158:159], v147 offset:46080\n"
    "s_waitcnt lgkmcnt(8)\n"
    "v_mfma_f32_32x32x16_bf16 v[48:63], a[128:131], v[64:67], v[48:63]\n"
    "ds_read_b64_tr_b16 a[160:161], v149 offset:0\n"
    "ds_read_b64_tr_b16 a[162:163], v149 offset:1024\n"
    "v_mfma_f32_32x32x16_bf16 v[48:63], a[132:135], v[68:71], v[48:63]\n"
    "ds_read_b64_tr_b16 a[164:165], v149 offset:4096\n"
    "ds_read_b64_tr_b16 a[166:167], v149 offset:5120\n"
    "v_mfma_f32_32x32x16_bf16 v[48:63], a[136:139], v[72:75], v[48:63]\n"
    "ds_read_b64_tr_b16 a[168:169], v149 offset:8192\n"
    "ds_read_b64_tr_b16 a[170:171], v149 offset:9216\n"
    "v_mfma_f32_32x32x16_bf16 v[48:63], a[140:143], v[76:79], v[48:63]\n"
    "ds_read_b64_tr_b16 a[172:173], v149 offset:12288\n"
    "ds_read_b64_tr_b16 a[174:175], v149 offset:13312\n"
    "s_waitcnt lgkmcnt(8)\n"
    "v_mfma_f32_32x32x16_bf16 v[48:63], a[144:147], v[80:83], v[48:63]\n"
    "ds_read_b64_tr_b16 a[128:129], v145 offset:0\n"
    "ds_read_b64_tr_b16 a[130:131], v145 offset:1024\n"
    "v_mfma_f32_32x32x16_bf16 v[48:63], a[148:151], v[84:87], v[48:63]\n"
    "ds_read_b64_tr_b16 a[132:133], v145 offset:4096\n"
    "ds_read_b64_tr_b16 a[134:135], v145 offset:5120\n"
    "v_mfma_f32_32x32x16_bf16 v[48:63], a[152:155], v[88:91], v[48:63]\n"
    "ds_read_b64_tr_b16 a[136:137], v145 offset:8192\n"
    "ds_read_b64_tr_b16 a[138:139], v145 offset:9216\n"
    "v_mfma_f32_32x32x16_bf16 v[48:63], a[156:159], v[92:95], v[48:63]\n"
    "ds_read_b64_tr_b16 a[140:141], v145 offset:12288\n"
    "ds_read_b64_tr_b16 a[142:143], v145 offset:13312\n"
    "s_waitcnt vmcnt(0)\n"
    "v_cmp_eq_u32 vcc, s72, v13\n"
    "s_nop 0\n"
    "s_andn2_b32 s5, 0xe, vcc_lo\n"
    "s_cbranch_scc1 LfsA8\n"
    "LfoA8:\n"
    "s_waitcnt lgkmcnt(8)\n"
    "v_mfma_f32_32x32x16_bf16 v[48:63], a[160:163], v[188:191], v[48:63]\n"
    "ds_read_b64_tr_b16 a[144:145], v147 offset:49152\n"
    "ds_read_b64_tr_b16 a[146:147], v147 offset:50176\n"
    "s_add_u32 s18, s94, 0\n"
    "buffer_load_dwordx4 v[164:167], v248, s[60:63], s18 offen offset:256 sc1\n"
    "buffer_load_dwordx4 v[168:171], v248, s[60:63], s18 offen offset:1280 sc1\n"
    "v_mfma_f32_32x32x16_bf16 v[48:63], a[164:167], v[192:195], v[48:63]\n"
    "ds_read_b64_tr_b16 a[148:149], v147 offset:53248\n"
    "ds_read_b64_tr_b16 a[150:151], v147 offset:54272\n"
    "buffer_load_dword v28, v144, s[60:63], s18 offen offset:0 sc1\n"
    "s_add_u32 s78, s94, 9216\n"
    "buffer_load_dwordx4 v[228:231], v248, s[60:63], s78 offen offset:256 sc1\n"
    "v_mfma_f32_32x32x16_bf16 v[48:63], a[168:171], v[196:199], v[48:63]\n"
    "ds_read_b64_tr_b16 a[152:153], v147 offset:57344\n"
    "ds_read_b64_tr_b16 a[154:155], v147 offset:58368\n"
    "buffer_load_dwordx4 v[232:235], v248, s[60:63], s78 offen offset:1280 sc1\n"
    "buffer_load_dword v29, v144, s[60:63], s78 offen offset:0 sc1\n"
    "v_mfma_f32_32x32x16_bf16 v[48:63], a[172:175], v[200:203], v[48:63]\n"
    "ds_read_b64_tr_b16 a[156:157], v147 offset:61440\n"
    "ds_read_b64_tr_b16 a[158:159], v147 offset:62464\n"
    "s_add_u32 s19, s94, 18432\n"
    "buffer_load_dwordx4 v[40:43], v248, s[60:63], s19 offen offset:256 sc1\n"
    "buffer_load_dwordx4 v[44:47], v248, s[60:63], s19 offen offset:1280 sc1\n"
    "s_waitcnt lgkmcnt(8)\n"
    "v_mfma_f32_32x32x16_bf16 v[48:63], a[128:131], v[204:207], v[48:63]\n"
    "buffer_load_dword v30, v144, s[60:63], s19 offen offset:0 sc1\n"
    "v_mfma_f32_32x32x16_bf16 v[48:63], a[132:135], v[208:211], v[48:63]\n"
    "v_mfma_f32_32x32x16_bf16 v[48:63], a[136:139], v[212:215], v[48:63]\n"
    "v_mfma_f32_32x32x16_bf16 v[48:63], a[140:143], v[216:219], v[48:63]\n"
    "s_waitcnt lgkmcnt(0)\n"
    "v_mfma_f32_32x32x16_bf16 v[48:63], a[144:147], v[172:175], v[48:63]\n"
    "v_mfma_f32_32x32x16_bf16 v[48:63], a[148:151], v[176:179], v[48:63]\n"
    "v_mfma_f32_32x32x16_bf16 v[48:63], a[152:155], v[180:183], v[48:63]\n"
    "v_mfma_f32_32x32x16_bf16 v[48:63], a[156:159], v[184:187], v[48:63]\n"
    "s_waitcnt vmcnt(8)\n"
    "v_add3_u32 v13, v164, v165, s72\n"
    "v_add3_u32 v13, v13, v166, v167\n"
    "s_waitcnt vmcnt(7)\n"
    "v_add3_u32 v13, v13, v168, v169\n"
    "v_add3_u32 v13, v13, v170, v171\n"
    "s_waitcnt vmcnt(6)\n"
    "v_cmp_ne_u32 vcc, v13, v28\n"
    "s_nop 0\n"
    "s_cbranch_vccnz LreA9\n"
    "LokA9:\n"
    "v_mfma_f32_32x32x16_bf16 v[48:63], a[208:211], v[164:167], v[48:63]\n"
    "v_mfma_f32_32x32x16_bf16 v[48:63], a[212:215], v[168:171], v[48:63]\n"
    "s_waitcnt vmcnt(5)\n"
    "v_add3_u32 v14, v228, v229, s72\n"
    "v_add3_u32 v14, v14, v230, v231\n"
    "s_waitcnt vmcnt(4)\n"
    "v_add3_u32 v14, v14, v232, v233\n"
    "v_add3_u32 v14, v14, v234, v235\n"
    "s_waitcnt vmcnt(3)\n"
    "v_cmp_ne_u32 vcc, v14, v29\n"
    "s_nop 0\n"
    "s_cbranch_vccnz LreA10\n"
    "LokA10:\n"
    "v_mfma_f32_32x32x16_bf16 v[48:63], a[208:211], v[228:231], v[48:63]\n"
    "v_mfma_f32_32x32x16_bf16 v[48:63], a[212:215], v[232:235], v[48:63]\n"
    "s_waitcnt vmcnt(2)\n"
    "v_add3_u32 v15, v40, v41, s72\n"
    "v_add3_u32 v15, v15, v42, v43\n"
    "s_waitcnt vmcnt(1)\n"
    "v_add3_u32 v15, v15, v44, v45\n"
    "v_add3_u32 v15, v15, v46, v47\n"
    "s_waitcnt vmcnt(0)\n"
    "v_cmp_ne_u32 vcc, v15, v30\n"
    "s_nop 0\n"
    "s_cbranch_vccnz LreA11\n"
    "LokA11:\n"
    "v_mfma_f32_32x32x16_bf16 v[48:63], a[208:211], v[40:43], v[48:63]\n"
    "v_mfma_f32_32x32x16_bf16 v[48:63], a[212:215], v[44:47], v[48:63]\n"
    "s_nop 11\n"
    "v_cvt_pk_bf16_f32 v48, v48, v49\n"
    "v_cvt_pk_bf16_f32 v49, v50, v51\n"
    "v_cvt_pk_bf16_f32 v50, v52, v53\n"
    "v_cvt_pk_bf16_f32 v51, v54, v55\n"
    "v_cvt_pk_bf16_f32 v52, v56, v57\n"
    "v_cvt_pk_bf16_f32 v53, v58, v59\n"
    "v_cvt_pk_bf16_f32 v54, v60, v61\n"
    "v_cvt_pk_bf16_f32 v55, v62, v63\n"
    "v_permlane16_swap_b32 v48, v52\n"
    "v_permlane16_swap_b32 v49, v53\n"
    "v_permlane16_swap_b32 v50, v54\n"
    "v_permlane16_swap_b32 v51, v55\n"
    "buffer_store_dwordx4 v[48:51], v242, s[64:67], s101 offen\n"
    "buffer_store_dwordx4 v[52:55], v242, s[64:67], s95 offen\n"
    "s_endpgm\n"
    "Lep1:\n"
    "s_mov_b32 s97, 4096\n"
    "buffer_load_dwordx4 v[112:115], v248, s[60:63], s97 offen offset:0 sc1\n"
    "buffer_load_dwordx4 v[116:119], v248, s[60:63], s97 offen offset:1024 sc1\n"
    "s_branch Lep1r\n"
    "Lep2:\n"
    "s_mov_b32 s71, 1000000\n"
    "Lep2l:\n"
    "v_cmp_ne_u64 vcc, s[72:73], v[112:113]\n"
    "s_nop 1\n"
    "s_cbranch_vccnz Lep2f\n"
    "v_cmp_ne_u64 vcc, s[72:73], v[114:115]\n"
    "s_nop 1\n"
    "s_cbranch_vccnz Lep2f\n"
    "v_cmp_ne_u64 vcc, s[72:73], v[116:117]\n"
    "s_nop 1\n"
    "s_cbranch_vccnz Lep2f\n"
    "v_cmp_ne_u64 vcc, s[72:73], v[118:119]\n"
    "s_nop 1\n"
    "s_cbranch_vccnz Lep2f\n"
    "v_mov_b32 v112, -1.0\n"
    "v_mov_b32 v114, s72\n"
    "v_mov_b32 v115, s73\n"
    "v_writelane_b32 v112, 0, 0\n"
    "s_nop 1\n"
    "buffer_store_dwordx2 v[114:115], v112, s[60:63], 0 offen sc1\n"
    "s_nop 1\n"
    "s_branch Lep2r\n"
    "Lep2f:\n"
    "s_sub_u32 s71, s71, 1\n"
    "s_cbranch_scc1 Lep2r\n"
    "s_mov_b32 s97, 4096\n"
    "buffer_load_dwordx4 v[112:115], v248, s[60:63], s97 offen offset:0 sc1\n"
    "buffer_load_dwordx4 v[116:119], v248, s[60:63], s97 offen offset:1024 sc1\n"
    "s_waitcnt vmcnt(0)\n"
    "s_branch Lep2l\n"
    "LreA1:\n"
    "s_mov_b32 s71, 1000000\n"
    "LlpA1:\n"
    "s_add_u32 s97, s100, 0\n"
    "buffer_load_dwordx4 v[172:175], v248, s[60:63], s97 offen offset:0 sc1\n"
    "buffer_load_dwordx4 v[176:179], v248, s[60:63], s97 offen offset:1024 sc1\n"
    "buffer_load_dwordx4 v[180:183], v248, s[60:63], s97 offen offset:2048 sc1\n"
    "buffer_load_dwordx4 v[184:187], v248, s[60:63], s97 offen offset:3072 sc1\n"
    "s_add_u32 s97, s100, 4096\n"
    "buffer_load_dwordx4 v[188:191], v248, s[60:63], s97 offen offset:0 sc1\n"
    "buffer_load_dwordx4 v[192:195], v248, s[60:63], s97 offen offset:1024 sc1\n"
    "buffer_load_dwordx4 v[196:199], v248, s[60:63], s97 offen offset:2048 sc1\n"
    "buffer_load_dwordx4 v[200:203], v248, s[60:63], s97 offen offset:3072 sc1\n"
    "s_add_u32 s97, s100, 8192\n"
    "buffer_load_dwordx4 v[204:207], v248, s[60:63], s97 offen offset:0 sc1\n"
    "buffer_load_dwordx4 v[208:211], v248, s[60:63], s97 offen offset:1024 sc1\n"
    "buffer_load_dwordx4 v[212:215], v248, s[60:63], s97 offen offset:2048 sc1\n"
    "buffer_load_dwordx4 v[216:219], v248, s[60:63], s97 offen offset:3072 sc1\n"
    "s_add_u32 s97, s100, 12288\n"
    "buffer_load_dwordx4 v[220:223], v248, s[60:63], s97 offen offset:0 sc1\n"
    "buffer_load_dwordx4 v[224:227], v248, s[60:63], s97 offen offset:1024 sc1\n"
    "buffer_load_dwordx4 v[228:231], v248, s[60:63], s97 offen offset:2048 sc1\n"
    "buffer_load_dwordx4 v[232:235], v248, s[60:63], s97 offen offset:3072 sc1\n"
    "s_waitcnt vmcnt(0)\n"
    "v_add_u32 v160, v172, v173\n"
    "v_add3_u32 v160, v160, v174, v175\n"
    "v_add3_u32 v160, v160, v176, v177\n"
    "v_add3_u32 v160, v160, v178, v179\n"
    "v_add3_u32 v160, v160, v180, v181\n"
    "v_add3_u32 v160, v160, v182, v183\n"
    "v_add3_u32 v160, v160, v184, v185\n"
    "v_add3_u32 v160, v160, v186, v187\n"
    "v_add3_u32 v160, v160, v188, v189\n"
    "v_add3_u32 v160, v160, v190, v191\n"
    "v_add3_u32 v160, v160, v192, v193\n"
    "v_add3_u32 v160, v160, v194, v195\n"
    "v_add3_u32 v160, v160, v196, v197\n"
    "v_add3_u32 v160, v160, v198, v199\n"
    "v_add3_u32 v160, v160, v200, v201\n"
    "v_add3_u32 v160, v160, v202, v203\n"
    "v_add3_u32 v160, v160, v204, v205\n"
    "v_add3_u32 v160, v160, v206, v207\n"
    "v_add3_u32 v160, v160, v208, v209\n"
    "v_add3_u32 v160, v160, v210, v211\n"
    "v_add3_u32 v160, v160, v212, v213\n"
    "v_add3_u32 v160, v160, v214, v215\n"
    "v_add3_u32 v160, v160, v216, v217\n"
    "v_add3_u32 v160, v160, v218, v219\n"
    "v_add3_u32 v160, v160, v220, v221\n"
    "v_add3_u32 v160, v160, v222, v223\n"
    "v_add3_u32 v160, v160, v224, v225\n"
    "v_add3_u32 v160, v160, v226, v227\n"
    "v_add3_u32 v160, v160, v228, v229\n"
    "v_add3_u32 v160, v160, v230, v231\n"
    "v_add3_u32 v160, v160, v232, v233\n"
    "v_add3_u32 v160, v160, v234, v235\n"
    "v_cmp_ne_u32 vcc, s70, v160\n"
    "s_nop 1\n"
    "s_cbranch_vccz LokA1\n"
    "s_sub_u32 s71, s71, 1\n"
    "s_cbranch_scc0 LlpA1\n"
    "s_branch LokA1\n"
    "LreA2:\n"
    "s_mov_b32 s71, 1000000\n"
    "LlpA2:\n"
    "s_add_u32 s97, s90, 0\n"
    "buffer_load_dwordx4 v[112:115], v248, s[60:63], s97 offen offset:512 sc1\n"
    "buffer_load_dwordx4 v[116:119], v248, s[60:63], s97 offen offset:1536 sc1\n"
    "buffer_load_dwordx4 v[120:123], v248, s[60:63], s97 offen offset:2560 sc1\n"
    "buffer_load_dwordx4 v[124:127], v248, s[60:63], s97 offen offset:3584 sc1\n"
    "buffer_load_dword v151, v144, s[60:63], s97 offen offset:0 sc1\n"
    "s_waitcnt vmcnt(0)\n"
    "v_add3_u32 v154, v112, v113, s72\n"
    "v_add3_u32 v154, v154, v114, v115\n"
    "v_add3_u32 v154, v154, v116, v117\n"
    "v_add3_u32 v154, v154, v118, v119\n"
    "v_add3_u32 v154, v154, v120, v121\n"
    "v_add3_u32 v154, v154, v122, v123\n"
    "v_add3_u32 v154, v154, v124, v125\n"
    "v_add3_u32 v154, v154, v126, v127\n"
    "v_cmp_ne_u32 vcc, v154, v151\n"
    "s_nop 1\n"
    "s_cbranch_vccz LokA2\n"
    "s_sub_u32 s71, s71, 1\n"
    "s_cbranch_scc0 LlpA2\n"
    "s_branch LokA2\n"
    "LreA3:\n"
    "s_mov_b32 s71, 1000000\n"
    "LlpA3:\n"
    "s_add_u32 s97, s88, 9216\n"
    "buffer_load_dwordx4 v[128:131], v248, s[60:63], s97 offen offset:512 sc1\n"
    "buffer_load_dwordx4 v[132:135], v248, s[60:63], s97 offen offset:1536 sc1\n"
    "buffer_load_dword v151, v144, s[60:63], s97 offen offset:0 sc1\n"
    "s_add_u32 s97, s88, 9216\n"
    "buffer_load_dwordx4 v[136:139], v248, s[60:63], s97 offen offset:2560 sc1\n"
    "buffer_load_dwordx4 v[140:143], v248, s[60:63], s97 offen offset:3584 sc1\n"
    "buffer_load_dword v152, v144, s[60:63], s97 offen offset:256 sc1\n"
    "s_waitcnt vmcnt(0)\n"
    "v_add3_u32 v154, v128, v129, s68\n"
    "v_add3_u32 v154, v154, v130, v131\n"
    "v_add3_u32 v154, v154, v132, v133\n"
    "v_add3_u32 v154, v154, v134, v135\n"
    "v_add3_u32 v154, v154, v136, v137\n"
    "v_add3_u32 v154, v154, v138, v139\n"
    "v_add3_u32 v154, v154, v140, v141\n"
    "v_add3_u32 v154, v154, v142, v143\n"
    "v_add_u32 v151, v151, v152\n"
    "v_cmp_ne_u32 vcc, v154, v151\n"
    "s_nop 1\n"
    "s_cbranch_vccz LokA3\n"
    "s_sub_u32 s71, s71, 1\n"
    "s_cbranch_scc0 LlpA3\n"
    "s_branch LokA3\n"
    "LfsA4:\n"
    "s_mov_b32 s71, 1000000\n"
    "LflA4:\n"
    "s_sleep 4\n"
    "v_and_b32 v245, 0x7c, v144\n"
    "buffer_load_dword v245, v245, s[60:63], s0 offen sc1\n"
    "s_waitcnt vmcnt(0)\n"
    "v_cmp_eq_u32 vcc, s72, v245\n"
    "s_nop 0\n"
    "s_andn2_b32 s5, 0x30, vcc_lo\n"
    "s_cbranch_scc0 LfdA4\n"
    "s_sub_u32 s71, s71, 1\n"
    "s_cbranch_scc0 LflA4\n"
    "LfdA4:\n"
    "s_sleep 8\n"
    "s_branch LfoA4\n"
    "LreA5:\n"
    "s_mov_b32 s71, 1000000\n"
    "LlpA5:\n"
    "s_add_u32 s97, s90, 13824\n"
    "buffer_load_dwordx4 v[80:83], v248, s[60:63], s97 offen offset:512 sc1\n"
    "buffer_load_dwordx4 v[84:87], v248, s[60:63], s97 offen offset:1536 sc1\n"
    "buffer_load_dwordx4 v[88:91], v248, s[60:63], s97 offen offset:2560 sc1\n"
    "buffer_load_dwordx4 v[92:95], v248, s[60:63], s97 offen offset:3584 sc1\n"
    "buffer_load_dword v151, v144, s[60:63], s97 offen offset:0 sc1\n"
    "s_waitcnt vmcnt(0)\n"
    "v_add3_u32 v154, v80, v81, s72\n"
    "v_add3_u32 v154, v154, v82, v83\n"
    "v_add3_u32 v154, v154, v84, v85\n"
    "v_add3_u32 v154, v154, v86, v87\n"
    "v_add3_u32 v154, v154, v88, v89\n"
    "v_add3_u32 v154, v154, v90, v91\n"
    "v_add3_u32 v154, v154, v92, v93\n"
    "v_add3_u32 v154, v154, v94, v95\n"
    "v_cmp_ne_u32 vcc, v154, v151\n"
    "s_nop 1\n"
    "s_cbranch_vccz LokA5\n"
    "s_sub_u32 s71, s71, 1\n"
    "s_cbranch_scc0 LlpA5\n"
    "s_branch LokA5\n"
    "LreA6:\n"
    "s_mov_b32 s71, 1000000\n"
    "LlpA6:\n"
    "s_add_u32 s97, s88, 32256\n"
    "buffer_load_dwordx4 v[188:191], v248, s[60:63], s97 offen offset:512 sc1\n"
    "buffer_load_dwordx4 v[192:195], v248, s[60:63], s97 offen offset:1536 sc1\n"
    "buffer_load_dwordx4 v[196:199], v248, s[60:63], s97 offen offset:2560 sc1\n"
    "buffer_load_dwordx4 v[200:203], v248, s[60:63], s97 offen offset:3584 sc1\n"
    "buffer_load_dword v20, v144, s[60:63], s97 offen offset:0 sc1\n"
    "s_add_u32 s97, s88, 36864\n"
    "buffer_load_dwordx4 v[204:207], v248, s[60:63], s97 offen offset:512 sc1\n"
    "buffer_load_dwordx4 v[208:211], v248, s[60:63], s97 offen offset:1536 sc1\n"
    "buffer_load_dwordx4 v[212:215], v248, s[60:63], s97 offen offset:2560 sc1\n"
    "buffer_load_dwordx4 v[216:219], v248, s[60:63], s97 offen offset:3584 sc1\n"
    "buffer_load_dword v21, v144, s[60:63], s97 offen offset:0 sc1\n"
    "s_waitcnt vmcnt(0)\n"
    "v_add3_u32 v23, v188, v189, s68\n"
    "v_add3_u32 v23, v23, v190, v191\n"
    "v_add3_u32 v23, v23, v192, v193\n"
    "v_add3_u32 v23, v23, v194, v195\n"
    "v_add3_u32 v23, v23, v196, v197\n"
    "v_add3_u32 v23, v23, v198, v199\n"
    "v_add3_u32 v23, v23, v200, v201\n"
    "v_add3_u32 v23, v23, v202, v203\n"
    "v_add3_u32 v23, v23, v204, v205\n"
    "v_add3_u32 v23, v23, v206, v207\n"
    "v_add3_u32 v23, v23, v208, v209\n"
    "v_add3_u32 v23, v23, v210, v211\n"
    "v_add3_u32 v23, v23, v212, v213\n"
    "v_add3_u32 v23, v23, v214, v215\n"
    "v_add3_u32 v23, v23, v216, v217\n"
    "v_add3_u32 v23, v23, v218, v219\n"
    "v_add_u32 v20, v20, v21\n"
    "v_cmp_ne_u32 vcc, v23, v20\n"
    "s_nop 1\n"
    "s_cbranch_vccz LokA6\n"
    "s_sub_u32 s71, s71, 1\n"
    "s_cbranch_scc0 LlpA6\n"
    "s_branch LokA6\n"
    "LreA7:\n"
    "s_mov_b32 s71, 1000000\n"
    "LlpA7:\n"
    "s_add_u32 s97, s90, 23040\n"
    "buffer_load_dwordx4 v[172:175], v248, s[60:63], s97 offen offset:512 sc1\n"
    "buffer_load_dwordx4 v[176:179], v248, s[60:63], s97 offen offset:1536 sc1\n"
    "buffer_load_dwordx4 v[180:183], v248, s[60:63], s97 offen offset:2560 sc1\n"
    "buffer_load_dwordx4 v[184:187], v248, s[60:63], s97 offen offset:3584 sc1\n"
    "buffer_load_dword v22, v144, s[60:63], s97 offen offset:0 sc1\n"
    "s_waitcnt vmcnt(0)\n"
    "v_add3_u32 v31, v172, v173, s72\n"
    "v_add3_u32 v31, v31, v174, v175\n"
    "v_add3_u32 v31, v31, v176, v177\n"
    "v_add3_u32 v31, v31, v178, v179\n"
    "v_add3_u32 v31, v31, v180, v181\n"
    "v_add3_u32 v31, v31, v182, v183\n"
    "v_add3_u32 v31, v31, v184, v185\n"
    "v_add3_u32 v31, v31, v186, v187\n"
    "v_cmp_ne_u32 vcc, v31, v22\n"
    "s_nop 1\n"
    "s_cbranch_vccz LokA7\n"
    "s_sub_u32 s71, s71, 1\n"
    "s_cbranch_scc0 LlpA7\n"
    "s_branch LokA7\n"
    "LfsA8:\n"
    "s_mov_b32 s71, 1000000\n"
    "LflA8:\n"
    "s_sleep 4\n"
    "v_and_b32 v13, 0x7c, v144\n"
    "buffer_load_dword v13, v13, s[60:63], s2 offen sc1\n"
    "s_waitcnt vmcnt(0)\n"
    "v_cmp_eq_u32 vcc, s72, v13\n"
    "s_nop 0\n"
    "s_andn2_b32 s5, 0xe, vcc_lo\n"
    "s_cbranch_scc0 LfdA8\n"
    "s_sub_u32 s71, s71, 1\n"
    "s_cbranch_scc0 LflA8\n"
    "LfdA8:\n"
    "s_branch LfoA8\n"
    "LreA9:\n"
    "s_mov_b32 s71, 1000000\n"
    "LlpA9:\n"
    "s_add_u32 s97, s94, 0\n"
    "buffer_load_dwordx4 v[164:167], v248, s[60:63], s97 offen offset:256 sc1\n"
    "buffer_load_dwordx4 v[168:171], v248, s[60:63], s97 offen offset:1280 sc1\n"
    "buffer_load_dword v28, v144, s[60:63], s97 offen offset:0 sc1\n"
    "s_waitcnt vmcnt(0)\n"
    "v_add3_u32 v13, v164, v165, s72\n"
    "v_add3_u32 v13, v13, v166, v167\n"
    "v_add3_u32 v13, v13, v168, v169\n"
    "v_add3_u32 v13, v13, v170, v171\n"
    "v_cmp_ne_u32 vcc, v13, v28\n"
    "s_nop 1\n"
    "s_cbranch_vccz LokA9\n"
    "s_sub_u32 s71, s71, 1\n"
    "s_cbranch_scc0 LlpA9\n"
    "s_branch LokA9\n"
    "LreA10:\n"
    "s_mov_b32 s71, 1000000\n"
    "LlpA10:\n"
    "s_add_u32 s97, s94, 9216\n"
    "buffer_load_dwordx4 v[228:231], v248, s[60:63], s97 offen offset:256 sc1\n"
    "buffer_load_dwordx4 v[232:235], v248, s[60:63], s97 offen offset:1280 sc1\n"
    "buffer_load_dword v29, v144, s[60:63], s97 offen offset:0 sc1\n"
    "s_waitcnt vmcnt(0)\n"
    "v_add3_u32 v14, v228, v229, s72\n"
    "v_add3_u32 v14, v14, v230, v231\n"
    "v_add3_u32 v14, v14, v232, v233\n"
    "v_add3_u32 v14, v14, v234, v235\n"
    "v_cmp_ne_u32 vcc, v14, v29\n"
    "s_nop 1\n"
    "s_cbranch_vccz LokA10\n"
    "s_sub_u32 s71, s71, 1\n"
    "s_cbranch_scc0 LlpA10\n"
    "s_branch LokA10\n"
    "LreA11:\n"
    "s_mov_b32 s71, 1000000\n"
    "LlpA11:\n"
    "s_add_u32 s97, s94, 18432\n"
    "buffer_load_dwordx4 v[40:43], v248, s[60:63], s97 offen offset:256 sc1\n"
    "buffer_load_dwordx4 v[44:47], v248, s[60:63], s97 offen offset:1280 sc1\n"
    "buffer_load_dword v30, v144, s[60:63], s97 offen offset:0 sc1\n"
    "s_waitcnt vmcnt(0)\n"
    "v_add3_u32 v15, v40, v41, s72\n"
    "v_add3_u32 v15, v15, v42, v43\n"
    "v_add3_u32 v15, v15, v44, v45\n"
    "v_add3_u32 v15, v15, v46, v47\n"
    "v_cmp_ne_u32 vcc, v15, v30\n"
    "s_nop 1\n"
    "s_cbranch_vccz LokA11\n"
    "s_sub_u32 s71, s71, 1\n"
    "s_cbranch_scc0 LlpA11\n"
    "s_branch LokA11\n"
    "LendA:\n"
    "LtypeB:\n"
    "v_mbcnt_lo_u32_b32 v96, -1, 0\n"
    "v_mbcnt_hi_u32_b32 v96, -1, v96\n"
    "s_mov_b32 s74, %2\n"
    "s_mov_b32 s20, %1\n"
    "s_and_b32 s14, s20, 1\n"
    "s_lshl_b32 s14, s14, 3\n"
    "s_bfe_u32 s15, s20, 0x30003\n"
    "s_add_u32 s21, s14, s15\n"
    "s_bfe_u32 s22, s20, 0x20001\n"
    "s_lshr_b32 s23, s20, 6\n"
    "s_lshr_b32 s24, s20, 7\n"
    "s_lshl2_add_u32 s14, s21, s74\n"
    "s_lshl_b32 s84, s14, 15\n"
    "s_lshl_b32 s75, s74, 12\n"
    "s_lshl1_add_u32 s18, s22, s24\n"
    "s_getpc_b64 s[12:13]\n"
    "LpcB:\n"
    "s_lshl2_add_u32 s14, s15, s74\n"
    "s_lshl_b32 s14, s14, 10\n"
    "v_and_b32 v228, 7, v96\n"
    "v_lshl_add_u32 v228, v228, 7, s14\n"
    "v_min_u32 v228, LendB-LpcB-4, v228\n"
    "global_load_dword v228, v228, s[12:13]\n"
    "v_lshrrev_b32 v97, 3, v96\n"
    "v_and_b32 v98, 7, v96\n"
    "v_lshrrev_b32 v99, 4, v96\n"
    "v_xor_b32 v100, v98, v99\n"
    "v_lshlrev_b32 v100, 4, v100\n"
    "v_lshl_add_u32 v172, v97, 10, v100\n"
    "v_xor_b32 v100, 64, v172\n"
    "v_add_u32 v173, 7168, v100\n"
    "v_add_u32 v174, 14336, v172\n"
    "v_add_u32 v175, 14336, v173\n"
    "v_and_b32 v101, 1, v99\n"
    "v_lshlrev_b32 v101, 2, v101\n"
    "v_xor_b32 v101, v98, v101\n"
    "v_lshlrev_b32 v101, 4, v101\n"
    "v_mov_b32 v102, 3072\n"
    "v_mad_u32_u24 v244, v97, v102, v101\n"
    "v_add_u32 v245, 23552, v244\n"
    "v_lshlrev_b32 v248, 4, v96\n"
    "s_mul_i32 s85, s74, 49152\n"
    "s_lshl_b32 s19, s18, 7\n"
    "s_add_u32 s85, s85, s19\n"
    "s_add_u32 s85, s85, 1024\n"
    "s_lshl_b32 s16, s74, 10\n"
    "s_add_u32 s16, s16, 147456\n"
    "s_waitcnt lgkmcnt(0)\n"
    "s_add_u32 m0, s75, 0\n"
    "s_nop 0\n"
    "buffer_load_dwordx4 v172, s[36:39], s84 offen offset:0 lds\n"
    "buffer_load_dwordx4 v173, s[36:39], s84 offen offset:1024 lds\n"
    "buffer_load_dwordx4 v174, s[36:39], s84 offen offset:2048 lds\n"
    "buffer_load_dwordx4 v175, s[36:39], s84 offen offset:3072 lds\n"
    "s_load_dwordx2 s[72:73], s[60:61], 0\n"
    "s_add_u32 m0, s75, 16384\n"
    "s_nop 0\n"
    "buffer_load_dwordx4 v244, s[48:51], s85 offen offset:0 lds\n"
    "buffer_load_dwordx4 v245, s[48:51], s85 offen offset:1024 lds\n"
    "s_sub_u32 s14, s85, 1024\n"
    "buffer_load_dwordx4 v244, s[48:51], s14 offen offset:2048 lds\n"
    "buffer_load_dwordx4 v245, s[48:51], s14 offen offset:3072 lds\n"
    "s_mov_b32 m0, s16\n"
    "s_nop 0\n"
    "buffer_load_dwordx4 v248, s[68:71], 0 offen lds\n"
    "s_add_u32 m0, s16, 3072\n"
    "s_nop 0\n"
    "buffer_load_dwordx4 v248, s[68:71], 0 offen offset:1024 lds\n"
    "v_and_b32 v104, 31, v96\n"
    "v_lshrrev_b32 v105, 5, v96\n"
    "v_bfe_u32 v106, v96, 1, 3\n"
    "v_xor_b32 v106, v106, v105\n"
    "v_lshlrev_b32 v106, 4, v106\n"
    "v_lshl_add_u32 v106, v104, 7, v106\n"
    "v_add_u32 v176, s75, v106\n"
    "v_xor_b32 v177, 32, v176\n"
    "v_xor_b32 v178, 64, v176\n"
    "v_xor_b32 v179, 96, v176\n"
    "v_bfe_u32 v107, v96, 2, 2\n"
    "v_lshl_add_u32 v108, v105, 3, v107\n"
    "v_bfe_u32 v109, v96, 4, 1\n"
    "v_bfe_u32 v110, v96, 1, 1\n"
    "v_lshl_add_u32 v109, v109, 1, v110\n"
    "v_bfe_u32 v110, v96, 3, 1\n"
    "v_lshl_or_b32 v110, v110, 2, v109\n"
    "v_lshlrev_b32 v110, 4, v110\n"
    "v_and_b32 v109, 1, v96\n"
    "v_lshl_or_b32 v110, v109, 3, v110\n"
    "v_lshl_add_u32 v236, v108, 7, v110\n"
    "v_xor_b32 v237, 64, v236\n"
    "v_lshlrev_b32 v111, 4, v105\n"
    "v_add_u32 v188, s16, v111\n"
    "v_lshrrev_b32 v109, 2, v96\n"
    "v_xor_b32 v109, v109, v99\n"
    "v_and_b32 v109, 1, v109\n"
    "v_and_or_b32 v109, v96, 3, v109\n"
    "v_cmp_eq_u32 vcc, 0, v109\n"
    "v_mov_b32 v109, 151616\n"
    "s_nop 0\n"
    "v_cndmask_b32 v188, v109, v188, vcc\n"
    "v_mov_b32 v204, 0x358637bd\n"
    "v_mov_b32 v205, 0x358637bd\n"
    "v_mov_b32 v206, 0x358637bd\n"
    "v_mov_b32 v207, 0x358637bd\n"
    "v_mov_b32 v208, 0x358637bd\n"
    "v_mov_b32 v209, 0x358637bd\n"
    "v_mov_b32 v210, 0x358637bd\n"
    "v_mov_b32 v211, 0x358637bd\n"
    "v_mov_b32 v212, 0x358637bd\n"
    "v_mov_b32 v213, 0x358637bd\n"
    "v_mov_b32 v214, 0x358637bd\n"
    "v_mov_b32 v215, 0x358637bd\n"
    "v_mov_b32 v216, 0x358637bd\n"
    "v_mov_b32 v217, 0x358637bd\n"
    "v_mov_b32 v218, 0x358637bd\n"
    "v_mov_b32 v219, 0x358637bd\n"
    "s_add_u32 m0, s75, 32768\n"
    "s_add_u32 s15, s84, 128\n"
    "buffer_load_dwordx4 v172, s[36:39], s15 offen offset:0 lds\n"
    "buffer_load_dwordx4 v173, s[36:39], s15 offen offset:1024 lds\n"
    "buffer_load_dwordx4 v174, s[36:39], s15 offen offset:2048 lds\n"
    "buffer_load_dwordx4 v175, s[36:39], s15 offen offset:3072 lds\n"
    "s_add_u32 m0, s75, 49152\n"
    "s_add_u32 s12, s85, 196608\n"
    "buffer_load_dwordx4 v244, s[48:51], s12 offen offset:0 lds\n"
    "buffer_load_dwordx4 v245, s[48:51], s12 offen offset:1024 lds\n"
    "s_sub_u32 s13, s12, 1024\n"
    "buffer_load_dwordx4 v244, s[48:51], s13 offen offset:2048 lds\n"
    "buffer_load_dwordx4 v245, s[48:51], s13 offen offset:3072 lds\n"
    "s_lshr_b32 s12, s20, 3\n"
    "s_and_b32 s13, s20, 1\n"
    "s_bitcmp1_b32 s74, 0\n"
    "s_cselect_b64 s[80:81], s[44:45], s[40:41]\n"
    "s_mov_b64 s[82:83], s[42:43]\n"
    "s_lshr_b32 s14, s74, 1\n"
    "s_lshl1_add_u32 s14, s12, s14\n"
    "s_lshl_b32 s14, s14, 3\n"
    "s_add_u32 s14, s14, s13\n"
    "v_and_b32 v229, 3, v96\n"
    "v_lshl_add_u32 v229, v229, 1, s14\n"
    "s_lshr_b32 s15, s48, 7\n"
    "s_add_u32 s15, s15, s18\n"
    "s_and_b32 s15, s15, 7\n"
    "s_cmp_eq_u32 s15, 3\n"
    "s_cselect_b32 s7, -1.0, 0\n"
    "s_lshr_b32 s15, s48, 7\n"
    "s_xor_b32 s6, s18, 1\n"
    "s_add_u32 s15, s15, s6\n"
    "s_and_b32 s15, s15, 7\n"
    "s_cmp_eq_u32 s15, 3\n"
    "s_cselect_b32 s6, 256, 0\n"
    "v_mul_u32_u24 v222, s6, v105\n"
    "v_xor_b32 v229, v229, v222\n"
    "v_bfe_u32 v231, v96, 2, 3\n"
    "v_min_u32 v231, 4, v231\n"
    "s_lshl2_add_u32 s14, s22, 1\n"
    "s_mul_i32 s15, s22, 3\n"
    "s_add_u32 s15, s15, 14\n"
    "v_lshl_add_u32 v232, v231, 1, s14\n"
    "v_cmp_gt_u32 vcc, 2, v231\n"
    "v_add_u32 v231, s15, v231\n"
    "s_nop 0\n"
    "v_cndmask_b32 v231, v231, v232, vcc\n"
    "v_mul_u32_u24 v229, 28, v229\n"
    "v_add_lshl_u32 v229, v229, v231, 7\n"
    "v_or_b32 v229, s7, v229\n"
    "v_and_b32 v231, 3, v96\n"
    "s_lshl_b32 s14, s13, 2\n"
    "v_add_u32 v231, s14, v231\n"
    "v_lshrrev_b32 v232, 2, v96\n"
    "s_cmp_eq_u32 s74, 2\n"
    "s_cbranch_scc1 LtqB\n"
    "s_cmp_eq_u32 s74, 3\n"
    "s_cbranch_scc1 LtoB\n"
    "s_mov_b64 s[76:77], s[52:53]\n"
    "s_mov_b64 s[78:79], s[54:55]\n"
    "s_lshl1_add_u32 s14, s12, s74\n"
    "s_mul_i32 s14, s14, 7\n"
    "v_min_u32 v232, 6, v232\n"
    "v_add_u32 v232, s14, v232\n"
    "s_lshl_b32 s14, s22, 8\n"
    "s_mul_i32 s15, s22, 192\n"
    "s_add_u32 s15, s15, 768\n"
    "v_cmp_gt_u32 vcc, 0x100, v232\n"
    "v_add_u32 v230, s14, v232\n"
    "v_add_u32 v232, s15, v232\n"
    "v_cndmask_b32 v232, v232, v230, vcc\n"
    "v_lshlrev_b32 v232, 3, v232\n"
    "s_branch LtdB\n"
    "LtqB:\n"
    "s_mov_b64 s[76:77], s[48:49]\n"
    "s_mov_b64 s[78:79], s[50:51]\n"
    "s_lshl_b32 s14, s12, 4\n"
    "s_lshl_b32 s15, s22, 1\n"
    "s_add_u32 s14, s14, s13\n"
    "v_and_b32 v232, 14, v96\n"
    "v_and_b32 v231, 1, v96\n"
    "v_add_u32 v232, s14, v232\n"
    "v_add_u32 v231, s15, v231\n"
    "s_lshr_b32 s14, s48, 7\n"
    "s_sub_u32 s14, 3, s14\n"
    "s_and_b32 s14, s14, 7\n"
    "v_cmp_eq_u32 vcc, s14, v231\n"
    "s_nop 0\n"
    "s_and_b32 vcc_lo, vcc_lo, 0xffff0000\n"
    "s_mov_b32 vcc_hi, 0\n"
    "v_cndmask_b32 v230, 0, 1, vcc\n"
    "v_xor_b32 v232, v232, v230\n"
    "v_mul_u32_u24 v232, 24, v232\n"
    "s_branch LtdB\n"
    "LtoB:\n"
    "s_mov_b64 s[76:77], s[56:57]\n"
    "s_mov_b64 s[78:79], s[58:59]\n"
    "s_lshl_b32 s14, s22, 5\n"
    "s_add_u32 s14, s14, s12\n"
    "s_lshl_b32 s14, s14, 2\n"
    "v_and_b32 v232, 3, v232\n"
    "v_add_lshl_u32 v232, s14, v232, 3\n"
    "LtdB:\n"
    "v_add_lshl_u32 v230, v232, v231, 7\n"
    "v_lshlrev_b32 v233, 7, v98\n"
    "v_lshl_add_u32 v233, v97, 10, v233\n"
    "s_lshl_b32 s14, s23, 13\n"
    "s_add_u32 s14, s14, s84\n"
    "s_lshl_b32 s15, s22, 11\n"
    "s_add_u32 s14, s14, s15\n"
    "v_add_u32 v233, s14, v233\n"
    "v_mov_b32 v235, -1.0\n"
    "v_cmp_le_u32 vcc, 2, v98\n"
    "s_nop 1\n"
    "v_cndmask_b32 v233, v235, v233, vcc\n"
    "v_cmp_gt_u32 vcc, 16, v96\n"
    "s_nop 1\n"
    "v_cndmask_b32 v233, v235, v233, vcc\n"
    "s_and_b32 s14, s21, 7\n"
    "s_lshr_b32 s15, s74, 1\n"
    "s_lshl1_add_u32 s14, s14, s15\n"
    "s_and_b32 s15, s20, 1\n"
    "s_lshl1_add_u32 s14, s14, s15\n"
    "s_mul_i32 s14, s14, 36864\n"
    "s_lshl_b32 s15, s18, 7\n"
    "s_add_u32 s14, s14, s15\n"
    "s_add_u32 s14, s14, 394240\n"
    "s_and_b32 s15, s74, 1\n"
    "s_lshl_b32 s15, s15, 10\n"
    "s_add_u32 s14, s14, s15\n"
    "v_mov_b32 v234, 3072\n"
    "v_mad_u32_u24 v234, v96, v234, s14\n"
    "s_lshr_b32 s14, s48, 7\n"
    "s_add_u32 s14, s14, s18\n"
    "s_and_b32 s14, s14, 7\n"
    "s_cmp_eq_u32 s14, 3\n"
    "s_cselect_b32 s14, 0, 12\n"
    "v_cmp_gt_u32 vcc, s14, v96\n"
    "s_nop 1\n"
    "v_cndmask_b32 v234, v235, v234, vcc\n"
    "buffer_load_dword v234, v234, s[48:51], 0 offen sc1\n"
    "buffer_load_dword v233, v233, s[36:39], 0 offen sc1\n"
    "s_waitcnt vmcnt(10)\n"
    "s_barrier\n"
    "ds_read_b128 a[0:3], v176 offset:0\n"
    "ds_read_b64_tr_b16 a[128:129], v236 offset:16384\n"
    "ds_read_b64_tr_b16 a[130:131], v236 offset:16896\n"
    "ds_read_b64_tr_b16 a[132:133], v237 offset:16384\n"
    "ds_read_b64_tr_b16 a[134:135], v237 offset:16896\n"
    "ds_read_b64_tr_b16 a[136:137], v236 offset:18432\n"
    "ds_read_b64_tr_b16 a[138:139], v236 offset:18944\n"
    "ds_read_b64_tr_b16 a[140:141], v237 offset:18432\n"
    "ds_read_b64_tr_b16 a[142:143], v237 offset:18944\n"
    "ds_read_b128 a[176:179], v188 offset:0\n"
    "ds_read_b128 a[4:7], v177 offset:0\n"
    "ds_read_b64_tr_b16 a[144:145], v236 offset:20480\n"
    "ds_read_b64_tr_b16 a[146:147], v236 offset:20992\n"
    "ds_read_b64_tr_b16 a[148:149], v237 offset:20480\n"
    "ds_read_b64_tr_b16 a[150:151], v237 offset:20992\n"
    "ds_read_b64_tr_b16 a[152:153], v236 offset:22528\n"
    "ds_read_b64_tr_b16 a[154:155], v236 offset:23040\n"
    "ds_read_b64_tr_b16 a[156:157], v237 offset:22528\n"
    "ds_read_b64_tr_b16 a[158:159], v237 offset:23040\n"
    "ds_read_b128 a[180:183], v188 offset:32\n"
    "s_waitcnt lgkmcnt(10)\n"
    "v_mfma_f32_32x32x16_bf16 v[0:15], a[0:3], a[128:131], v[204:219]\n"
    "ds_read_b128 a[8:11], v178 offset:0\n"
    "ds_read_b64_tr_b16 a[160:161], v236 offset:24576\n"
    "s_add_u32 m0, s75, 65536\n"
    "s_add_u32 s14, s84, 256\n"
    "buffer_load_dwordx4 v172, s[36:39], s14 offen offset:0 lds\n"
    "v_mfma_f32_32x32x16_bf16 v[16:31], a[0:3], a[132:135], v[204:219]\n"
    "ds_read_b64_tr_b16 a[162:163], v236 offset:25088\n"
    "ds_read_b64_tr_b16 a[164:165], v237 offset:24576\n"
    "ds_read_b64_tr_b16 a[166:167], v237 offset:25088\n"
    "buffer_load_dwordx4 v173, s[36:39], s14 offen offset:1024 lds\n"
    "v_mfma_f32_32x32x16_bf16 v[32:47], a[0:3], a[136:139], 0\n"
    "ds_read_b64_tr_b16 a[168:169], v236 offset:26624\n"
    "ds_read_b64_tr_b16 a[170:171], v236 offset:27136\n"
    "buffer_load_dwordx4 v174, s[36:39], s14 offen offset:2048 lds\n"
    "buffer_load_dwordx4 v175, s[36:39], s14 offen offset:3072 lds\n"
    "v_mfma_f32_32x32x16_bf16 v[48:63], a[0:3], a[140:143], 0\n"
    "ds_read_b64_tr_b16 a[172:173], v237 offset:26624\n"
    "ds_read_b64_tr_b16 a[174:175], v237 offset:27136\n"
    "ds_read_b128 a[184:187], v188 offset:64\n"
    "s_add_u32 m0, s75, 81920\n"
    "s_add_u32 s15, s85, 393216\n"
    "buffer_load_dwordx4 v244, s[48:51], s15 offen offset:0 lds\n"
    "v_mfma_f32_16x16x32_bf16 v[156:159], a[176:179], a[0:3], 0\n"
    "buffer_load_dwordx4 v245, s[48:51], s15 offen offset:1024 lds\n"
    "s_waitcnt lgkmcnt(10)\n"
    "v_mfma_f32_32x32x16_bf16 v[0:15], a[4:7], a[144:147], v[0:15]\n"
    "ds_read_b128 a[12:15], v179 offset:0\n"
    "ds_read_b64_tr_b16 a[128:129], v236 offset:28672\n"
    "s_sub_u32 s12, s15, 1024\n"
    "buffer_load_dwordx4 v244, s[48:51], s12 offen offset:2048 lds\n"
    "v_mfma_f32_32x32x16_bf16 v[16:31], a[4:7], a[148:151], v[16:31]\n"
    "ds_read_b64_tr_b16 a[130:131], v236 offset:29184\n"
    "ds_read_b64_tr_b16 a[132:133], v237 offset:28672\n"
    "ds_read_b64_tr_b16 a[134:135], v237 offset:29184\n"
    "buffer_load_dwordx4 v245, s[48:51], s12 offen offset:3072 lds\n"
    "v_mfma_f32_32x32x16_bf16 v[32:47], a[4:7], a[152:155], v[32:47]\n"
    "ds_read_b64_tr_b16 a[136:137], v236 offset:30720\n"
    "ds_read_b64_tr_b16 a[138:139], v236 offset:31232\n"
    "v_add_u32 v180, 0x10000, v176\n"
    "v_add_u32 v181, 0x10000, v177\n"
    "v_add_u32 v182, 0x10000, v178\n"
    "v_add_u32 v183, 0x10000, v179\n"
    "v_mfma_f32_32x32x16_bf16 v[48:63], a[4:7], a[156:159], v[48:63]\n"
    "ds_read_b64_tr_b16 a[140:141], v237 offset:30720\n"
    "ds_read_b64_tr_b16 a[142:143], v237 offset:31232\n"
    "ds_read_b128 a[176:179], v188 offset:96\n"
    "v_add_u32 v238, 0x10000, v236\n"
    "v_add_u32 v239, 0x10000, v237\n"
    "v_add_u32 v184, 0x20000, v176\n"
    "v_mfma_f32_16x16x32_bf16 v[156:159], a[180:183], a[4:7], v[156:159]\n"
    "v_add_u32 v185, 0x20000, v177\n"
    "v_add_u32 v186, 0x20000, v178\n"
    "s_waitcnt vmcnt(10) lgkmcnt(10)\n"
    "s_barrier\n"
    "v_mfma_f32_32x32x16_bf16 v[0:15], a[8:11], a[160:163], v[0:15]\n"
    "ds_read_b128 a[16:19], v176 offset:32768\n"
    "ds_read_b64_tr_b16 a[144:145], v236 offset:49152\n"
    "s_add_u32 m0, s75, 98304\n"
    "s_add_u32 s13, s84, 384\n"
    "buffer_load_dwordx4 v172, s[36:39], s13 offen offset:0 lds\n"
    "v_mfma_f32_32x32x16_bf16 v[16:31], a[8:11], a[164:167], v[16:31]\n"
    "ds_read_b64_tr_b16 a[146:147], v236 offset:49664\n"
    "ds_read_b64_tr_b16 a[148:149], v237 offset:49152\n"
    "ds_read_b64_tr_b16 a[150:151], v237 offset:49664\n"
    "buffer_load_dwordx4 v173, s[36:39], s13 offen offset:1024 lds\n"
    "v_mfma_f32_32x32x16_bf16 v[32:47], a[8:11], a[168:171], v[32:47]\n"
    "ds_read_b64_tr_b16 a[152:153], v236 offset:51200\n"
    "ds_read_b64_tr_b16 a[154:155], v236 offset:51712\n"
    "buffer_load_dwordx4 v174, s[36:39], s13 offen offset:2048 lds\n"
    "buffer_load_dwordx4 v175, s[36:39], s13 offen offset:3072 lds\n"
    "v_mfma_f32_32x32x16_bf16 v[48:63], a[8:11], a[172:175], v[48:63]\n"
    "ds_read_b64_tr_b16 a[156:157], v237 offset:51200\n"
    "ds_read_b64_tr_b16 a[158:159], v237 offset:51712\n"
    "ds_read_b128 a[180:183], v188 offset:128\n"
    "s_add_u32 m0, s75, 114688\n"
    "s_add_u32 s14, s85, 589824\n"
    "buffer_load_dwordx4 v244, s[48:51], s14 offen offset:0 lds\n"
    "v_mfma_f32_16x16x32_bf16 v[156:159], a[184:187], a[8:11], v[156:159]\n"
    "buffer_load_dwordx4 v245, s[48:51], s14 offen offset:1024 lds\n"
    "s_waitcnt lgkmcnt(10)\n"
    "v_mfma_f32_32x32x16_bf16 v[0:15], a[12:15], a[128:131], v[0:15]\n"
    "ds_read_b128 a[20:23], v177 offset:32768\n"
    "ds_read_b64_tr_b16 a[160:161], v236 offset:53248\n"
    "s_sub_u32 s15, s14, 1024\n"
    "buffer_load_dwordx4 v244, s[48:51], s15 offen offset:2048 lds\n"
    "v_mfma_f32_32x32x16_bf16 v[16:31], a[12:15], a[132:135], v[16:31]\n"
    "ds_read_b64_tr_b16 a[162:163], v236 offset:53760\n"
    "ds_read_b64_tr_b16 a[164:165], v237 offset:53248\n"
    "ds_read_b64_tr_b16 a[166:167], v237 offset:53760\n"
    "buffer_load_dwordx4 v245, s[48:51], s15 offen offset:3072 lds\n"
    "v_mfma_f32_32x32x16_bf16 v[32:47], a[12:15], a[136:139], v[32:47]\n"
    "ds_read_b64_tr_b16 a[168:169], v236 offset:55296\n"
    "ds_read_b64_tr_b16 a[170:171], v236 offset:55808\n"
    "v_add_u32 v187, 0x20000, v179\n"
    "v_add_u32 v240, 0x20000, v236\n"
    "v_add_u32 v241, 0x20000, v237\n"
    "v_mov_b32 v102, 3584\n"
    "v_mfma_f32_32x32x16_bf16 v[48:63], a[12:15], a[140:143], v[48:63]\n"
    "ds_read_b64_tr_b16 a[172:173], v237 offset:55296\n"
    "ds_read_b64_tr_b16 a[174:175], v237 offset:55808\n"
    "ds_read_b128 a[184:187], v188 offset:160\n"
    "v_mad_u32_u24 v242, v97, v102, v101\n"
    "v_add_u32 v243, 27648, v242\n"
    "s_waitcnt lgkmcnt(0)\n"
    "s_add_u32 s72, s72, 0x7f4a7c15\n"
    "s_addc_u32 s73, s73, 0x9e3779b9\n"
    "v_mfma_f32_16x16x32_bf16 v[156:159], a[176:179], a[12:15], v[156:159]\n"
    "s_lshl3_add_u32 s16, s20, 4096\n"
    "s_cmp_eq_u32 s74, 0\n"
    "s_cselect_b32 s16, s16, -1.0\n"
    "v_mfma_f32_32x32x16_bf16 v[0:15], a[16:19], a[144:147], v[0:15]\n"
    "ds_read_b128 a[24:27], v178 offset:32768\n"
    "ds_read_b64_tr_b16 a[128:129], v236 offset:57344\n"
    "v_mov_b32 v112, -1.0\n"
    "v_mov_b32 v114, s72\n"
    "v_writelane_b32 v112, s16, 0\n"
    "v_mov_b32 v115, s73\n"
    "v_mfma_f32_32x32x16_bf16 v[16:31], a[16:19], a[148:151], v[16:31]\n"
    "ds_read_b64_tr_b16 a[130:131], v236 offset:57856\n"
    "ds_read_b64_tr_b16 a[132:133], v237 offset:57344\n"
    "ds_read_b64_tr_b16 a[134:135], v237 offset:57856\n"
    "s_lshl_b32 s68, s72, 1\n"
    "buffer_store_dwordx2 v[114:115], v112, s[60:63], 0 offen sc1\n"
    "v_mfma_f32_32x32x16_bf16 v[32:47], a[16:19], a[152:155], v[32:47]\n"
    "ds_read_b64_tr_b16 a[136:137], v236 offset:59392\n"
    "ds_read_b64_tr_b16 a[138:139], v236 offset:59904\n"
    "s_add_u32 s69, s68, s72\n"
    "s_lshl_b32 s70, s72, 4\n"
    "s_mov_b32 s98, 0x0000ffff\n"
    "s_mov_b32 s99, 0x0000ffff\n"
    "v_mfma_f32_32x32x16_bf16 v[48:63], a[16:19], a[156:159], v[48:63]\n"
    "ds_read_b64_tr_b16 a[140:141], v237 offset:59392\n"
    "ds_read_b64_tr_b16 a[142:143], v237 offset:59904\n"
    "ds_read_b128 a[176:179], v188 offset:192\n"
    "s_add_u32 m0, s75, 131072\n"
    "s_add_u32 s12, s84, 512\n"
    "buffer_load_dwordx4 v172, s[36:39], s12 offen offset:0 lds\n"
    "v_mfma_f32_16x16x32_bf16 v[156:159], a[180:183], a[16:19], v[156:159]\n"
    "s_lshr_b32 s16, s74, 1\n"
    "s_and_b32 s17, s74, 1\n"
    "v_mfma_f32_32x32x16_bf16 v[0:15], a[20:23], a[160:163], v[0:15]\n"
    "ds_read_b128 a[28:31], v179 offset:32768\n"
    "ds_read_b64_tr_b16 a[144:145], v236 offset:61440\n"
    "s_mul_i32 s86, s17, 114688\n"
    "s_mul_i32 s16, s16, 229376\n"
    "s_add_u32 s86, s86, s16\n"
    "s_mul_i32 s97, s22, 384\n"
    "v_mfma_f32_32x32x16_bf16 v[16:31], a[20:23], a[164:167], v[16:31]\n"
    "ds_read_b64_tr_b16 a[146:147], v236 offset:61952\n"
    "ds_read_b64_tr_b16 a[148:149], v237 offset:61440\n"
    "ds_read_b64_tr_b16 a[150:151], v237 offset:61952\n"
    "buffer_load_dwordx4 v173, s[36:39], s12 offen offset:1024 lds\n"
    "v_mfma_f32_32x32x16_bf16 v[32:47], a[20:23], a[168:171], v[32:47]\n"
    "ds_read_b64_tr_b16 a[152:153], v236 offset:63488\n"
    "ds_read_b64_tr_b16 a[154:155], v236 offset:64000\n"
    "s_add_u32 s86, s86, s97\n"
    "s_lshl_b32 s16, s24, 6\n"
    "s_add_u32 s86, s86, s16\n"
    "s_add_u32 s86, s86, 2304\n"
    "v_mfma_f32_32x32x16_bf16 v[48:63], a[20:23], a[172:175], v[48:63]\n"
    "ds_read_b64_tr_b16 a[156:157], v237 offset:63488\n"
    "ds_read_b64_tr_b16 a[158:159], v237 offset:64000\n"
    "ds_read_b128 a[180:183], v188 offset:224\n"
    "buffer_load_dwordx4 v174, s[36:39], s12 offen offset:2048 lds\n"
    "v_mfma_f32_16x16x32_bf16 v[156:159], a[184:187], a[20:23], v[156:159]\n"
    "s_mul_i32 s87, s74, 57344\n"
    "s_add_u32 s87, s87, s97\n"
    "s_waitcnt vmcnt(12) lgkmcnt(10)\n"
    "s_barrier\n"
    "v_mfma_f32_32x32x16_bf16 v[0:15], a[24:27], a[128:131], v[0:15]\n"
    "ds_read_b128 a[32:35], v180 offset:0\n"
    "ds_read_b64_tr_b16 a[160:161], v238 offset:16384\n"
    "buffer_load_dwordx4 v175, s[36:39], s12 offen offset:3072 lds\n"
    "s_add_u32 m0, s75, 0\n"
    "v_mfma_f32_32x32x16_bf16 v[16:31], a[24:27], a[132:135], v[16:31]\n"
    "ds_read_b64_tr_b16 a[162:163], v238 offset:16896\n"
    "ds_read_b64_tr_b16 a[164:165], v239 offset:16384\n"
    "ds_read_b64_tr_b16 a[166:167], v239 offset:16896\n"
    "s_add_u32 s13, s85, 786432\n"
    "buffer_load_dwordx4 v244, s[48:51], s13 offen offset:0 lds\n"
    "v_mfma_f32_32x32x16_bf16 v[32:47], a[24:27], a[136:139], v[32:47]\n"
    "ds_read_b64_tr_b16 a[168:169], v238 offset:18432\n"
    "ds_read_b64_tr_b16 a[170:171], v238 offset:18944\n"
    "buffer_load_dwordx4 v245, s[48:51], s13 offen offset:1024 lds\n"
    "s_sub_u32 s14, s13, 1024\n"
    "v_mfma_f32_32x32x16_bf16 v[48:63], a[24:27], a[140:143], v[48:63]\n"
    "ds_read_b64_tr_b16 a[172:173], v239 offset:18432\n"
    "ds_read_b64_tr_b16 a[174:175], v239 offset:18944\n"
    "ds_read_b128 a[184:187], v188 offset:256\n"
    "buffer_load_dwordx4 v244, s[48:51], s14 offen offset:2048 lds\n"
    "v_mfma_f32_16x16x32_bf16 v[156:159], a[176:179], a[24:27], v[156:159]\n"
    "buffer_load_dwordx4 v245, s[48:51], s14 offen offset:3072 lds\n"
    "s_waitcnt lgkmcnt(10)\n"
    "v_mfma_f32_32x32x16_bf16 v[0:15], a[28:31], a[144:147], v[0:15]\n"
    "ds_read_b128 a[36:39], v181 offset:0\n"
    "ds_read_b64_tr_b16 a[128:129], v238 offset:20480\n"
    "s_lshl_b32 s16, s24, 7\n"
    "s_add_u32 s87, s87, s16\n"
    "s_add_u32 s87, s87, 2048\n"
    "v_lshrrev_b32 v112, 2, v96\n"
    "v_mfma_f32_32x32x16_bf16 v[16:31], a[28:31], a[148:151], v[16:31]\n"
    "ds_read_b64_tr_b16 a[130:131], v238 offset:20992\n"
    "ds_read_b64_tr_b16 a[132:133], v239 offset:20480\n"
    "ds_read_b64_tr_b16 a[134:135], v239 offset:20992\n"
    "v_and_b32 v113, 3, v96\n"
    "v_mov_b32 v114, 3584\n"
    "v_lshlrev_b32 v115, 4, v113\n"
    "v_mfma_f32_32x32x16_bf16 v[32:47], a[28:31], a[152:155], v[32:47]\n"
    "ds_read_b64_tr_b16 a[136:137], v238 offset:22528\n"
    "ds_read_b64_tr_b16 a[138:139], v238 offset:23040\n"
    "v_mad_u32_u24 v251, v112, v114, v115\n"
    "v_bfe_u32 v115, v96, 4, 1\n"
    "v_lshlrev_b32 v115, 5, v115\n"
    "v_lshl_or_b32 v115, v113, 3, v115\n"
    "v_mfma_f32_32x32x16_bf16 v[48:63], a[28:31], a[156:159], v[48:63]\n"
    "ds_read_b64_tr_b16 a[140:141], v239 offset:22528\n"
    "ds_read_b64_tr_b16 a[142:143], v239 offset:23040\n"
    "ds_read_b128 a[176:179], v188 offset:288\n"
    "v_lshl_add_u32 v252, v108, 6, v115\n"
    "v_add_u32 v253, 0x10000, v252\n"
    "v_add_u32 v254, 0x20000, v252\n"
    "v_mfma_f32_16x16x32_bf16 v[156:159], a[180:183], a[28:31], v[156:159]\n"
    "v_lshlrev_b32 v144, 2, v96\n"
    "v_mov_b32 v155, 0xb5c1a2a0\n"
    "s_waitcnt lgkmcnt(10)\n"
    "v_mfma_f32_32x32x16_bf16 v[0:15], a[32:35], a[160:163], v[0:15]\n"
    "ds_read_b128 a[40:43], v182 offset:0\n"
    "ds_read_b64_tr_b16 a[144:145], v238 offset:24576\n"
    "buffer_load_dword v229, v229, s[80:83], 0 offen sc1\n"
    "s_mov_b32 s96, 0x358637bd\n"
    "s_mov_b32 s4, 0x3fb8aa3b\n"
    "v_mfma_f32_32x32x16_bf16 v[16:31], a[32:35], a[164:167], v[16:31]\n"
    "ds_read_b64_tr_b16 a[146:147], v238 offset:25088\n"
    "ds_read_b64_tr_b16 a[148:149], v239 offset:24576\n"
    "ds_read_b64_tr_b16 a[150:151], v239 offset:25088\n"
    "v_lshl_add_u32 v246, v97, 10, v101\n"
    "v_add_u32 v247, 7168, v246\n"
    "s_lshl2_add_u32 s16, s21, s22\n"
    "v_mfma_f32_32x32x16_bf16 v[32:47], a[32:35], a[168:171], v[32:47]\n"
    "ds_read_b64_tr_b16 a[152:153], v238 offset:26624\n"
    "ds_read_b64_tr_b16 a[154:155], v238 offset:27136\n"
    "s_lshl2_add_u32 s16, s16, s74\n"
    "s_lshl_b32 s0, s16, 7\n"
    "s_add_u32 s0, s0, 589824\n"
    "s_lshl2_add_u32 s1, s24, s0\n"
    "v_mfma_f32_32x32x16_bf16 v[48:63], a[32:35], a[172:175], v[48:63]\n"
    "ds_read_b64_tr_b16 a[156:157], v239 offset:26624\n"
    "ds_read_b64_tr_b16 a[158:159], v239 offset:27136\n"
    "ds_read_b128 a[180:183], v188 offset:320\n"
    "s_lshl2_add_u32 s2, s20, s74\n"
    "s_lshl_b32 s2, s2, 7\n"
    "s_add_u32 s2, s2, 655360\n"
    "v_mfma_f32_16x16x32_bf16 v[156:159], a[184:187], a[32:35], v[156:159]\n"
    "s_mul_i32 s88, s16, 41472\n"
    "s_add_u32 s88, s88, 1048576\n"
    "s_waitcnt lgkmcnt(10)\n"
    "v_mfma_f32_32x32x16_bf16 v[0:15], a[36:39], a[128:131], v[0:15]\n"
    "ds_read_b128 a[44:47], v183 offset:0\n"
    "ds_read_b64_tr_b16 a[160:161], v238 offset:28672\n"
    "s_mul_i32 s16, s24, 4608\n"
    "s_add_u32 s89, s88, s16\n"
    "s_sub_u32 s90, s88, s16\n"
    "s_add_u32 s90, s90, 4608\n"
    "v_mfma_f32_32x32x16_bf16 v[16:31], a[36:39], a[132:135], v[16:31]\n"
    "ds_read_b64_tr_b16 a[162:163], v238 offset:29184\n"
    "ds_read_b64_tr_b16 a[164:165], v239 offset:28672\n"
    "ds_read_b64_tr_b16 a[166:167], v239 offset:29184\n"
    "s_add_u32 m0, s75, 16384\n"
    "s_add_u32 s15, s84, 640\n"
    "buffer_load_dwordx4 v172, s[36:39], s15 offen offset:0 lds\n"
    "v_mfma_f32_32x32x16_bf16 v[32:47], a[36:39], a[136:139], v[32:47]\n"
    "ds_read_b64_tr_b16 a[168:169], v238 offset:30720\n"
    "ds_read_b64_tr_b16 a[170:171], v238 offset:31232\n"
    "s_lshl2_add_u32 s16, s18, s74\n"
    "s_lshl_b32 s100, s16, 14\n"
    "s_add_u32 s100, s100, 65536\n"
    "s_lshl_b32 s3, s16, 7\n"
    "v_mfma_f32_32x32x16_bf16 v[48:63], a[36:39], a[140:143], v[48:63]\n"
    "ds_read_b64_tr_b16 a[172:173], v239 offset:30720\n"
    "ds_read_b64_tr_b16 a[174:175], v239 offset:31232\n"
    "ds_read_b128 a[184:187], v188 offset:352\n"
    "buffer_load_dwordx4 v173, s[36:39], s15 offen offset:1024 lds\n"
    "v_mfma_f32_16x16x32_bf16 v[156:159], a[176:179], a[36:39], v[156:159]\n"
    "s_add_u32 s3, s3, 622592\n"
    "s_lshl2_add_u32 s3, s21, s3\n"
    "s_waitcnt vmcnt(12) lgkmcnt(10)\n"
    "s_barrier\n"
    "v_mfma_f32_32x32x16_bf16 v[0:15], a[40:43], a[144:147], v[0:15]\n"
    "ds_read_b128 a[48:51], v180 offset:32768\n"
    "ds_read_b64_tr_b16 a[128:129], v238 offset:49152\n"
    "buffer_load_dwordx4 v174, s[36:39], s15 offen offset:2048 lds\n"
    "buffer_load_dwordx4 v175, s[36:39], s15 offen offset:3072 lds\n"
    "v_mfma_f32_32x32x16_bf16 v[16:31], a[40:43], a[148:151], v[16:31]\n"
    "ds_read_b64_tr_b16 a[130:131], v238 offset:49664\n"
    "ds_read_b64_tr_b16 a[132:133], v239 offset:49152\n"
    "ds_read_b64_tr_b16 a[134:135], v239 offset:49664\n"
    "s_add_u32 m0, s75, 32768\n"
    "s_add_u32 s12, s85, 983040\n"
    "buffer_load_dwordx4 v244, s[48:51], s12 offen offset:0 lds\n"
    "v_mfma_f32_32x32x16_bf16 v[32:47], a[40:43], a[152:155], v[32:47]\n"
    "ds_read_b64_tr_b16 a[136:137], v238 offset:51200\n"
    "ds_read_b64_tr_b16 a[138:139], v238 offset:51712\n"
    "buffer_load_dwordx4 v245, s[48:51], s12 offen offset:1024 lds\n"
    "s_sub_u32 s13, s12, 1024\n"
    "v_mfma_f32_32x32x16_bf16 v[48:63], a[40:43], a[156:159], v[48:63]\n"
    "ds_read_b64_tr_b16 a[140:141], v239 offset:51200\n"
    "ds_read_b64_tr_b16 a[142:143], v239 offset:51712\n"
    "ds_read_b128 a[176:179], v188 offset:384\n"
    "buffer_load_dwordx4 v244, s[48:51], s13 offen offset:2048 lds\n"
    "v_mfma_f32_16x16x32_bf16 v[156:159], a[180:183], a[40:43], v[156:159]\n"
    "buffer_load_dwordx4 v245, s[48:51], s13 offen offset:3072 lds\n"
    "s_waitcnt lgkmcnt(10)\n"
    "v_mfma_f32_32x32x16_bf16 v[0:15], a[44:47], a[160:163], v[0:15]\n"
    "ds_read_b128 a[52:55], v181 offset:32768\n"
    "ds_read_b64_tr_b16 a[144:145], v238 offset:53248\n"
    "s_lshl_b32 s16, s21, 10\n"
    "s_add_u32 s100, s100, s16\n"
    "s_lshl_b32 s16, s74, 14\n"
    "s_lshl_b32 s17, s23, 8\n"
    "v_mfma_f32_32x32x16_bf16 v[16:31], a[44:47], a[164:167], v[16:31]\n"
    "ds_read_b64_tr_b16 a[146:147], v238 offset:53760\n"
    "ds_read_b64_tr_b16 a[148:149], v239 offset:53248\n"
    "ds_read_b64_tr_b16 a[150:151], v239 offset:53760\n"
    "s_add_u32 s16, s16, s17\n"
    "s_lshr_b32 s19, s22, 1\n"
    "s_lshl_b32 s19, s19, 7\n"
    "v_mfma_f32_32x32x16_bf16 v[32:47], a[44:47], a[168:171], v[32:47]\n"
    "ds_read_b64_tr_b16 a[152:153], v238 offset:55296\n"
    "ds_read_b64_tr_b16 a[154:155], v238 offset:55808\n"
    "s_add_u32 s16, s16, s19\n"
    "s_lshl_b32 s19, s22, 18\n"
    "s_add_u32 s25, s16, s19\n"
    "s_lshl_b32 s19, s22, 17\n"
    "v_mfma_f32_32x32x16_bf16 v[48:63], a[44:47], a[172:175], v[48:63]\n"
    "ds_read_b64_tr_b16 a[156:157], v239 offset:55296\n"
    "ds_read_b64_tr_b16 a[158:159], v239 offset:55808\n"
    "ds_read_b128 a[180:183], v188 offset:416\n"
    "s_add_u32 s27, s16, s19\n"
    "s_mul_i32 s19, s22, 196608\n"
    "s_add_u32 s26, s16, s19\n"
    "v_mfma_f32_16x16x32_bf16 v[156:159], a[184:187], a[44:47], v[156:159]\n"
    "s_add_u32 s26, s26, 1048576\n"
    "s_lshl_b32 s19, s22, 6\n"
    "s_waitcnt lgkmcnt(10)\n"
    "v_mfma_f32_32x32x16_bf16 v[0:15], a[48:51], a[128:131], v[0:15]\n"
    "ds_read_b128 a[56:59], v182 offset:32768\n"
    "ds_read_b64_tr_b16 a[160:161], v238 offset:57344\n"
    "buffer_load_dword v141, off, s[60:63], s0 sc1\n"
    "buffer_load_dword v142, off, s[60:63], s2 sc1\n"
    "v_mfma_f32_32x32x16_bf16 v[16:31], a[48:51], a[132:135], v[16:31]\n"
    "ds_read_b64_tr_b16 a[162:163], v238 offset:57856\n"
    "ds_read_b64_tr_b16 a[164:165], v239 offset:57344\n"
    "ds_read_b64_tr_b16 a[166:167], v239 offset:57856\n"
    "s_add_u32 m0, s75, 49152\n"
    "s_add_u32 s14, s84, 768\n"
    "buffer_load_dwordx4 v172, s[36:39], s14 offen offset:0 lds\n"
    "v_mfma_f32_32x32x16_bf16 v[32:47], a[48:51], a[136:139], v[32:47]\n"
    "ds_read_b64_tr_b16 a[168:169], v238 offset:59392\n"
    "ds_read_b64_tr_b16 a[170:171], v238 offset:59904\n"
    "s_add_u32 s101, s84, s17\n"
    "s_add_u32 s101, s101, s19\n"
    "s_add_u32 s95, s101, 16384\n"
    "s_lshl2_add_u32 s16, s21, s23\n"
    "v_mfma_f32_32x32x16_bf16 v[48:63], a[48:51], a[140:143], v[48:63]\n"
    "ds_read_b64_tr_b16 a[172:173], v239 offset:59392\n"
    "ds_read_b64_tr_b16 a[174:175], v239 offset:59904\n"
    "ds_read_b128 a[184:187], v188 offset:448\n"
    "buffer_load_dwordx4 v173, s[36:39], s14 offen offset:1024 lds\n"
    "v_mfma_f32_16x16x32_bf16 v[156:159], a[176:179], a[48:51], v[156:159]\n"
    "s_mul_i32 s16, s16, 48\n"
    "s_add_u32 s16, s16, s74\n"
    "s_waitcnt lgkmcnt(10)\n"
    "v_mfma_f32_32x32x16_bf16 v[0:15], a[52:55], a[144:147], v[0:15]\n"
    "ds_read_b128 a[60:63], v183 offset:32768\n"
    "ds_read_b64_tr_b16 a[128:129], v238 offset:61440\n"
    "s_mul_i32 s16, s16, 2304\n"
    "s_add_u32 s16, s16, 12582912\n"
    "s_mul_i32 s17, s22, 27648\n"
    "s_add_u32 s94, s16, s17\n"
    "v_mfma_f32_32x32x16_bf16 v[16:31], a[52:55], a[148:151], v[16:31]\n"
    "ds_read_b64_tr_b16 a[130:131], v238 offset:61952\n"
    "ds_read_b64_tr_b16 a[132:133], v239 offset:61440\n"
    "ds_read_b64_tr_b16 a[134:135], v239 offset:61952\n"
    "buffer_load_dwordx4 v174, s[36:39], s14 offen offset:2048 lds\n"
    "v_mfma_f32_32x32x16_bf16 v[32:47], a[52:55], a[152:155], v[32:47]\n"
    "ds_read_b64_tr_b16 a[136:137], v238 offset:63488\n"
    "ds_read_b64_tr_b16 a[138:139], v238 offset:64000\n"
    "s_xor_b32 s17, s22, 1\n"
    "s_mul_i32 s17, s17, 27648\n"
    "s_add_u32 s17, s17, s16\n"
    "s_add_u32 s91, s17, 0\n"
    "v_mfma_f32_32x32x16_bf16 v[48:63], a[52:55], a[156:159], v[48:63]\n"
    "ds_read_b64_tr_b16 a[140:141], v239 offset:63488\n"
    "ds_read_b64_tr_b16 a[142:143], v239 offset:64000\n"
    "ds_read_b128 a[176:179], v188 offset:480\n"
    "buffer_load_dwordx4 v175, s[36:39], s14 offen offset:3072 lds\n"
    "v_mfma_f32_16x16x32_bf16 v[156:159], a[180:183], a[52:55], v[156:159]\n"
    "s_xor_b32 s17, s22, 2\n"
    "s_mul_i32 s17, s17, 27648\n"
    "s_waitcnt vmcnt(15) lgkmcnt(10)\n"
    "s_barrier\n"
    "v_mfma_f32_32x32x16_bf16 v[0:15], a[56:59], a[160:163], v[0:15]\n"
    "ds_read_b128 a[64:67], v184 offset:0\n"
    "ds_read_b64_tr_b16 a[144:145], v236 offset:0\n"
    "s_add_u32 m0, s75, 65536\n"
    "s_add_u32 s15, s85, 1179648\n"
    "buffer_load_dwordx4 v244, s[48:51], s15 offen offset:0 lds\n"
    "v_mfma_f32_32x32x16_bf16 v[16:31], a[56:59], a[164:167], v[16:31]\n"
    "ds_read_b64_tr_b16 a[146:147], v236 offset:512\n"
    "ds_read_b64_tr_b16 a[148:149], v237 offset:0\n"
    "ds_read_b64_tr_b16 a[150:151], v237 offset:512\n"
    "buffer_load_dwordx4 v245, s[48:51], s15 offen offset:1024 lds\n"
    "v_mfma_f32_32x32x16_bf16 v[32:47], a[56:59], a[168:171], v[32:47]\n"
    "ds_read_b64_tr_b16 a[152:153], v236 offset:2048\n"
    "ds_read_b64_tr_b16 a[154:155], v236 offset:2560\n"
    "s_sub_u32 s12, s15, 1024\n"
    "buffer_load_dwordx4 v244, s[48:51], s12 offen offset:2048 lds\n"
    "v_mfma_f32_32x32x16_bf16 v[48:63], a[56:59], a[172:175], v[48:63]\n"
    "ds_read_b64_tr_b16 a[156:157], v237 offset:2048\n"
    "ds_read_b64_tr_b16 a[158:159], v237 offset:2560\n"
    "ds_read_b128 a[180:183], v188 offset:512\n"
    "buffer_load_dwordx4 v245, s[48:51], s12 offen offset:3072 lds\n"
    "v_mfma_f32_16x16x32_bf16 v[156:159], a[184:187], a[56:59], v[156:159]\n"
    "s_add_u32 s17, s17, s16\n"
    "s_add_u32 s92, s17, 9216\n"
    "s_waitcnt lgkmcnt(10)\n"
    "v_mfma_f32_32x32x16_bf16 v[0:15], a[60:63], a[128:131], v[0:15]\n"
    "ds_read_b128 a[68:71], v185 offset:0\n"
    "ds_read_b64_tr_b16 a[160:161], v236 offset:4096\n"
    "s_xor_b32 s17, s22, 3\n"
    "s_mul_i32 s17, s17, 27648\n"
    "s_add_u32 s17, s17, s16\n"
    "s_add_u32 s93, s17, 18432\n"
    "v_mfma_f32_32x32x16_bf16 v[16:31], a[60:63], a[132:135], v[16:31]\n"
    "ds_read_b64_tr_b16 a[162:163], v236 offset:4608\n"
    "ds_read_b64_tr_b16 a[164:165], v237 offset:4096\n"
    "ds_read_b64_tr_b16 a[166:167], v237 offset:4608\n"
    "v_lshl_add_u32 v112, v105, 2, v107\n"
    "v_and_b32 v113, 1, v96\n"
    "v_bfe_u32 v114, v96, 4, 1\n"
    "v_mfma_f32_32x32x16_bf16 v[32:47], a[60:63], a[136:139], v[32:47]\n"
    "ds_read_b64_tr_b16 a[168:169], v236 offset:6144\n"
    "ds_read_b64_tr_b16 a[170:171], v236 offset:6656\n"
    "v_lshl_add_u32 v113, v113, 1, v114\n"
    "v_bfe_u32 v114, v96, 3, 1\n"
    "v_lshl_or_b32 v113, v114, 2, v113\n"
    "v_lshlrev_b32 v113, 4, v113\n"
    "v_mfma_f32_32x32x16_bf16 v[48:63], a[60:63], a[140:143], v[48:63]\n"
    "ds_read_b64_tr_b16 a[172:173], v237 offset:6144\n"
    "ds_read_b64_tr_b16 a[174:175], v237 offset:6656\n"
    "ds_read_b128 a[184:187], v188 offset:544\n"
    "v_bfe_u32 v114, v96, 1, 1\n"
    "v_lshl_or_b32 v113, v114, 3, v113\n"
    "v_lshl_add_u32 v112, v112, 7, v113\n"
    "v_mfma_f32_16x16x32_bf16 v[156:159], a[176:179], a[60:63], v[156:159]\n"
    "s_add_u32 m0, s75, 81920\n"
    "s_add_u32 s13, s84, 896\n"
    "s_waitcnt lgkmcnt(10)\n"
    "v_mfma_f32_32x32x16_bf16 v[0:15], a[64:67], a[144:147], v[0:15]\n"
    "ds_read_b128 a[72:75], v186 offset:0\n"
    "ds_read_b64_tr_b16 a[128:129], v236 offset:8192\n"
    "buffer_load_dword v230, v230, s[76:79], 0 offen sc1\n"
    "s_and_b32 s16, s22, 1\n"
    "s_lshl_b32 s16, s16, 6\n"
    "v_mfma_f32_32x32x16_bf16 v[16:31], a[64:67], a[148:151], v[16:31]\n"
    "ds_read_b64_tr_b16 a[130:131], v236 offset:8704\n"
    "ds_read_b64_tr_b16 a[132:133], v237 offset:8192\n"
    "ds_read_b64_tr_b16 a[134:135], v237 offset:8704\n"
    "v_xor_b32 v145, s16, v112\n"
    "v_xor_b32 v146, 64, v145\n"
    "v_add_u32 v147, 0x10000, v145\n"
    "v_mfma_f32_32x32x16_bf16 v[32:47], a[64:67], a[152:155], v[32:47]\n"
    "ds_read_b64_tr_b16 a[136:137], v236 offset:10240\n"
    "ds_read_b64_tr_b16 a[138:139], v236 offset:10752\n"
    "v_add_u32 v148, 0x10000, v146\n"
    "v_add_u32 v149, 0x20000, v145\n"
    "v_add_u32 v150, 0x20000, v146\n"
    "v_bfe_u32 v112, v96, 2, 1\n"
    "v_mfma_f32_32x32x16_bf16 v[48:63], a[64:67], a[156:159], v[48:63]\n"
    "ds_read_b64_tr_b16 a[140:141], v237 offset:10240\n"
    "ds_read_b64_tr_b16 a[142:143], v237 offset:10752\n"
    "ds_read_b128 a[176:179], v188 offset:576\n"
    "v_and_b32 v113, 1, v96\n"
    "v_lshlrev_b32 v113, 4, v113\n"
    "v_mov_b32 v114, 0x3f80\n"
    "v_mfma_f32_16x16x32_bf16 v[156:159], a[180:183], a[64:67], v[156:159]\n"
    "v_lshlrev_b32 v113, v113, v114\n"
    "v_xor_b32 v112, v112, v105\n"
    "s_waitcnt lgkmcnt(10)\n"
    "v_mfma_f32_32x32x16_bf16 v[0:15], a[68:71], a[160:163], v[0:15]\n"
    "ds_read_b128 a[76:79], v187 offset:0\n"
    "ds_read_b64_tr_b16 a[144:145], v236 offset:12288\n"
    "v_bfe_u32 v114, v96, 3, 2\n"
    "v_bfe_u32 v115, v96, 1, 1\n"
    "v_lshl_or_b32 v114, v114, 1, v115\n"
    "v_lshl_or_b32 v114, v112, 3, v114\n"
    "v_mfma_f32_32x32x16_bf16 v[16:31], a[68:71], a[164:167], v[16:31]\n"
    "ds_read_b64_tr_b16 a[146:147], v236 offset:12800\n"
    "ds_read_b64_tr_b16 a[148:149], v237 offset:12288\n"
    "ds_read_b64_tr_b16 a[150:151], v237 offset:12800\n"
    "v_cmp_eq_u32 vcc, 0, v114\n"
    "s_nop 1\n"
    "v_cndmask_b32 v116, 0, v113, vcc\n"
    "v_mfma_f32_32x32x16_bf16 v[32:47], a[68:71], a[168:171], v[32:47]\n"
    "ds_read_b64_tr_b16 a[152:153], v236 offset:14336\n"
    "ds_read_b64_tr_b16 a[154:155], v236 offset:14848\n"
    "buffer_load_dwordx4 v172, s[36:39], s13 offen offset:0 lds\n"
    "v_cmp_eq_u32 vcc, 1, v114\n"
    "s_nop 1\n"
    "v_cndmask_b32 v117, 0, v113, vcc\n"
    "v_mfma_f32_32x32x16_bf16 v[48:63], a[68:71], a[172:175], v[48:63]\n"
    "ds_read_b64_tr_b16 a[156:157], v237 offset:14336\n"
    "ds_read_b64_tr_b16 a[158:159], v237 offset:14848\n"
    "ds_read_b128 a[180:183], v188 offset:608\n"
    "v_cmp_eq_u32 vcc, 2, v114\n"
    "s_nop 1\n"
    "v_cndmask_b32 v118, 0, v113, vcc\n"
    "v_mfma_f32_16x16x32_bf16 v[156:159], a[184:187], a[68:71], v[156:159]\n"
    "buffer_load_dwordx4 v173, s[36:39], s13 offen offset:1024 lds\n"
    "s_waitcnt vmcnt(13) lgkmcnt(10)\n"
    "s_barrier\n"
    "v_mfma_f32_32x32x16_bf16 v[0:15], a[72:75], a[128:131], v[0:15]\n"
    "ds_read_b128 a[80:83], v176 offset:16384\n"
    "ds_read_b64_tr_b16 a[160:161], v236 offset:32768\n"
    "buffer_load_dwordx4 v174, s[36:39], s13 offen offset:2048 lds\n"
    "buffer_load_dwordx4 v175, s[36:39], s13 offen offset:3072 lds\n"
    "v_mfma_f32_32x32x16_bf16 v[16:31], a[72:75], a[132:135], v[16:31]\n"
    "ds_read_b64_tr_b16 a[162:163], v236 offset:33280\n"
    "ds_read_b64_tr_b16 a[164:165], v237 offset:32768\n"
    "ds_read_b64_tr_b16 a[166:167], v237 offset:33280\n"
    "s_add_u32 m0, s75, 98304\n"
    "s_add_u32 s14, s85, 1376256\n"
    "buffer_load_dwordx4 v244, s[48:51], s14 offen offset:0 lds\n"
    "v_mfma_f32_32x32x16_bf16 v[32:47], a[72:75], a[136:139], v[32:47]\n"
    "ds_read_b64_tr_b16 a[168:169], v236 offset:34816\n"
    "ds_read_b64_tr_b16 a[170:171], v236 offset:35328\n"
    "buffer_load_dwordx4 v245, s[48:51], s14 offen offset:1024 lds\n"
    "s_sub_u32 s15, s14, 1024\n"
    "v_mfma_f32_32x32x16_bf16 v[48:63], a[72:75], a[140:143], v[48:63]\n"
    "ds_read_b64_tr_b16 a[172:173], v237 offset:34816\n"
    "ds_read_b64_tr_b16 a[174:175], v237 offset:35328\n"
    "ds_read_b128 a[184:187], v188 offset:640\n"
    "buffer_load_dwordx4 v244, s[48:51], s15 offen offset:2048 lds\n"
    "v_mfma_f32_16x16x32_bf16 v[156:159], a[176:179], a[72:75], v[156:159]\n"
    "buffer_load_dwordx4 v245, s[48:51], s15 offen offset:3072 lds\n"
    "s_waitcnt lgkmcnt(10)\n"
    "v_mfma_f32_32x32x16_bf16 v[0:15], a[76:79], a[144:147], v[0:15]\n"
    "ds_read_b128 a[84:87], v177 offset:16384\n"
    "ds_read_b64_tr_b16 a[128:129], v236 offset:36864\n"
    "v_cmp_eq_u32 vcc, 3, v114\n"
    "s_nop 1\n"
    "v_cndmask_b32 v119, 0, v113, vcc\n"
    "v_cmp_eq_u32 vcc, 4, v114\n"
    "s_nop 1\n"
    "v_cndmask_b32 v120, 0, v113, vcc\n"
    "v_mfma_f32_32x32x16_bf16 v[16:31], a[76:79], a[148:151], v[16:31]\n"
    "ds_read_b64_tr_b16 a[130:131], v236 offset:37376\n"
    "ds_read_b64_tr_b16 a[132:133], v237 offset:36864\n"
    "ds_read_b64_tr_b16 a[134:135], v237 offset:37376\n"
    "v_cmp_eq_u32 vcc, 5, v114\n"
    "s_nop 1\n"
    "v_cndmask_b32 v121, 0, v113, vcc\n"
    "v_mfma_f32_32x32x16_bf16 v[32:47], a[76:79], a[152:155], v[32:47]\n"
    "ds_read_b64_tr_b16 a[136:137], v236 offset:38912\n"
    "ds_read_b64_tr_b16 a[138:139], v236 offset:39424\n"
    "v_cmp_eq_u32 vcc, 6, v114\n"
    "s_nop 1\n"
    "v_cndmask_b32 v122, 0, v113, vcc\n"
    "v_cmp_eq_u32 vcc, 7, v114\n"
    "s_nop 1\n"
    "v_cndmask_b32 v123, 0, v113, vcc\n"
    "v_mfma_f32_32x32x16_bf16 v[48:63], a[76:79], a[156:159], v[48:63]\n"
    "ds_read_b64_tr_b16 a[140:141], v237 offset:38912\n"
    "ds_read_b64_tr_b16 a[142:143], v237 offset:39424\n"
    "ds_read_b128 a[176:179], v188 offset:672\n"
    "v_accvgpr_write_b32 a208, v116\n"
    "v_accvgpr_write_b32 a209, v117\n"
    "v_accvgpr_write_b32 a210, v118\n"
    "v_mfma_f32_16x16x32_bf16 v[156:159], a[180:183], a[76:79], v[156:159]\n"
    "v_accvgpr_write_b32 a211, v119\n"
    "v_accvgpr_write_b32 a212, v120\n"
    "s_waitcnt lgkmcnt(10)\n"
    "v_mfma_f32_32x32x16_bf16 v[0:15], a[80:83], a[160:163], v[0:15]\n"
    "ds_read_b128 a[88:91], v178 offset:16384\n"
    "ds_read_b64_tr_b16 a[144:145], v236 offset:40960\n"
    "v_accvgpr_write_b32 a213, v121\n"
    "v_accvgpr_write_b32 a214, v122\n"
    "v_accvgpr_write_b32 a215, v123\n"
    "v_mfma_f32_32x32x16_bf16 v[16:31], a[80:83], a[164:167], v[16:31]\n"
    "ds_read_b64_tr_b16 a[146:147], v236 offset:41472\n"
    "ds_read_b64_tr_b16 a[148:149], v237 offset:40960\n"
    "ds_read_b64_tr_b16 a[150:151], v237 offset:41472\n"
    "s_add_u32 m0, s75, 114688\n"
    "s_nop 0\n"
    "buffer_load_dwordx4 v251, s[40:43], s86 offen offset:0 lds\n"
    "v_mfma_f32_32x32x16_bf16 v[32:47], a[80:83], a[168:171], v[32:47]\n"
    "ds_read_b64_tr_b16 a[152:153], v236 offset:43008\n"
    "ds_read_b64_tr_b16 a[154:155], v236 offset:43520\n"
    "v_mfma_f32_32x32x16_bf16 v[48:63], a[80:83], a[172:175], v[48:63]\n"
    "ds_read_b64_tr_b16 a[156:157], v237 offset:43008\n"
    "ds_read_b64_tr_b16 a[158:159], v237 offset:43520\n"
    "ds_read_b128 a[180:183], v188 offset:704\n"
    "v_mfma_f32_16x16x32_bf16 v[156:159], a[184:187], a[80:83], v[156:159]\n"
    "s_waitcnt lgkmcnt(10)\n"
    "v_mfma_f32_32x32x16_bf16 v[0:15], a[84:87], a[128:131], v[0:15]\n"
    "ds_read_b128 a[92:95], v179 offset:16384\n"
    "ds_read_b64_tr_b16 a[160:161], v236 offset:45056\n"
    "s_sub_u32 s13, s86, 1024\n"
    "buffer_load_dwordx4 v251, s[44:47], s13 offen offset:1024 lds\n"
    "v_mfma_f32_32x32x16_bf16 v[16:31], a[84:87], a[132:135], v[16:31]\n"
    "ds_read_b64_tr_b16 a[162:163], v236 offset:45568\n"
    "ds_read_b64_tr_b16 a[164:165], v237 offset:45056\n"
    "ds_read_b64_tr_b16 a[166:167], v237 offset:45568\n"
    "v_mfma_f32_32x32x16_bf16 v[32:47], a[84:87], a[136:139], v[32:47]\n"
    "ds_read_b64_tr_b16 a[168:169], v236 offset:47104\n"
    "ds_read_b64_tr_b16 a[170:171], v236 offset:47616\n"
    "v_mfma_f32_32x32x16_bf16 v[48:63], a[84:87], a[140:143], v[48:63]\n"
    "ds_read_b64_tr_b16 a[172:173], v237 offset:47104\n"
    "ds_read_b64_tr_b16 a[174:175], v237 offset:47616\n"
    "ds_read_b128 a[184:187], v188 offset:736\n"
    "v_mfma_f32_16x16x32_bf16 v[156:159], a[176:179], a[84:87], v[156:159]\n"
    "s_waitcnt vmcnt(11) lgkmcnt(10)\n"
    "s_barrier\n"
    "v_mfma_f32_32x32x16_bf16 v[0:15], a[88:91], a[144:147], v[0:15]\n"
    "ds_read_b128 a[96:99], v176 offset:49152\n"
    "ds_read_b64_tr_b16 a[128:129], v238 offset:0\n"
    "s_add_u32 s14, s86, 55296\n"
    "buffer_load_dwordx4 v251, s[40:43], s14 offen offset:2048 lds\n"
    "v_mfma_f32_32x32x16_bf16 v[16:31], a[88:91], a[148:151], v[16:31]\n"
    "ds_read_b64_tr_b16 a[130:131], v238 offset:512\n"
    "ds_read_b64_tr_b16 a[132:133], v239 offset:0\n"
    "ds_read_b64_tr_b16 a[134:135], v239 offset:512\n"
    "s_add_u32 s15, s86, 54272\n"
    "buffer_load_dwordx4 v251, s[44:47], s15 offen offset:3072 lds\n"
    "v_mfma_f32_32x32x16_bf16 v[32:47], a[88:91], a[152:155], v[32:47]\n"
    "ds_read_b64_tr_b16 a[136:137], v238 offset:2048\n"
    "ds_read_b64_tr_b16 a[138:139], v238 offset:2560\n"
    "v_mfma_f32_32x32x16_bf16 v[48:63], a[88:91], a[156:159], v[48:63]\n"
    "ds_read_b64_tr_b16 a[140:141], v239 offset:2048\n"
    "ds_read_b64_tr_b16 a[142:143], v239 offset:2560\n"
    "ds_read_b128 a[176:179], v188 offset:768\n"
    "v_mfma_f32_16x16x32_bf16 v[156:159], a[180:183], a[88:91], v[156:159]\n"
    "s_waitcnt lgkmcnt(10)\n"
    "v_mfma_f32_32x32x16_bf16 v[0:15], a[92:95], a[160:163], v[0:15]\n"
    "ds_read_b128 a[100:103], v177 offset:49152\n"
    "ds_read_b64_tr_b16 a[144:145], v238 offset:4096\n"
    "v_mfma_f32_32x32x16_bf16 v[16:31], a[92:95], a[164:167], v[16:31]\n"
    "ds_read_b64_tr_b16 a[146:147], v238 offset:4608\n"
    "ds_read_b64_tr_b16 a[148:149], v239 offset:4096\n"
    "ds_read_b64_tr_b16 a[150:151], v239 offset:4608\n"
    "v_mfma_f32_32x32x16_bf16 v[32:47], a[92:95], a[168:171], v[32:47]\n"
    "ds_read_b64_tr_b16 a[152:153], v238 offset:6144\n"
    "ds_read_b64_tr_b16 a[154:155], v238 offset:6656\n"
    "v_mfma_f32_32x32x16_bf16 v[48:63], a[92:95], a[172:175], v[48:63]\n"
    "ds_read_b64_tr_b16 a[156:157], v239 offset:6144\n"
    "ds_read_b64_tr_b16 a[158:159], v239 offset:6656\n"
    "ds_read_b128 a[180:183], v188 offset:800\n"
    "s_add_u32 m0, s75, 131072\n"
    "s_add_u32 s12, s86, 458752\n"
    "buffer_load_dwordx4 v251, s[40:43], s12 offen offset:0 lds\n"
    "v_mfma_f32_16x16x32_bf16 v[156:159], a[184:187], a[92:95], v[156:159]\n"
    "s_waitcnt lgkmcnt(10)\n"
    "v_mfma_f32_32x32x16_bf16 v[0:15], a[96:99], a[128:131], v[0:15]\n"
    "ds_read_b128 a[104:107], v178 offset:49152\n"
    "ds_read_b64_tr_b16 a[160:161], v238 offset:8192\n"
    "v_mfma_f32_32x32x16_bf16 v[16:31], a[96:99], a[132:135], v[16:31]\n"
    "ds_read_b64_tr_b16 a[162:163], v238 offset:8704\n"
    "ds_read_b64_tr_b16 a[164:165], v239 offset:8192\n"
    "ds_read_b64_tr_b16 a[166:167], v239 offset:8704\n"
    "v_mfma_f32_32x32x16_bf16 v[32:47], a[96:99], a[136:139], v[32:47]\n"
    "ds_read_b64_tr_b16 a[168:169], v238 offset:10240\n"
    "ds_read_b64_tr_b16 a[170:171], v238 offset:10752\n"
    "s_sub_u32 s13, s12, 1024\n"
    "buffer_load_dwordx4 v251, s[44:47], s13 offen offset:1024 lds\n"
    "v_mfma_f32_32x32x16_bf16 v[48:63], a[96:99], a[140:143], v[48:63]\n"
    "ds_read_b64_tr_b16 a[172:173], v239 offset:10240\n"
    "ds_read_b64_tr_b16 a[174:175], v239 offset:10752\n"
    "ds_read_b128 a[184:187], v188 offset:832\n"
    "v_mfma_f32_16x16x32_bf16 v[156:159], a[176:179], a[96:99], v[156:159]\n"
    "s_waitcnt lgkmcnt(10)\n"
    "v_mfma_f32_32x32x16_bf16 v[0:15], a[100:103], a[144:147], v[0:15]\n"
    "ds_read_b128 a[108:111], v179 offset:49152\n"
    "ds_read_b64_tr_b16 a[128:129], v238 offset:12288\n"
    "v_mfma_f32_32x32x16_bf16 v[16:31], a[100:103], a[148:151], v[16:31]\n"
    "ds_read_b64_tr_b16 a[130:131], v238 offset:12800\n"
    "ds_read_b64_tr_b16 a[132:133], v239 offset:12288\n"
    "ds_read_b64_tr_b16 a[134:135], v239 offset:12800\n"
    "v_mfma_f32_32x32x16_bf16 v[32:47], a[100:103], a[152:155], v[32:47]\n"
    "ds_read_b64_tr_b16 a[136:137], v238 offset:14336\n"
    "ds_read_b64_tr_b16 a[138:139], v238 offset:14848\n"
    "s_add_u32 s14, s12, 55296\n"
    "buffer_load_dwordx4 v251, s[40:43], s14 offen offset:2048 lds\n"
    "v_mfma_f32_32x32x16_bf16 v[48:63], a[100:103], a[156:159], v[48:63]\n"
    "ds_read_b64_tr_b16 a[140:141], v239 offset:14336\n"
    "ds_read_b64_tr_b16 a[142:143], v239 offset:14848\n"
    "ds_read_b128 a[176:179], v188 offset:864\n"
    "v_mfma_f32_16x16x32_bf16 v[156:159], a[180:183], a[100:103], v[156:159]\n"
    "s_waitcnt vmcnt(7) lgkmcnt(10)\n"
    "s_barrier\n"
    "v_mfma_f32_32x32x16_bf16 v[0:15], a[104:107], a[160:163], v[0:15]\n"
    "ds_read_b128 a[112:115], v180 offset:16384\n"
    "ds_read_b64_tr_b16 a[144:145], v238 offset:32768\n"
    "s_add_u32 s15, s12, 54272\n"
    "buffer_load_dwordx4 v251, s[44:47], s15 offen offset:3072 lds\n"
    "v_mfma_f32_32x32x16_bf16 v[16:31], a[104:107], a[164:167], v[16:31]\n"
    "ds_read_b64_tr_b16 a[146:147], v238 offset:33280\n"
    "ds_read_b64_tr_b16 a[148:149], v239 offset:32768\n"
    "ds_read_b64_tr_b16 a[150:151], v239 offset:33280\n"
    "v_mfma_f32_32x32x16_bf16 v[32:47], a[104:107], a[168:171], v[32:47]\n"
    "ds_read_b64_tr_b16 a[152:153], v238 offset:34816\n"
    "ds_read_b64_tr_b16 a[154:155], v238 offset:35328\n"
    "v_mfma_f32_32x32x16_bf16 v[48:63], a[104:107], a[172:175], v[48:63]\n"
    "ds_read_b64_tr_b16 a[156:157], v239 offset:34816\n"
    "ds_read_b64_tr_b16 a[158:159], v239 offset:35328\n"
    "ds_read_b128 a[180:183], v188 offset:896\n"
    "v_mfma_f32_16x16x32_bf16 v[156:159], a[184:187], a[104:107], v[156:159]\n"
    "s_waitcnt lgkmcnt(10)\n"
    "v_mfma_f32_32x32x16_bf16 v[0:15], a[108:111], a[128:131], v[0:15]\n"
    "ds_read_b128 a[116:119], v181 offset:16384\n"
    "ds_read_b64_tr_b16 a[160:161], v238 offset:36864\n"
    "v_mfma_f32_32x32x16_bf16 v[16:31], a[108:111], a[132:135], v[16:31]\n"
    "ds_read_b64_tr_b16 a[162:163], v238 offset:37376\n"
    "ds_read_b64_tr_b16 a[164:165], v239 offset:36864\n"
    "ds_read_b64_tr_b16 a[166:167], v239 offset:37376\n"
    "s_add_u32 m0, s75, 0\n"
    "s_add_u32 s12, s86, 917504\n"
    "buffer_load_dwordx4 v251, s[40:43], s12 offen offset:0 lds\n"
    "v_mfma_f32_32x32x16_bf16 v[32:47], a[108:111], a[136:139], v[32:47]\n"
    "ds_read_b64_tr_b16 a[168:169], v238 offset:38912\n"
    "ds_read_b64_tr_b16 a[170:171], v238 offset:39424\n"
    "v_mfma_f32_32x32x16_bf16 v[48:63], a[108:111], a[140:143], v[48:63]\n"
    "ds_read_b64_tr_b16 a[172:173], v239 offset:38912\n"
    "ds_read_b64_tr_b16 a[174:175], v239 offset:39424\n"
    "ds_read_b128 a[184:187], v188 offset:928\n"
    "v_mfma_f32_16x16x32_bf16 v[156:159], a[176:179], a[108:111], v[156:159]\n"
    "s_waitcnt lgkmcnt(10)\n"
    "v_mfma_f32_32x32x16_bf16 v[0:15], a[112:115], a[144:147], v[0:15]\n"
    "ds_read_b128 a[120:123], v182 offset:16384\n"
    "ds_read_b64_tr_b16 a[128:129], v238 offset:40960\n"
    "s_sub_u32 s13, s12, 1024\n"
    "buffer_load_dwordx4 v251, s[44:47], s13 offen offset:1024 lds\n"
    "v_mfma_f32_32x32x16_bf16 v[16:31], a[112:115], a[148:151], v[16:31]\n"
    "ds_read_b64_tr_b16 a[130:131], v238 offset:41472\n"
    "ds_read_b64_tr_b16 a[132:133], v239 offset:40960\n"
    "ds_read_b64_tr_b16 a[134:135], v239 offset:41472\n"
    "v_mfma_f32_32x32x16_bf16 v[32:47], a[112:115], a[152:155], v[32:47]\n"
    "ds_read_b64_tr_b16 a[136:137], v238 offset:43008\n"
    "ds_read_b64_tr_b16 a[138:139], v238 offset:43520\n"
    "v_mfma_f32_32x32x16_bf16 v[48:63], a[112:115], a[156:159], v[48:63]\n"
    "ds_read_b64_tr_b16 a[140:141], v239 offset:43008\n"
    "ds_read_b64_tr_b16 a[142:143], v239 offset:43520\n"
    "ds_read_b128 a[176:179], v188 offset:960\n"
    "v_mfma_f32_16x16x32_bf16 v[156:159], a[180:183], a[112:115], v[156:159]\n"
    "s_waitcnt lgkmcnt(10)\n"
    "v_mfma_f32_32x32x16_bf16 v[0:15], a[116:119], a[160:163], v[0:15]\n"
    "ds_read_b128 a[124:127], v183 offset:16384\n"
    "ds_read_b64_tr_b16 a[144:145], v238 offset:45056\n"
    "s_add_u32 s14, s12, 55296\n"
    "buffer_load_dwordx4 v251, s[40:43], s14 offen offset:2048 lds\n"
    "v_mfma_f32_32x32x16_bf16 v[16:31], a[116:119], a[164:167], v[16:31]\n"
    "ds_read_b64_tr_b16 a[146:147], v238 offset:45568\n"
    "ds_read_b64_tr_b16 a[148:149], v239 offset:45056\n"
    "ds_read_b64_tr_b16 a[150:151], v239 offset:45568\n"
    "v_mfma_f32_32x32x16_bf16 v[32:47], a[116:119], a[168:171], v[32:47]\n"
    "ds_read_b64_tr_b16 a[152:153], v238 offset:47104\n"
    "ds_read_b64_tr_b16 a[154:155], v238 offset:47616\n"
    "v_mfma_f32_32x32x16_bf16 v[48:63], a[116:119], a[172:175], v[48:63]\n"
    "ds_read_b64_tr_b16 a[156:157], v239 offset:47104\n"
    "ds_read_b64_tr_b16 a[158:159], v239 offset:47616\n"
    "ds_read_b128 a[180:183], v188 offset:992\n"
    "v_mfma_f32_16x16x32_bf16 v[156:159], a[184:187], a[116:119], v[156:159]\n"
    "s_add_u32 s15, s12, 54272\n"
    "buffer_load_dwordx4 v251, s[44:47], s15 offen offset:3072 lds\n"
    "s_waitcnt vmcnt(8) lgkmcnt(10)\n"
    "s_barrier\n"
    "v_mfma_f32_32x32x16_bf16 v[0:15], a[120:123], a[128:131], v[0:15]\n"
    "ds_read_b64_tr_b16 a[160:161], v253 offset:49152\n"
    "ds_read_b64_tr_b16 a[162:163], v253 offset:49408\n"
    "v_mfma_f32_32x32x16_bf16 v[16:31], a[120:123], a[132:135], v[16:31]\n"
    "ds_read_b64_tr_b16 a[164:165], v253 offset:50176\n"
    "ds_read_b64_tr_b16 a[166:167], v253 offset:50432\n"
    "v_mfma_f32_32x32x16_bf16 v[32:47], a[120:123], a[136:139], v[32:47]\n"
    "ds_read_b64_tr_b16 a[168:169], v253 offset:51200\n"
    "ds_read_b64_tr_b16 a[170:171], v253 offset:51456\n"
    "v_mfma_f32_32x32x16_bf16 v[48:63], a[120:123], a[140:143], v[48:63]\n"
    "ds_read_b64_tr_b16 a[172:173], v253 offset:52224\n"
    "ds_read_b64_tr_b16 a[174:175], v253 offset:52480\n"
    "s_add_u32 m0, s75, 16384\n"
    "s_add_u32 s12, s86, 1376256\n"
    "buffer_load_dwordx4 v251, s[40:43], s12 offen offset:0 lds\n"
    "v_mfma_f32_16x16x32_bf16 v[156:159], a[176:179], a[120:123], v[156:159]\n"
    "s_waitcnt lgkmcnt(8)\n"
    "v_mfma_f32_32x32x16_bf16 v[0:15], a[124:127], a[144:147], v[0:15]\n"
    "ds_read_b64_tr_b16 a[128:129], v253 offset:53248\n"
    "ds_read_b64_tr_b16 a[130:131], v253 offset:53504\n"
    "v_mfma_f32_32x32x16_bf16 v[16:31], a[124:127], a[148:151], v[16:31]\n"
    "ds_read_b64_tr_b16 a[132:133], v253 offset:54272\n"
    "ds_read_b64_tr_b16 a[134:135], v253 offset:54528\n"
    "v_mfma_f32_32x32x16_bf16 v[32:47], a[124:127], a[152:155], v[32:47]\n"
    "ds_read_b64_tr_b16 a[136:137], v253 offset:55296\n"
    "ds_read_b64_tr_b16 a[138:139], v253 offset:55552\n"
    "v_mfma_f32_32x32x16_bf16 v[48:63], a[124:127], a[156:159], v[48:63]\n"
    "ds_read_b64_tr_b16 a[140:141], v253 offset:56320\n"
    "ds_read_b64_tr_b16 a[142:143], v253 offset:56576\n"
    "s_sub_u32 s13, s12, 1024\n"
    "buffer_load_dwordx4 v251, s[44:47], s13 offen offset:1024 lds\n"
    "v_mfma_f32_16x16x32_bf16 v[156:159], a[180:183], a[124:127], v[156:159]\n"
    "s_waitcnt lgkmcnt(8)\n"
    "v_mfma_f32_32x32x16_bf16 v[64:79], a[160:163], a[0:3], 0\n"
    "ds_read_b64_tr_b16 a[144:145], v253 offset:57344\n"
    "ds_read_b64_tr_b16 a[146:147], v253 offset:57600\n"
    "v_mfma_f32_32x32x16_bf16 v[80:95], a[164:167], a[0:3], 0\n"
    "ds_read_b64_tr_b16 a[148:149], v253 offset:58368\n"
    "ds_read_b64_tr_b16 a[150:151], v253 offset:58624\n"
    "v_mfma_f32_32x32x16_bf16 v[64:79], a[168:171], a[4:7], v[64:79]\n"
    "ds_read_b64_tr_b16 a[152:153], v253 offset:59392\n"
    "ds_read_b64_tr_b16 a[154:155], v253 offset:59648\n"
    "v_mfma_f32_32x32x16_bf16 v[80:95], a[172:175], a[4:7], v[80:95]\n"
    "ds_read_b64_tr_b16 a[156:157], v253 offset:60416\n"
    "ds_read_b64_tr_b16 a[158:159], v253 offset:60672\n"
    "s_add_u32 s14, s12, 55296\n"
    "buffer_load_dwordx4 v251, s[40:43], s14 offen offset:2048 lds\n"
    "s_waitcnt lgkmcnt(8)\n"
    "v_mfma_f32_32x32x16_bf16 v[64:79], a[128:131], a[8:11], v[64:79]\n"
    "ds_read_b64_tr_b16 a[160:161], v253 offset:61440\n"
    "ds_read_b64_tr_b16 a[162:163], v253 offset:61696\n"
    "v_mul_f32 v249, 0xbfb8aa3b, v156\n"
    "v_exp_f32 v249, v249\n"
    "s_nop 0\n"
    "v_add_f32 v249, 1.0, v249\n"
    "v_rcp_f32 v249, v249\n"
    "s_nop 0\n"
    "v_sub_f32 v250, 1.0, v249\n"
    "v_pk_fma_f32 v[158:159], v[0:1], s[4:5], v[154:155] op_sel:[0,0,1] op_sel_hi:[1,0,1]\n"
    "v_pk_fma_f32 v[160:161], v[2:3], s[4:5], v[154:155] op_sel:[0,0,1] op_sel_hi:[1,0,1]\n"
    "v_exp_f32_e64 v158, v158 clamp\n"
    "v_exp_f32_e64 v159, v159 clamp\n"
    "v_exp_f32_e64 v160, v160 clamp\n"
    "v_exp_f32_e64 v161, v161 clamp\n"
    "v_max_f32 v0, s96, v0\n"
    "v_max_f32 v1, s96, v1\n"
    "v_max_f32 v2, s96, v2\n"
    "v_max_f32 v3, s96, v3\n"
    "v_mfma_f32_32x32x16_bf16 v[80:95], a[132:135], a[8:11], v[80:95]\n"
    "ds_read_b64_tr_b16 a[164:165], v253 offset:62464\n"
    "ds_read_b64_tr_b16 a[166:167], v253 offset:62720\n"
    "v_pk_add_f32 v[0:1], v[0:1], v[158:159]\n"
    "v_pk_add_f32 v[2:3], v[2:3], v[160:161]\n"
    "v_pk_fma_f32 v[158:159], v[4:5], s[4:5], v[154:155] op_sel:[0,0,1] op_sel_hi:[1,0,1]\n"
    "v_pk_fma_f32 v[160:161], v[6:7], s[4:5], v[154:155] op_sel:[0,0,1] op_sel_hi:[1,0,1]\n"
    "v_exp_f32_e64 v158, v158 clamp\n"
    "v_exp_f32_e64 v159, v159 clamp\n"
    "v_exp_f32_e64 v160, v160 clamp\n"
    "v_exp_f32_e64 v161, v161 clamp\n"
    "v_max_f32 v4, s96, v4\n"
    "v_max_f32 v5, s96, v5\n"
    "v_max_f32 v6, s96, v6\n"
    "v_max_f32 v7, s96, v7\n"
    "v_pk_add_f32 v[4:5], v[4:5], v[158:159]\n"
    "v_pk_add_f32 v[6:7], v[6:7], v[160:161]\n"
    "v_pk_fma_f32 v[158:159], v[8:9], s[4:5], v[154:155] op_sel:[0,0,1] op_sel_hi:[1,0,1]\n"
    "v_pk_fma_f32 v[160:161], v[10:11], s[4:5], v[154:155] op_sel:[0,0,1] op_sel_hi:[1,0,1]\n"
    "v_exp_f32_e64 v158, v158 clamp\n"
    "v_exp_f32_e64 v159, v159 clamp\n"
    "v_mfma_f32_32x32x16_bf16 v[64:79], a[136:139], a[12:15], v[64:79]\n"
    "ds_read_b64_tr_b16 a[168:169], v253 offset:63488\n"
    "ds_read_b64_tr_b16 a[170:171], v253 offset:63744\n"
    "v_exp_f32_e64 v160, v160 clamp\n"
    "v_exp_f32_e64 v161, v161 clamp\n"
    "v_max_f32 v8, s96, v8\n"
    "v_max_f32 v9, s96, v9\n"
    "v_max_f32 v10, s96, v10\n"
    "v_max_f32 v11, s96, v11\n"
    "v_pk_add_f32 v[8:9], v[8:9], v[158:159]\n"
    "v_pk_add_f32 v[10:11], v[10:11], v[160:161]\n"
    "v_pk_fma_f32 v[158:159], v[12:13], s[4:5], v[154:155] op_sel:[0,0,1] op_sel_hi:[1,0,1]\n"
    "v_pk_fma_f32 v[160:161], v[14:15], s[4:5], v[154:155] op_sel:[0,0,1] op_sel_hi:[1,0,1]\n"
    "v_exp_f32_e64 v158, v158 clamp\n"
    "v_exp_f32_e64 v159, v159 clamp\n"
    "v_exp_f32_e64 v160, v160 clamp\n"
    "v_exp_f32_e64 v161, v161 clamp\n"
    "v_max_f32 v12, s96, v12\n"
    "v_max_f32 v13, s96, v13\n"
    "v_max_f32 v14, s96, v14\n"
    "v_mfma_f32_32x32x16_bf16 v[80:95], a[140:143], a[12:15], v[80:95]\n"
    "ds_read_b64_tr_b16 a[172:173], v253 offset:64512\n"
    "ds_read_b64_tr_b16 a[174:175], v253 offset:64768\n"
    "s_add_u32 s15, s12, 54272\n"
    "buffer_load_dwordx4 v251, s[44:47], s15 offen offset:3072 lds\n"
    "v_max_f32 v15, s96, v15\n"
    "v_pk_add_f32 v[12:13], v[12:13], v[158:159]\n"
    "v_pk_add_f32 v[14:15], v[14:15], v[160:161]\n"
    "v_cvt_pk_bf16_f32 v172, v0, v1\n"
    "v_cvt_pk_bf16_f32 v173, v2, v3\n"
    "v_cvt_pk_bf16_f32 v174, v4, v5\n"
    "v_cvt_pk_bf16_f32 v175, v6, v7\n"
    "v_cvt_pk_bf16_f32 v176, v8, v9\n"
    "v_cvt_pk_bf16_f32 v177, v10, v11\n"
    "v_cvt_pk_bf16_f32 v178, v12, v13\n"
    "v_cvt_pk_bf16_f32 v179, v14, v15\n"
    "v_cvt_pk_bf16_f32 v188, v32, v33\n"
    "v_cvt_pk_bf16_f32 v189, v34, v35\n"
    "v_cvt_pk_bf16_f32 v190, v36, v37\n"
    "v_cvt_pk_bf16_f32 v191, v38, v39\n"
    "v_cvt_pk_bf16_f32 v192, v40, v41\n"
    "v_cvt_pk_bf16_f32 v193, v42, v43\n"
    "v_cvt_pk_bf16_f32 v194, v44, v45\n"
    "v_cvt_pk_bf16_f32 v195, v46, v47\n"
    "s_waitcnt vmcnt(8) lgkmcnt(8)\n"
    "s_barrier\n"
    "v_mfma_f32_32x32x16_bf16 v[64:79], a[144:147], a[16:19], v[64:79]\n"
    "ds_read_b64_tr_b16 a[128:129], v254 offset:0\n"
    "ds_read_b64_tr_b16 a[130:131], v254 offset:256\n"
    "v_pk_add_f32 v[0:1], v[0:1], v[8:9]\n"
    "v_pk_add_f32 v[2:3], v[2:3], v[10:11]\n"
    "v_mfma_f32_32x32x16_bf16 v[204:219], v[172:175], v[188:191], 0\n"
    "v_mfma_f32_32x32x16_bf16 v[204:219], v[176:179], v[192:195], v[204:219]\n"
    "v_pk_add_f32 v[4:5], v[4:5], v[12:13]\n"
    "v_pk_add_f32 v[6:7], v[6:7], v[14:15]\n"
    "v_pk_add_f32 v[0:1], v[0:1], v[4:5]\n"
    "v_pk_add_f32 v[2:3], v[2:3], v[6:7]\n"
    "v_pk_add_f32 v[0:1], v[0:1], v[2:3]\n"
    "v_add_f32 v166, v0, v1\n"
    "v_pk_fma_f32 v[158:159], v[16:17], s[4:5], v[154:155] op_sel:[0,0,1] op_sel_hi:[1,0,1]\n"
    "v_pk_fma_f32 v[160:161], v[18:19], s[4:5], v[154:155] op_sel:[0,0,1] op_sel_hi:[1,0,1]\n"
    "v_exp_f32_e64 v158, v158 clamp\n"
    "v_exp_f32_e64 v159, v159 clamp\n"
    "v_exp_f32_e64 v160, v160 clamp\n"
    "v_exp_f32_e64 v161, v161 clamp\n"
    "v_max_f32 v16, s96, v16\n"
    "v_max_f32 v17, s96, v17\n"
    "v_max_f32 v18, s96, v18\n"
    "v_max_f32 v19, s96, v19\n"
    "v_pk_add_f32 v[16:17], v[16:17], v[158:159]\n"
    "v_pk_add_f32 v[18:19], v[18:19], v[160:161]\n"
    "v_pk_fma_f32 v[158:159], v[20:21], s[4:5], v[154:155] op_sel:[0,0,1] op_sel_hi:[1,0,1]\n"
    "v_pk_fma_f32 v[160:161], v[22:23], s[4:5], v[154:155] op_sel:[0,0,1] op_sel_hi:[1,0,1]\n"
    "v_exp_f32_e64 v158, v158 clamp\n"
    "v_mfma_f32_32x32x16_bf16 v[80:95], a[148:151], a[16:19], v[80:95]\n"
    "ds_read_b64_tr_b16 a[132:133], v254 offset:1024\n"
    "ds_read_b64_tr_b16 a[134:135], v254 offset:1280\n"
    "v_exp_f32_e64 v159, v159 clamp\n"
    "v_exp_f32_e64 v160, v160 clamp\n"
    "v_exp_f32_e64 v161, v161 clamp\n"
    "v_max_f32 v20, s96, v20\n"
    "v_max_f32 v21, s96, v21\n"
    "v_max_f32 v22, s96, v22\n"
    "v_max_f32 v23, s96, v23\n"
    "v_pk_add_f32 v[20:21], v[20:21], v[158:159]\n"
    "v_pk_add_f32 v[22:23], v[22:23], v[160:161]\n"
    "v_pk_fma_f32 v[158:159], v[24:25], s[4:5], v[154:155] op_sel:[0,0,1] op_sel_hi:[1,0,1]\n"
    "v_pk_fma_f32 v[160:161], v[26:27], s[4:5], v[154:155] op_sel:[0,0,1] op_sel_hi:[1,0,1]\n"
    "v_exp_f32_e64 v158, v158 clamp\n"
    "v_exp_f32_e64 v159, v159 clamp\n"
    "v_exp_f32_e64 v160, v160 clamp\n"
    "v_exp_f32_e64 v161, v161 clamp\n"
    "v_max_f32 v24, s96, v24\n"
    "v_mfma_f32_32x32x16_bf16 v[64:79], a[152:155], a[20:23], v[64:79]\n"
    "ds_read_b64_tr_b16 a[136:137], v254 offset:2048\n"
    "ds_read_b64_tr_b16 a[138:139], v254 offset:2304\n"
    "s_add_u32 m0, s75, 32768\n"
    "s_nop 0\n"
    "buffer_load_dwordx4 v242, s[40:43], s87 offen offset:0 lds\n"
    "v_max_f32 v25, s96, v25\n"
    "v_max_f32 v26, s96, v26\n"
    "v_max_f32 v27, s96, v27\n"
    "v_pk_add_f32 v[24:25], v[24:25], v[158:159]\n"
    "v_pk_add_f32 v[26:27], v[26:27], v[160:161]\n"
    "v_pk_fma_f32 v[158:159], v[28:29], s[4:5], v[154:155] op_sel:[0,0,1] op_sel_hi:[1,0,1]\n"
    "v_pk_fma_f32 v[160:161], v[30:31], s[4:5], v[154:155] op_sel:[0,0,1] op_sel_hi:[1,0,1]\n"
    "v_exp_f32_e64 v158, v158 clamp\n"
    "v_exp_f32_e64 v159, v159 clamp\n"
    "v_exp_f32_e64 v160, v160 clamp\n"
    "v_exp_f32_e64 v161, v161 clamp\n"
    "v_max_f32 v28, s96, v28\n"
    "v_max_f32 v29, s96, v29\n"
    "v_max_f32 v30, s96, v30\n"
    "v_max_f32 v31, s96, v31\n"
    "v_mfma_f32_32x32x16_bf16 v[80:95], a[156:159], a[20:23], v[80:95]\n"
    "ds_read_b64_tr_b16 a[140:141], v254 offset:3072\n"
    "ds_read_b64_tr_b16 a[142:143], v254 offset:3328\n"
    "v_pk_add_f32 v[28:29], v[28:29], v[158:159]\n"
    "v_pk_add_f32 v[30:31], v[30:31], v[160:161]\n"
    "v_cvt_pk_bf16_f32 v180, v16, v17\n"
    "v_cvt_pk_bf16_f32 v181, v18, v19\n"
    "v_cvt_pk_bf16_f32 v182, v20, v21\n"
    "v_cvt_pk_bf16_f32 v183, v22, v23\n"
    "v_cvt_pk_bf16_f32 v184, v24, v25\n"
    "v_cvt_pk_bf16_f32 v185, v26, v27\n"
    "v_cvt_pk_bf16_f32 v186, v28, v29\n"
    "v_cvt_pk_bf16_f32 v187, v30, v31\n"
    "v_cvt_pk_bf16_f32 v196, v48, v49\n"
    "v_cvt_pk_bf16_f32 v197, v50, v51\n"
    "v_cvt_pk_bf16_f32 v198, v52, v53\n"
    "v_cvt_pk_bf16_f32 v199, v54, v55\n"
    "v_cvt_pk_bf16_f32 v200, v56, v57\n"
    "v_cvt_pk_bf16_f32 v201, v58, v59\n"
    "v_cvt_pk_bf16_f32 v202, v60, v61\n"
    "v_cvt_pk_bf16_f32 v203, v62, v63\n"
    "v_pk_add_f32 v[16:17], v[16:17], v[24:25]\n"
    "v_pk_add_f32 v[18:19], v[18:19], v[26:27]\n"
    "v_mfma_f32_32x32x16_bf16 v[220:235], v[180:183], v[196:199], 0\n"
    "buffer_load_dwordx4 v243, s[40:43], s87 offen offset:1024 lds\n"
    "v_mfma_f32_32x32x16_bf16 v[220:235], v[184:187], v[200:203], v[220:235]\n"
    "v_pk_add_f32 v[20:21], v[20:21], v[28:29]\n"
    "v_pk_add_f32 v[22:23], v[22:23], v[30:31]\n"
    "v_pk_add_f32 v[16:17], v[16:17], v[20:21]\n"
    "v_pk_add_f32 v[18:19], v[18:19], v[22:23]\n"
    "v_pk_add_f32 v[16:17], v[16:17], v[18:19]\n"
    "v_add_f32 v167, v16, v17\n"
    "s_lshl_b32 s16, s74, 10\n"
    "s_add_u32 s16, s16, 147456\n"
    "v_permlane32_swap_b32 v166, v167\n"
    "v_add_u32 v168, s16, v248\n"
    "s_add_u32 s17, s75, 147456\n"
    "v_add_u32 v169, s17, v248\n"
    "v_cndmask_b32 v204, v212, v204, s[98:99]\n"
    "v_cndmask_b32 v205, v213, v205, s[98:99]\n"
    "v_cndmask_b32 v206, v214, v206, s[98:99]\n"
    "v_cndmask_b32 v207, v215, v207, s[98:99]\n"
    "ds_write_b128 v168, v[204:207] offset:0\n"
    "v_cndmask_b32 v208, v216, v208, s[98:99]\n"
    "v_cndmask_b32 v209, v217, v209, s[98:99]\n"
    "v_cndmask_b32 v210, v218, v210, s[98:99]\n"
    "v_cndmask_b32 v211, v219, v211, s[98:99]\n"
    "ds_write_b128 v168, v[208:211] offset:4096\n"
    "v_cndmask_b32 v220, v228, v220, s[98:99]\n"
    "s_waitcnt lgkmcnt(10)\n"
    "v_mfma_f32_32x32x16_bf16 v[64:79], a[160:163], a[24:27], v[64:79]\n"
    "ds_read_b64_tr_b16 a[144:145], v254 offset:4096\n"
    "ds_read_b64_tr_b16 a[146:147], v254 offset:4352\n"
    "s_add_u32 m0, s75, 34816\n"
    "s_nop 0\n"
    "buffer_load_dwordx4 v242, s[44:47], s87 offen offset:0 lds\n"
    "v_cndmask_b32 v221, v229, v221, s[98:99]\n"
    "v_cndmask_b32 v222, v230, v222, s[98:99]\n"
    "v_cndmask_b32 v223, v231, v223, s[98:99]\n"
    "ds_write_b128 v168, v[220:223] offset:8192\n"
    "v_cndmask_b32 v224, v232, v224, s[98:99]\n"
    "v_cndmask_b32 v225, v233, v225, s[98:99]\n"
    "v_cndmask_b32 v226, v234, v226, s[98:99]\n"
    "v_cndmask_b32 v227, v235, v227, s[98:99]\n"
    "ds_write_b128 v168, v[224:227] offset:12288\n"
    "v_mfma_f32_32x32x16_bf16 v[80:95], a[164:167], a[24:27], v[80:95]\n"
    "ds_read_b64_tr_b16 a[148:149], v254 offset:5120\n"
    "ds_read_b64_tr_b16 a[150:151], v254 offset:5376\n"
    "v_mfma_f32_32x32x16_bf16 v[64:79], a[168:171], a[28:31], v[64:79]\n"
    "ds_read_b64_tr_b16 a[152:153], v254 offset:6144\n"
    "ds_read_b64_tr_b16 a[154:155], v254 offset:6400\n"
    "buffer_load_dwordx4 v243, s[44:47], s87 offen offset:1024 lds\n"
    "v_mfma_f32_32x32x16_bf16 v[80:95], a[172:175], a[28:31], v[80:95]\n"
    "ds_read_b64_tr_b16 a[156:157], v254 offset:7168\n"
    "ds_read_b64_tr_b16 a[158:159], v254 offset:7424\n"
    "s_waitcnt lgkmcnt(6)\n"
    "s_barrier\n"
    "v_mfma_f32_32x32x16_bf16 v[64:79], a[128:131], a[32:35], v[64:79]\n"
    "ds_read_b64_tr_b16 a[160:161], v254 offset:8192\n"
    "ds_read_b64_tr_b16 a[162:163], v254 offset:8448\n"
    "ds_read_b128 v[172:175], v169 offset:0\n"
    "ds_read_b128 v[176:179], v169 offset:1024\n"
    "ds_read_b128 v[180:183], v169 offset:2048\n"
    "ds_read_b128 v[184:187], v169 offset:3072\n"
    "v_add_f32 v190, v166, v167\n"
    "v_mfma_f32_32x32x16_bf16 v[80:95], a[132:135], a[32:35], v[80:95]\n"
    "ds_read_b64_tr_b16 a[164:165], v254 offset:9216\n"
    "ds_read_b64_tr_b16 a[166:167], v254 offset:9472\n"
    "s_add_u32 m0, s75, 49152\n"
    "s_add_u32 s13, s87, 229376\n"
    "buffer_load_dwordx4 v242, s[40:43], s13 offen offset:0 lds\n"
    "v_mfma_f32_32x32x16_bf16 v[64:79], a[136:139], a[36:39], v[64:79]\n"
    "ds_read_b64_tr_b16 a[168:169], v254 offset:10240\n"
    "ds_read_b64_tr_b16 a[170:171], v254 offset:10496\n"
    "s_waitcnt lgkmcnt(5)\n"
    "v_pk_add_f32 v[172:173], v[172:173], v[180:181]\n"
    "v_pk_add_f32 v[174:175], v[174:175], v[182:183]\n"
    "s_waitcnt lgkmcnt(4)\n"
    "v_pk_add_f32 v[176:177], v[176:177], v[184:185]\n"
    "v_pk_add_f32 v[178:179], v[178:179], v[186:187]\n"
    "v_pk_add_f32 v[172:173], v[172:173], v[176:177]\n"
    "v_pk_add_f32 v[174:175], v[174:175], v[178:179]\n"
    "v_cvt_pk_bf16_f32 v188, v172, v173\n"
    "v_cvt_pk_bf16_f32 v189, v174, v175\n"
    "v_add3_u32 v191, v188, v189, v190\n"
    "v_sub_u32 v191, s72, v191\n"
    "buffer_store_dwordx4 v[188:191], v248, s[60:63], s100 offen sc1\n"
    "v_mfma_f32_32x32x16_bf16 v[80:95], a[140:143], a[36:39], v[80:95]\n"
    "ds_read_b64_tr_b16 a[172:173], v254 offset:11264\n"
    "ds_read_b64_tr_b16 a[174:175], v254 offset:11520\n"
    "v_mfma_f32_32x32x16_bf16 v[64:79], a[144:147], a[40:43], v[64:79]\n"
    "ds_read_b64_tr_b16 a[128:129], v254 offset:12288\n"
    "ds_read_b64_tr_b16 a[130:131], v254 offset:12544\n"
    "v_mfma_f32_32x32x16_bf16 v[80:95], a[148:151], a[40:43], v[80:95]\n"
    "ds_read_b64_tr_b16 a[132:133], v254 offset:13312\n"
    "ds_read_b64_tr_b16 a[134:135], v254 offset:13568\n"
    "buffer_load_dwordx4 v243, s[40:43], s13 offen offset:1024 lds\n"
    "v_mfma_f32_32x32x16_bf16 v[64:79], a[152:155], a[44:47], v[64:79]\n"
    "ds_read_b64_tr_b16 a[136:137], v254 offset:14336\n"
    "ds_read_b64_tr_b16 a[138:139], v254 offset:14592\n"
    "v_mfma_f32_32x32x16_bf16 v[80:95], a[156:159], a[44:47], v[80:95]\n"
    "ds_read_b64_tr_b16 a[140:141], v254 offset:15360\n"
    "ds_read_b64_tr_b16 a[142:143], v254 offset:15616\n"
    "s_add_u32 m0, s75, 51200\n"
    "s_nop 0\n"
    "buffer_load_dwordx4 v242, s[44:47], s13 offen offset:0 lds\n"
    "s_waitcnt vmcnt(12) lgkmcnt(8)\n"
    "s_barrier\n"
    "v_mfma_f32_32x32x16_bf16 v[64:79], a[160:163], a[48:51], v[64:79]\n"
    "ds_read_b64_tr_b16 a[144:145], v252 offset:0\n"
    "ds_read_b64_tr_b16 a[146:147], v252 offset:256\n"
    "v_mfma_f32_32x32x16_bf16 v[80:95], a[164:167], a[48:51], v[80:95]\n"
    "ds_read_b64_tr_b16 a[148:149], v252 offset:1024\n"
    "ds_read_b64_tr_b16 a[150:151], v252 offset:1280\n"
    "v_mfma_f32_32x32x16_bf16 v[64:79], a[168:171], a[52:55], v[64:79]\n"
    "ds_read_b64_tr_b16 a[152:153], v252 offset:2048\n"
    "ds_read_b64_tr_b16 a[154:155], v252 offset:2304\n"
    "buffer_load_dwordx4 v243, s[44:47], s13 offen offset:1024 lds\n"
    "v_mfma_f32_32x32x16_bf16 v[80:95], a[172:175], a[52:55], v[80:95]\n"
    "ds_read_b64_tr_b16 a[156:157], v252 offset:3072\n"
    "ds_read_b64_tr_b16 a[158:159], v252 offset:3328\n"
    "s_waitcnt lgkmcnt(8)\n"
    "v_mfma_f32_32x32x16_bf16 v[64:79], a[128:131], a[56:59], v[64:79]\n"
    "ds_read_b64_tr_b16 a[160:161], v252 offset:4096\n"
    "ds_read_b64_tr_b16 a[162:163], v252 offset:4352\n"
    "s_add_u32 m0, s75, 65536\n"
    "s_add_u32 s14, s87, 458752\n"
    "buffer_load_dwordx4 v242, s[40:43], s14 offen offset:0 lds\n"
    "v_mfma_f32_32x32x16_bf16 v[80:95], a[132:135], a[56:59], v[80:95]\n"
    "ds_read_b64_tr_b16 a[164:165], v252 offset:5120\n"
    "ds_read_b64_tr_b16 a[166:167], v252 offset:5376\n"
    "v_mfma_f32_32x32x16_bf16 v[64:79], a[136:139], a[60:63], v[64:79]\n"
    "ds_read_b64_tr_b16 a[168:169], v252 offset:6144\n"
    "ds_read_b64_tr_b16 a[170:171], v252 offset:6400\n"
    "buffer_load_dwordx4 v243, s[40:43], s14 offen offset:1024 lds\n"
    "v_mfma_f32_32x32x16_bf16 v[80:95], a[140:143], a[60:63], v[80:95]\n"
    "ds_read_b64_tr_b16 a[172:173], v252 offset:7168\n"
    "ds_read_b64_tr_b16 a[174:175], v252 offset:7424\n"
    "s_waitcnt lgkmcnt(8)\n"
    "v_mfma_f32_32x32x16_bf16 v[64:79], a[144:147], a[64:67], v[64:79]\n"
    "ds_read_b64_tr_b16 a[128:129], v252 offset:8192\n"
    "ds_read_b64_tr_b16 a[130:131], v252 offset:8448\n"
    "s_add_u32 m0, s75, 67584\n"
    "s_nop 0\n"
    "buffer_load_dwordx4 v242, s[44:47], s14 offen offset:0 lds\n"
    "v_mfma_f32_32x32x16_bf16 v[80:95], a[148:151], a[64:67], v[80:95]\n"
    "ds_read_b64_tr_b16 a[132:133], v252 offset:9216\n"
    "ds_read_b64_tr_b16 a[134:135], v252 offset:9472\n"
    "v_mfma_f32_32x32x16_bf16 v[64:79], a[152:155], a[68:71], v[64:79]\n"
    "ds_read_b64_tr_b16 a[136:137], v252 offset:10240\n"
    "ds_read_b64_tr_b16 a[138:139], v252 offset:10496\n"
    "buffer_load_dwordx4 v243, s[44:47], s14 offen offset:1024 lds\n"
    "v_mfma_f32_32x32x16_bf16 v[80:95], a[156:159], a[68:71], v[80:95]\n"
    "ds_read_b64_tr_b16 a[140:141], v252 offset:11264\n"
    "ds_read_b64_tr_b16 a[142:143], v252 offset:11520\n"
    "s_waitcnt lgkmcnt(8)\n"
    "v_mfma_f32_32x32x16_bf16 v[64:79], a[160:163], a[72:75], v[64:79]\n"
    "ds_read_b64_tr_b16 a[144:145], v252 offset:12288\n"
    "ds_read_b64_tr_b16 a[146:147], v252 offset:12544\n"
    "v_mfma_f32_32x32x16_bf16 v[80:95], a[164:167], a[72:75], v[80:95]\n"
    "ds_read_b64_tr_b16 a[148:149], v252 offset:13312\n"
    "ds_read_b64_tr_b16 a[150:151], v252 offset:13568\n"
    "s_add_u32 m0, s75, 81920\n"
    "s_add_u32 s15, s87, 688128\n"
    "buffer_load_dwordx4 v242, s[40:43], s15 offen offset:0 lds\n"
    "v_mfma_f32_32x32x16_bf16 v[64:79], a[168:171], a[76:79], v[64:79]\n"
    "ds_read_b64_tr_b16 a[152:153], v252 offset:14336\n"
    "ds_read_b64_tr_b16 a[154:155], v252 offset:14592\n"
    "v_mfma_f32_32x32x16_bf16 v[80:95], a[172:175], a[76:79], v[80:95]\n"
    "ds_read_b64_tr_b16 a[156:157], v252 offset:15360\n"
    "ds_read_b64_tr_b16 a[158:159], v252 offset:15616\n"
    "buffer_load_dwordx4 v243, s[40:43], s15 offen offset:1024 lds\n"
    "s_waitcnt vmcnt(15) lgkmcnt(8)\n"
    "s_barrier\n"
    "v_mfma_f32_32x32x16_bf16 v[64:79], a[128:131], a[80:83], v[64:79]\n"
    "ds_read_b64_tr_b16 a[160:161], v252 offset:16384\n"
    "ds_read_b64_tr_b16 a[162:163], v252 offset:16640\n"
    "v_mfma_f32_32x32x16_bf16 v[80:95], a[132:135], a[80:83], v[80:95]\n"
    "ds_read_b64_tr_b16 a[164:165], v252 offset:17408\n"
    "ds_read_b64_tr_b16 a[166:167], v252 offset:17664\n"
    "s_add_u32 m0, s75, 83968\n"
    "s_nop 0\n"
    "buffer_load_dwordx4 v242, s[44:47], s15 offen offset:0 lds\n"
    "v_mfma_f32_32x32x16_bf16 v[64:79], a[136:139], a[84:87], v[64:79]\n"
    "ds_read_b64_tr_b16 a[168:169], v252 offset:18432\n"
    "ds_read_b64_tr_b16 a[170:171], v252 offset:18688\n"
    "v_mfma_f32_32x32x16_bf16 v[80:95], a[140:143], a[84:87], v[80:95]\n"
    "ds_read_b64_tr_b16 a[172:173], v252 offset:19456\n"
    "ds_read_b64_tr_b16 a[174:175], v252 offset:19712\n"
    "buffer_load_dwordx4 v243, s[44:47], s15 offen offset:1024 lds\n"
    "s_waitcnt lgkmcnt(8)\n"
    "v_mfma_f32_32x32x16_bf16 v[64:79], a[144:147], a[88:91], v[64:79]\n"
    "ds_read_b64_tr_b16 a[128:129], v252 offset:20480\n"
    "ds_read_b64_tr_b16 a[130:131], v252 offset:20736\n"
    "v_mfma_f32_32x32x16_bf16 v[80:95], a[148:151], a[88:91], v[80:95]\n"
    "ds_read_b64_tr_b16 a[132:133], v252 offset:21504\n"
    "ds_read_b64_tr_b16 a[134:135], v252 offset:21760\n"
    "v_mfma_f32_32x32x16_bf16 v[64:79], a[152:155], a[92:95], v[64:79]\n"
    "ds_read_b64_tr_b16 a[136:137], v252 offset:22528\n"
    "ds_read_b64_tr_b16 a[138:139], v252 offset:22784\n"
    "s_add_u32 m0, s75, 98304\n"
    "s_add_u32 s12, s87, 917504\n"
    "buffer_load_dwordx4 v242, s[40:43], s12 offen offset:0 lds\n"
    "v_mfma_f32_32x32x16_bf16 v[80:95], a[156:159], a[92:95], v[80:95]\n"
    "ds_read_b64_tr_b16 a[140:141], v252 offset:23552\n"
    "ds_read_b64_tr_b16 a[142:143], v252 offset:23808\n"
    "s_waitcnt lgkmcnt(8)\n"
    "v_mfma_f32_32x32x16_bf16 v[64:79], a[160:163], a[96:99], v[64:79]\n"
    "ds_read_b64_tr_b16 a[144:145], v252 offset:24576\n"
    "ds_read_b64_tr_b16 a[146:147], v252 offset:24832\n"
    "buffer_load_dwordx4 v243, s[40:43], s12 offen offset:1024 lds\n"
    "v_mfma_f32_32x32x16_bf16 v[80:95], a[164:167], a[96:99], v[80:95]\n"
    "ds_read_b64_tr_b16 a[148:149], v252 offset:25600\n"
    "ds_read_b64_tr_b16 a[150:151], v252 offset:25856\n"
    "v_mfma_f32_32x32x16_bf16 v[64:79], a[168:171], a[100:103], v[64:79]\n"
    "ds_read_b64_tr_b16 a[152:153], v252 offset:26624\n"
    "ds_read_b64_tr_b16 a[154:155], v252 offset:26880\n"
    "s_add_u32 m0, s75, 100352\n"
    "s_nop 0\n"
    "buffer_load_dwordx4 v242, s[44:47], s12 offen offset:0 lds\n"
    "v_mfma_f32_32x32x16_bf16 v[80:95], a[172:175], a[100:103], v[80:95]\n"
    "ds_read_b64_tr_b16 a[156:157], v252 offset:27648\n"
    "ds_read_b64_tr_b16 a[158:159], v252 offset:27904\n"
    "s_waitcnt lgkmcnt(8)\n"
    "v_mfma_f32_32x32x16_bf16 v[64:79], a[128:131], a[104:107], v[64:79]\n"
    "ds_read_b64_tr_b16 a[160:161], v252 offset:28672\n"
    "ds_read_b64_tr_b16 a[162:163], v252 offset:28928\n"
    "buffer_load_dwordx4 v243, s[44:47], s12 offen offset:1024 lds\n"
    "v_mfma_f32_32x32x16_bf16 v[80:95], a[132:135], a[104:107], v[80:95]\n"
    "ds_read_b64_tr_b16 a[164:165], v252 offset:29696\n"
    "ds_read_b64_tr_b16 a[166:167], v252 offset:29952\n"
    "v_mfma_f32_32x32x16_bf16 v[64:79], a[136:139], a[108:111], v[64:79]\n"
    "ds_read_b64_tr_b16 a[168:169], v252 offset:30720\n"
    "ds_read_b64_tr_b16 a[170:171], v252 offset:30976\n"
    "s_add_u32 m0, s75, 114688\n"
    "s_add_u32 s13, s87, 1146880\n"
    "buffer_load_dwordx4 v242, s[40:43], s13 offen offset:0 lds\n"
    "v_mfma_f32_32x32x16_bf16 v[80:95], a[140:143], a[108:111], v[80:95]\n"
    "ds_read_b64_tr_b16 a[172:173], v252 offset:31744\n"
    "ds_read_b64_tr_b16 a[174:175], v252 offset:32000\n"
    "s_waitcnt vmcnt(18) lgkmcnt(8)\n"
    "s_barrier\n"
    "v_mfma_f32_32x32x16_bf16 v[64:79], a[144:147], a[112:115], v[64:79]\n"
    "ds_read_b64_tr_b16 a[128:129], v236 offset:32768\n"
    "ds_read_b64_tr_b16 a[130:131], v236 offset:33280\n"
    "v_mfma_f32_32x32x16_bf16 v[80:95], a[148:151], a[112:115], v[80:95]\n"
    "ds_read_b64_tr_b16 a[132:133], v237 offset:32768\n"
    "ds_read_b64_tr_b16 a[134:135], v237 offset:33280\n"
    "buffer_load_dwordx4 v243, s[40:43], s13 offen offset:1024 lds\n"
    "v_mfma_f32_32x32x16_bf16 v[64:79], a[152:155], a[116:119], v[64:79]\n"
    "ds_read_b64_tr_b16 a[136:137], v236 offset:34816\n"
    "ds_read_b64_tr_b16 a[138:139], v236 offset:35328\n"
    "v_mfma_f32_32x32x16_bf16 v[80:95], a[156:159], a[116:119], v[80:95]\n"
    "ds_read_b64_tr_b16 a[140:141], v237 offset:34816\n"
    "ds_read_b64_tr_b16 a[142:143], v237 offset:35328\n"
    "s_add_u32 m0, s75, 116736\n"
    "s_nop 0\n"
    "buffer_load_dwordx4 v242, s[44:47], s13 offen offset:0 lds\n"
    "s_waitcnt lgkmcnt(8)\n"
    "v_mfma_f32_32x32x16_bf16 v[64:79], a[160:163], a[120:123], v[64:79]\n"
    "ds_read_b64_tr_b16 a[144:145], v236 offset:36864\n"
    "ds_read_b64_tr_b16 a[146:147], v236 offset:37376\n"
    "v_mfma_f32_32x32x16_bf16 v[80:95], a[164:167], a[120:123], v[80:95]\n"
    "ds_read_b64_tr_b16 a[148:149], v237 offset:36864\n"
    "ds_read_b64_tr_b16 a[150:151], v237 offset:37376\n"
    "buffer_load_dwordx4 v243, s[44:47], s13 offen offset:1024 lds\n"
    "v_mfma_f32_32x32x16_bf16 v[64:79], a[168:171], a[124:127], v[64:79]\n"
    "ds_read_b64_tr_b16 a[152:153], v236 offset:38912\n"
    "ds_read_b64_tr_b16 a[154:155], v236 offset:39424\n"
    "v_mfma_f32_32x32x16_bf16 v[80:95], a[172:175], a[124:127], v[80:95]\n"
    "ds_read_b64_tr_b16 a[156:157], v237 offset:38912\n"
    "ds_read_b64_tr_b16 a[158:159], v237 offset:39424\n"
    "s_add_u32 m0, s75, 131072\n"
    "s_add_u32 s14, s87, 1376256\n"
    "buffer_load_dwordx4 v242, s[40:43], s14 offen offset:0 lds\n"
    "s_waitcnt lgkmcnt(8)\n"
    "v_mfma_f32_32x32x16_bf16 v[0:15], a[128:131], a[0:3], 0\n"
    "ds_read_b64_tr_b16 a[160:161], v236 offset:40960\n"
    "ds_read_b64_tr_b16 a[162:163], v236 offset:41472\n"
    "v_mfma_f32_32x32x16_bf16 v[16:31], a[132:135], a[0:3], 0\n"
    "ds_read_b64_tr_b16 a[164:165], v237 offset:40960\n"
    "ds_read_b64_tr_b16 a[166:167], v237 offset:41472\n"
    "v_mfma_f32_32x32x16_bf16 v[32:47], a[136:139], a[0:3], 0\n"
    "ds_read_b64_tr_b16 a[168:169], v236 offset:43008\n"
    "ds_read_b64_tr_b16 a[170:171], v236 offset:43520\n"
    "buffer_load_dwordx4 v243, s[40:43], s14 offen offset:1024 lds\n"
    "v_mfma_f32_32x32x16_bf16 v[48:63], a[140:143], a[0:3], 0\n"
    "ds_read_b64_tr_b16 a[172:173], v237 offset:43008\n"
    "ds_read_b64_tr_b16 a[174:175], v237 offset:43520\n"
    "s_waitcnt lgkmcnt(8)\n"
    "v_mfma_f32_32x32x16_bf16 v[0:15], a[144:147], a[4:7], v[0:15]\n"
    "ds_read_b64_tr_b16 a[128:129], v236 offset:45056\n"
    "ds_read_b64_tr_b16 a[130:131], v236 offset:45568\n"
    "s_add_u32 m0, s75, 133120\n"
    "s_nop 0\n"
    "buffer_load_dwordx4 v242, s[44:47], s14 offen offset:0 lds\n"
    "v_mfma_f32_32x32x16_bf16 v[16:31], a[148:151], a[4:7], v[16:31]\n"
    "ds_read_b64_tr_b16 a[132:133], v237 offset:45056\n"
    "ds_read_b64_tr_b16 a[134:135], v237 offset:45568\n"
    "s_lshl_b32 s79, s24, 11\n"
    "s_add_u32 s79, s79, s88\n"
    "s_add_u32 s79, s79, 9216\n"
    "s_lshl_b32 s80, s24, 8\n"
    "v_mfma_f32_32x32x16_bf16 v[32:47], a[152:155], a[4:7], v[32:47]\n"
    "ds_read_b64_tr_b16 a[136:137], v236 offset:47104\n"
    "ds_read_b64_tr_b16 a[138:139], v236 offset:47616\n"
    "buffer_load_dwordx4 v243, s[44:47], s14 offen offset:1024 lds\n"
    "s_add_u32 s80, s80, s88\n"
    "v_mfma_f32_32x32x16_bf16 v[48:63], a[156:159], a[4:7], v[48:63]\n"
    "ds_read_b64_tr_b16 a[140:141], v237 offset:47104\n"
    "ds_read_b64_tr_b16 a[142:143], v237 offset:47616\n"
    "s_add_u32 s80, s80, 9216\n"
    "v_pk_mul_f32 v[64:65], v[64:65], v[80:81]\n"
    "v_pk_mul_f32 v[66:67], v[66:67], v[82:83]\n"
    "v_pk_mul_f32 v[68:69], v[68:69], v[84:85]\n"
    "s_waitcnt vmcnt(20) lgkmcnt(8)\n"
    "s_barrier\n"
    "v_mfma_f32_32x32x16_bf16 v[0:15], a[160:163], a[8:11], v[0:15]\n"
    "ds_read_b64_tr_b16 a[144:145], v236 offset:49152\n"
    "ds_read_b64_tr_b16 a[146:147], v236 offset:49664\n"
    "s_add_u32 m0, s75, 0\n"
    "s_add_u32 s15, s87, 1605632\n"
    "buffer_load_dwordx4 v242, s[40:43], s15 offen offset:0 lds\n"
    "v_mfma_f32_32x32x16_bf16 v[16:31], a[164:167], a[8:11], v[16:31]\n"
    "ds_read_b64_tr_b16 a[148:149], v237 offset:49152\n"
    "ds_read_b64_tr_b16 a[150:151], v237 offset:49664\n"
    "v_pk_mul_f32 v[70:71], v[70:71], v[86:87]\n"
    "v_pk_mul_f32 v[64:65], v[64:65], v[250:251] op_sel_hi:[1,0]\n"
    "v_pk_mul_f32 v[66:67], v[66:67], v[250:251] op_sel_hi:[1,0]\n"
    "v_pk_mul_f32 v[68:69], v[68:69], v[250:251] op_sel_hi:[1,0]\n"
    "v_mfma_f32_32x32x16_bf16 v[32:47], a[168:171], a[8:11], v[32:47]\n"
    "ds_read_b64_tr_b16 a[152:153], v236 offset:51200\n"
    "ds_read_b64_tr_b16 a[154:155], v236 offset:51712\n"
    "buffer_load_dwordx4 v243, s[40:43], s15 offen offset:1024 lds\n"
    "v_pk_mul_f32 v[70:71], v[70:71], v[250:251] op_sel_hi:[1,0]\n"
    "v_mfma_f32_32x32x16_bf16 v[48:63], a[172:175], a[8:11], v[48:63]\n"
    "ds_read_b64_tr_b16 a[156:157], v237 offset:51200\n"
    "ds_read_b64_tr_b16 a[158:159], v237 offset:51712\n"
    "v_cvt_pk_bf16_f32 v64, v64, v65\n"
    "v_cvt_pk_bf16_f32 v65, v66, v67\n"
    "v_cvt_pk_bf16_f32 v66, v68, v69\n"
    "v_cvt_pk_bf16_f32 v67, v70, v71\n"
    "s_waitcnt lgkmcnt(8)\n"
    "v_mfma_f32_32x32x16_bf16 v[0:15], a[128:131], a[12:15], v[0:15]\n"
    "ds_read_b64_tr_b16 a[160:161], v236 offset:53248\n"
    "ds_read_b64_tr_b16 a[162:163], v236 offset:53760\n"
    "v_mfma_f32_32x32x16_bf16 v[16:31], a[132:135], a[12:15], v[16:31]\n"
    "ds_read_b64_tr_b16 a[164:165], v237 offset:53248\n"
    "ds_read_b64_tr_b16 a[166:167], v237 offset:53760\n"
    "buffer_store_dwordx4 v[64:67], v248, s[60:63], s79 offen offset:512 sc1\n"
    "v_add3_u32 v80, v64, v65, s72\n"
    "v_mfma_f32_32x32x16_bf16 v[32:47], a[136:139], a[12:15], v[32:47]\n"
    "ds_read_b64_tr_b16 a[168:169], v236 offset:55296\n"
    "ds_read_b64_tr_b16 a[170:171], v236 offset:55808\n"
    "v_add3_u32 v80, v80, v66, v67\n"
    "v_pk_mul_f32 v[72:73], v[72:73], v[88:89]\n"
    "v_pk_mul_f32 v[74:75], v[74:75], v[90:91]\n"
    "v_pk_mul_f32 v[76:77], v[76:77], v[92:93]\n"
    "v_mfma_f32_32x32x16_bf16 v[48:63], a[140:143], a[12:15], v[48:63]\n"
    "ds_read_b64_tr_b16 a[172:173], v237 offset:55296\n"
    "ds_read_b64_tr_b16 a[174:175], v237 offset:55808\n"
    "s_add_u32 m0, s75, 2048\n"
    "s_nop 0\n"
    "buffer_load_dwordx4 v242, s[44:47], s15 offen offset:0 lds\n"
    "s_waitcnt lgkmcnt(8)\n"
    "v_mfma_f32_32x32x16_bf16 v[0:15], a[144:147], a[16:19], v[0:15]\n"
    "ds_read_b64_tr_b16 a[128:129], v236 offset:57344\n"
    "ds_read_b64_tr_b16 a[130:131], v236 offset:57856\n"
    "v_pk_mul_f32 v[78:79], v[78:79], v[94:95]\n"
    "v_pk_mul_f32 v[72:73], v[72:73], v[250:251] op_sel_hi:[1,0]\n"
    "v_pk_mul_f32 v[74:75], v[74:75], v[250:251] op_sel_hi:[1,0]\n"
    "v_pk_mul_f32 v[76:77], v[76:77], v[250:251] op_sel_hi:[1,0]\n"
    "v_mfma_f32_32x32x16_bf16 v[16:31], a[148:151], a[16:19], v[16:31]\n"
    "ds_read_b64_tr_b16 a[132:133], v237 offset:57344\n"
    "ds_read_b64_tr_b16 a[134:135], v237 offset:57856\n"
    "buffer_load_dwordx4 v243, s[44:47], s15 offen offset:1024 lds\n"
    "v_pk_mul_f32 v[78:79], v[78:79], v[250:251] op_sel_hi:[1,0]\n"
    "v_mfma_f32_32x32x16_bf16 v[32:47], a[152:155], a[16:19], v[32:47]\n"
    "ds_read_b64_tr_b16 a[136:137], v236 offset:59392\n"
    "ds_read_b64_tr_b16 a[138:139], v236 offset:59904\n"
    "v_cvt_pk_bf16_f32 v72, v72, v73\n"
    "v_cvt_pk_bf16_f32 v73, v74, v75\n"
    "v_cvt_pk_bf16_f32 v74, v76, v77\n"
    "v_cvt_pk_bf16_f32 v75, v78, v79\n"
    "v_mfma_f32_32x32x16_bf16 v[48:63], a[156:159], a[16:19], v[48:63]\n"
    "ds_read_b64_tr_b16 a[140:141], v237 offset:59392\n"
    "ds_read_b64_tr_b16 a[142:143], v237 offset:59904\n"
    "buffer_store_dwordx4 v[72:75], v248, s[60:63], s79 offen offset:1536 sc1\n"
    "v_add3_u32 v80, v80, v72, v73\n"
    "s_waitcnt lgkmcnt(8)\n"
    "v_mfma_f32_32x32x16_bf16 v[0:15], a[160:163], a[20:23], v[0:15]\n"
    "ds_read_b64_tr_b16 a[144:145], v236 offset:61440\n"
    "ds_read_b64_tr_b16 a[146:147], v236 offset:61952\n"
    "v_add3_u32 v80, v80, v74, v75\n"
    "buffer_store_dword v80, v144, s[60:63], s80 offen offset:0 sc1\n"
    "v_mfma_f32_32x32x16_bf16 v[16:31], a[164:167], a[20:23], v[16:31]\n"
    "ds_read_b64_tr_b16 a[148:149], v237 offset:61440\n"
    "ds_read_b64_tr_b16 a[150:151], v237 offset:61952\n"
    "v_mfma_f32_32x32x16_bf16 v[32:47], a[168:171], a[20:23], v[32:47]\n"
    "ds_read_b64_tr_b16 a[152:153], v236 offset:63488\n"
    "ds_read_b64_tr_b16 a[154:155], v236 offset:64000\n"
    "s_add_u32 m0, s75, 16384\n"
    "s_nop 0\n"
    "buffer_load_dwordx4 v246, s[52:55], s25 offen offset:0 lds\n"
    "v_mfma_f32_32x32x16_bf16 v[48:63], a[172:175], a[20:23], v[48:63]\n"
    "ds_read_b64_tr_b16 a[156:157], v237 offset:63488\n"
    "ds_read_b64_tr_b16 a[158:159], v237 offset:64000\n"
    "s_waitcnt vmcnt(24) lgkmcnt(8)\n"
    "s_barrier\n"
    "v_mfma_f32_32x32x16_bf16 v[0:15], a[128:131], a[24:27], v[0:15]\n"
    "ds_read_b64_tr_b16 a[160:161], v238 offset:0\n"
    "ds_read_b64_tr_b16 a[162:163], v238 offset:512\n"
    "v_mfma_f32_32x32x16_bf16 v[16:31], a[132:135], a[24:27], v[16:31]\n"
    "ds_read_b64_tr_b16 a[164:165], v239 offset:0\n"
    "ds_read_b64_tr_b16 a[166:167], v239 offset:512\n"
    "buffer_load_dwordx4 v247, s[52:55], s25 offen offset:1024 lds\n"
    "v_mfma_f32_32x32x16_bf16 v[32:47], a[136:139], a[24:27], v[32:47]\n"
    "ds_read_b64_tr_b16 a[168:169], v238 offset:2048\n"
    "ds_read_b64_tr_b16 a[170:171], v238 offset:2560\n"
    "v_mfma_f32_32x32x16_bf16 v[48:63], a[140:143], a[24:27], v[48:63]\n"
    "ds_read_b64_tr_b16 a[172:173], v239 offset:2048\n"
    "ds_read_b64_tr_b16 a[174:175], v239 offset:2560\n"
    "s_xor_b32 s13, s25, 128\n"
    "s_add_u32 m0, s75, 18432\n"
    "s_nop 0\n"
    "buffer_load_dwordx4 v246, s[52:55], s13 offen offset:0 lds\n"
    "s_waitcnt lgkmcnt(8)\n"
    "v_mfma_f32_32x32x16_bf16 v[0:15], a[144:147], a[28:31], v[0:15]\n"
    "ds_read_b64_tr_b16 a[128:129], v238 offset:4096\n"
    "ds_read_b64_tr_b16 a[130:131], v238 offset:4608\n"
    "v_mfma_f32_32x32x16_bf16 v[16:31], a[148:151], a[28:31], v[16:31]\n"
    "ds_read_b64_tr_b16 a[132:133], v239 offset:4096\n"
    "ds_read_b64_tr_b16 a[134:135], v239 offset:4608\n"
    "buffer_load_dwordx4 v247, s[52:55], s13 offen offset:1024 lds\n"
    "v_mfma_f32_32x32x16_bf16 v[32:47], a[152:155], a[28:31], v[32:47]\n"
    "ds_read_b64_tr_b16 a[136:137], v238 offset:6144\n"
    "ds_read_b64_tr_b16 a[138:139], v238 offset:6656\n"
    "v_mfma_f32_32x32x16_bf16 v[48:63], a[156:159], a[28:31], v[48:63]\n"
    "ds_read_b64_tr_b16 a[140:141], v239 offset:6144\n"
    "ds_read_b64_tr_b16 a[142:143], v239 offset:6656\n"
    "s_add_u32 m0, s75, 32768\n"
    "s_add_u32 s15, s25, 131072\n"
    "buffer_load_dwordx4 v246, s[52:55], s15 offen offset:0 lds\n"
    "s_waitcnt lgkmcnt(8)\n"
    "v_mfma_f32_32x32x16_bf16 v[0:15], a[160:163], a[32:35], v[0:15]\n"
    "ds_read_b64_tr_b16 a[144:145], v238 offset:8192\n"
    "ds_read_b64_tr_b16 a[146:147], v238 offset:8704\n"
    "v_mfma_f32_32x32x16_bf16 v[16:31], a[164:167], a[32:35], v[16:31]\n"
    "ds_read_b64_tr_b16 a[148:149], v239 offset:8192\n"
    "ds_read_b64_tr_b16 a[150:151], v239 offset:8704\n"
    "buffer_load_dwordx4 v247, s[52:55], s15 offen offset:1024 lds\n"
    "v_mfma_f32_32x32x16_bf16 v[32:47], a[168:171], a[32:35], v[32:47]\n"
    "ds_read_b64_tr_b16 a[152:153], v238 offset:10240\n"
    "ds_read_b64_tr_b16 a[154:155], v238 offset:10752\n"
    "v_mfma_f32_32x32x16_bf16 v[48:63], a[172:175], a[32:35], v[48:63]\n"
    "ds_read_b64_tr_b16 a[156:157], v239 offset:10240\n"
    "ds_read_b64_tr_b16 a[158:159], v239 offset:10752\n"
    "s_waitcnt lgkmcnt(8)\n"
    "v_mfma_f32_32x32x16_bf16 v[0:15], a[128:131], a[36:39], v[0:15]\n"
    "ds_read_b64_tr_b16 a[160:161], v238 offset:12288\n"
    "ds_read_b64_tr_b16 a[162:163], v238 offset:12800\n"
    "s_xor_b32 s12, s15, 128\n"
    "s_add_u32 m0, s75, 34816\n"
    "s_nop 0\n"
    "buffer_load_dwordx4 v246, s[52:55], s12 offen offset:0 lds\n"
    "v_mfma_f32_32x32x16_bf16 v[16:31], a[132:135], a[36:39], v[16:31]\n"
    "ds_read_b64_tr_b16 a[164:165], v239 offset:12288\n"
    "ds_read_b64_tr_b16 a[166:167], v239 offset:12800\n"
    "v_mfma_f32_32x32x16_bf16 v[32:47], a[136:139], a[36:39], v[32:47]\n"
    "ds_read_b64_tr_b16 a[168:169], v238 offset:14336\n"
    "ds_read_b64_tr_b16 a[170:171], v238 offset:14848\n"
    "buffer_load_dwordx4 v247, s[52:55], s12 offen offset:1024 lds\n"
    "v_mfma_f32_32x32x16_bf16 v[48:63], a[140:143], a[36:39], v[48:63]\n"
    "ds_read_b64_tr_b16 a[172:173], v239 offset:14336\n"
    "ds_read_b64_tr_b16 a[174:175], v239 offset:14848\n"
    "s_waitcnt vmcnt(27) lgkmcnt(8)\n"
    "s_barrier\n"
    "v_mfma_f32_32x32x16_bf16 v[0:15], a[144:147], a[40:43], v[0:15]\n"
    "ds_read_b64_tr_b16 a[128:129], v238 offset:16384\n"
    "ds_read_b64_tr_b16 a[130:131], v238 offset:16896\n"
    "s_add_u32 m0, s75, 49152\n"
    "s_add_u32 s14, s26, 131072\n"
    "s_lshl_b32 s12, s24, 15\n"
    "s_xor_b32 s14, s14, s12\n"
    "v_mfma_f32_32x32x16_bf16 v[16:31], a[148:151], a[40:43], v[16:31]\n"
    "ds_read_b64_tr_b16 a[132:133], v239 offset:16384\n"
    "ds_read_b64_tr_b16 a[134:135], v239 offset:16896\n"
    "buffer_load_dwordx4 v246, s[52:55], s14 offen offset:0 lds\n"
    "v_mfma_f32_32x32x16_bf16 v[32:47], a[152:155], a[40:43], v[32:47]\n"
    "ds_read_b64_tr_b16 a[136:137], v238 offset:18432\n"
    "ds_read_b64_tr_b16 a[138:139], v238 offset:18944\n"
    "v_mfma_f32_32x32x16_bf16 v[48:63], a[156:159], a[40:43], v[48:63]\n"
    "ds_read_b64_tr_b16 a[140:141], v239 offset:18432\n"
    "ds_read_b64_tr_b16 a[142:143], v239 offset:18944\n"
    "buffer_load_dwordx4 v247, s[52:55], s14 offen offset:1024 lds\n"
    "s_waitcnt lgkmcnt(8)\n"
    "v_mfma_f32_32x32x16_bf16 v[0:15], a[160:163], a[44:47], v[0:15]\n"
    "ds_read_b64_tr_b16 a[144:145], v238 offset:20480\n"
    "ds_read_b64_tr_b16 a[146:147], v238 offset:20992\n"
    "s_add_u32 s18, s88, 0\n"
    "buffer_load_dwordx4 v[96:99], v248, s[60:63], s18 offen offset:512 sc1\n"
    "buffer_load_dwordx4 v[100:103], v248, s[60:63], s18 offen offset:1536 sc1\n"
    "v_mfma_f32_32x32x16_bf16 v[16:31], a[164:167], a[44:47], v[16:31]\n"
    "ds_read_b64_tr_b16 a[148:149], v239 offset:20480\n"
    "ds_read_b64_tr_b16 a[150:151], v239 offset:20992\n"
    "buffer_load_dwordx4 v[104:107], v248, s[60:63], s18 offen offset:2560 sc1\n"
    "buffer_load_dwordx4 v[108:111], v248, s[60:63], s18 offen offset:3584 sc1\n"
    "v_mfma_f32_32x32x16_bf16 v[32:47], a[168:171], a[44:47], v[32:47]\n"
    "ds_read_b64_tr_b16 a[152:153], v238 offset:22528\n"
    "ds_read_b64_tr_b16 a[154:155], v238 offset:23040\n"
    "buffer_load_dword v151, v144, s[60:63], s18 offen offset:0 sc1\n"
    "s_add_u32 s19, s88, 4608\n"
    "buffer_load_dwordx4 v[112:115], v248, s[60:63], s19 offen offset:512 sc1\n"
    "v_mfma_f32_32x32x16_bf16 v[48:63], a[172:175], a[44:47], v[48:63]\n"
    "ds_read_b64_tr_b16 a[156:157], v239 offset:22528\n"
    "ds_read_b64_tr_b16 a[158:159], v239 offset:23040\n"
    "buffer_load_dwordx4 v[116:119], v248, s[60:63], s19 offen offset:1536 sc1\n"
    "buffer_load_dwordx4 v[120:123], v248, s[60:63], s19 offen offset:2560 sc1\n"
    "s_waitcnt lgkmcnt(8)\n"
    "v_mfma_f32_32x32x16_bf16 v[0:15], a[128:131], a[48:51], v[0:15]\n"
    "ds_read_b64_tr_b16 a[160:161], v238 offset:24576\n"
    "ds_read_b64_tr_b16 a[162:163], v238 offset:25088\n"
    "buffer_load_dwordx4 v[124:127], v248, s[60:63], s19 offen offset:3584 sc1\n"
    "buffer_load_dword v152, v144, s[60:63], s19 offen offset:0 sc1\n"
    "v_mfma_f32_32x32x16_bf16 v[16:31], a[132:135], a[48:51], v[16:31]\n"
    "ds_read_b64_tr_b16 a[164:165], v239 offset:24576\n"
    "ds_read_b64_tr_b16 a[166:167], v239 offset:25088\n"
    "v_mfma_f32_32x32x16_bf16 v[32:47], a[136:139], a[48:51], v[32:47]\n"
    "ds_read_b64_tr_b16 a[168:169], v238 offset:26624\n"
    "ds_read_b64_tr_b16 a[170:171], v238 offset:27136\n"
    "v_mfma_f32_32x32x16_bf16 v[48:63], a[140:143], a[48:51], v[48:63]\n"
    "ds_read_b64_tr_b16 a[172:173], v239 offset:26624\n"
    "ds_read_b64_tr_b16 a[174:175], v239 offset:27136\n"
    "s_waitcnt lgkmcnt(8)\n"
    "v_mfma_f32_32x32x16_bf16 v[0:15], a[144:147], a[52:55], v[0:15]\n"
    "ds_read_b64_tr_b16 a[128:129], v238 offset:28672\n"
    "ds_read_b64_tr_b16 a[130:131], v238 offset:29184\n"
    "v_mfma_f32_32x32x16_bf16 v[16:31], a[148:151], a[52:55], v[16:31]\n"
    "ds_read_b64_tr_b16 a[132:133], v239 offset:28672\n"
    "ds_read_b64_tr_b16 a[134:135], v239 offset:29184\n"
    "v_mfma_f32_32x32x16_bf16 v[32:47], a[152:155], a[52:55], v[32:47]\n"
    "ds_read_b64_tr_b16 a[136:137], v238 offset:30720\n"
    "ds_read_b64_tr_b16 a[138:139], v238 offset:31232\n"
    "v_mfma_f32_32x32x16_bf16 v[48:63], a[156:159], a[52:55], v[48:63]\n"
    "ds_read_b64_tr_b16 a[140:141], v239 offset:30720\n"
    "ds_read_b64_tr_b16 a[142:143], v239 offset:31232\n"
    "s_xor_b32 s15, s14, 128\n"
    "s_add_u32 m0, s75, 51200\n"
    "s_nop 0\n"
    "buffer_load_dwordx4 v246, s[52:55], s15 offen offset:0 lds\n"
    "s_waitcnt vmcnt(36) lgkmcnt(8)\n"
    "s_barrier\n"
    "v_mfma_f32_32x32x16_bf16 v[0:15], a[160:163], a[56:59], v[0:15]\n"
    "ds_read_b64_tr_b16 a[144:145], v238 offset:32768\n"
    "ds_read_b64_tr_b16 a[146:147], v238 offset:33280\n"
    "v_mfma_f32_32x32x16_bf16 v[16:31], a[164:167], a[56:59], v[16:31]\n"
    "ds_read_b64_tr_b16 a[148:149], v239 offset:32768\n"
    "ds_read_b64_tr_b16 a[150:151], v239 offset:33280\n"
    "buffer_load_dwordx4 v247, s[52:55], s15 offen offset:1024 lds\n"
    "v_mfma_f32_32x32x16_bf16 v[32:47], a[168:171], a[56:59], v[32:47]\n"
    "ds_read_b64_tr_b16 a[152:153], v238 offset:34816\n"
    "ds_read_b64_tr_b16 a[154:155], v238 offset:35328\n"
    "v_mfma_f32_32x32x16_bf16 v[48:63], a[172:175], a[56:59], v[48:63]\n"
    "ds_read_b64_tr_b16 a[156:157], v239 offset:34816\n"
    "ds_read_b64_tr_b16 a[158:159], v239 offset:35328\n"
    "s_waitcnt lgkmcnt(8)\n"
    "v_mfma_f32_32x32x16_bf16 v[0:15], a[128:131], a[60:63], v[0:15]\n"
    "ds_read_b64_tr_b16 a[160:161], v238 offset:36864\n"
    "ds_read_b64_tr_b16 a[162:163], v238 offset:37376\n"
    "s_add_u32 m0, s75, 65536\n"
    "s_lshl_b32 s15, s24, 16\n"
    "s_add_u32 s13, s26, s15\n"
    "buffer_load_dwordx4 v246, s[52:55], s13 offen offset:0 lds\n"
    "v_mfma_f32_32x32x16_bf16 v[16:31], a[132:135], a[60:63], v[16:31]\n"
    "ds_read_b64_tr_b16 a[164:165], v239 offset:36864\n"
    "ds_read_b64_tr_b16 a[166:167], v239 offset:37376\n"
    "v_mfma_f32_32x32x16_bf16 v[32:47], a[136:139], a[60:63], v[32:47]\n"
    "ds_read_b64_tr_b16 a[168:169], v238 offset:38912\n"
    "ds_read_b64_tr_b16 a[170:171], v238 offset:39424\n"
    "buffer_load_dwordx4 v247, s[52:55], s13 offen offset:1024 lds\n"
    "v_mfma_f32_32x32x16_bf16 v[48:63], a[140:143], a[60:63], v[48:63]\n"
    "ds_read_b64_tr_b16 a[172:173], v239 offset:38912\n"
    "ds_read_b64_tr_b16 a[174:175], v239 offset:39424\n"
    "s_waitcnt lgkmcnt(8)\n"
    "v_mfma_f32_32x32x16_bf16 v[0:15], a[144:147], a[64:67], v[0:15]\n"
    "ds_read_b64_tr_b16 a[128:129], v238 offset:40960\n"
    "ds_read_b64_tr_b16 a[130:131], v238 offset:41472\n"
    "s_xor_b32 s14, s13, 128\n"
    "s_add_u32 m0, s75, 67584\n"
    "s_nop 0\n"
    "buffer_load_dwordx4 v246, s[52:55], s14 offen offset:0 lds\n"
    "v_mfma_f32_32x32x16_bf16 v[16:31], a[148:151], a[64:67], v[16:31]\n"
    "ds_read_b64_tr_b16 a[132:133], v239 offset:40960\n"
    "ds_read_b64_tr_b16 a[134:135], v239 offset:41472\n"
    "v_mfma_f32_32x32x16_bf16 v[32:47], a[152:155], a[64:67], v[32:47]\n"
    "ds_read_b64_tr_b16 a[136:137], v238 offset:43008\n"
    "ds_read_b64_tr_b16 a[138:139], v238 offset:43520\n"
    "buffer_load_dwordx4 v247, s[52:55], s14 offen offset:1024 lds\n"
    "v_mfma_f32_32x32x16_bf16 v[48:63], a[156:159], a[64:67], v[48:63]\n"
    "ds_read_b64_tr_b16 a[140:141], v239 offset:43008\n"
    "ds_read_b64_tr_b16 a[142:143], v239 offset:43520\n"
    "s_waitcnt lgkmcnt(8)\n"
    "v_mfma_f32_32x32x16_bf16 v[0:15], a[160:163], a[68:71], v[0:15]\n"
    "ds_read_b64_tr_b16 a[144:145], v238 offset:45056\n"
    "ds_read_b64_tr_b16 a[146:147], v238 offset:45568\n"
    "v_mfma_f32_32x32x16_bf16 v[16:31], a[164:167], a[68:71], v[16:31]\n"
    "ds_read_b64_tr_b16 a[148:149], v239 offset:45056\n"
    "ds_read_b64_tr_b16 a[150:151], v239 offset:45568\n"
    "v_mfma_f32_32x32x16_bf16 v[32:47], a[168:171], a[68:71], v[32:47]\n"
    "ds_read_b64_tr_b16 a[152:153], v238 offset:47104\n"
    "ds_read_b64_tr_b16 a[154:155], v238 offset:47616\n"
    "v_mfma_f32_32x32x16_bf16 v[48:63], a[172:175], a[68:71], v[48:63]\n"
    "ds_read_b64_tr_b16 a[156:157], v239 offset:47104\n"
    "ds_read_b64_tr_b16 a[158:159], v239 offset:47616\n"
    "s_waitcnt vmcnt(37) lgkmcnt(8)\n"
    "s_barrier\n"
    "v_mfma_f32_32x32x16_bf16 v[0:15], a[128:131], a[72:75], v[0:15]\n"
    "ds_read_b64_tr_b16 a[160:161], v238 offset:49152\n"
    "ds_read_b64_tr_b16 a[162:163], v238 offset:49664\n"
    "s_add_u32 m0, s75, 81920\n"
    "s_nop 0\n"
    "buffer_load_dwordx4 v246, s[56:59], s27 offen offset:0 lds\n"
    "v_mfma_f32_32x32x16_bf16 v[16:31], a[132:135], a[72:75], v[16:31]\n"
    "ds_read_b64_tr_b16 a[164:165], v239 offset:49152\n"
    "ds_read_b64_tr_b16 a[166:167], v239 offset:49664\n"
    "v_mfma_f32_32x32x16_bf16 v[32:47], a[136:139], a[72:75], v[32:47]\n"
    "ds_read_b64_tr_b16 a[168:169], v238 offset:51200\n"
    "ds_read_b64_tr_b16 a[170:171], v238 offset:51712\n"
    "buffer_load_dwordx4 v247, s[56:59], s27 offen offset:1024 lds\n"
    "v_mfma_f32_32x32x16_bf16 v[48:63], a[140:143], a[72:75], v[48:63]\n"
    "ds_read_b64_tr_b16 a[172:173], v239 offset:51200\n"
    "ds_read_b64_tr_b16 a[174:175], v239 offset:51712\n"
    "s_waitcnt lgkmcnt(8)\n"
    "v_mfma_f32_32x32x16_bf16 v[0:15], a[144:147], a[76:79], v[0:15]\n"
    "ds_read_b64_tr_b16 a[128:129], v238 offset:53248\n"
    "ds_read_b64_tr_b16 a[130:131], v238 offset:53760\n"
    "s_xor_b32 s13, s27, 128\n"
    "s_add_u32 m0, s75, 83968\n"
    "s_nop 0\n"
    "buffer_load_dwordx4 v246, s[56:59], s13 offen offset:0 lds\n"
    "v_mfma_f32_32x32x16_bf16 v[16:31], a[148:151], a[76:79], v[16:31]\n"
    "ds_read_b64_tr_b16 a[132:133], v239 offset:53248\n"
    "ds_read_b64_tr_b16 a[134:135], v239 offset:53760\n"
    "s_waitcnt vmcnt(18)\n"
    "v_add3_u32 v154, v96, v97, s68\n"
    "v_add3_u32 v154, v154, v98, v99\n"
    "s_waitcnt vmcnt(17)\n"
    "v_add3_u32 v154, v154, v100, v101\n"
    "v_add3_u32 v154, v154, v102, v103\n"
    "v_mfma_f32_32x32x16_bf16 v[32:47], a[152:155], a[76:79], v[32:47]\n"
    "ds_read_b64_tr_b16 a[136:137], v238 offset:55296\n"
    "ds_read_b64_tr_b16 a[138:139], v238 offset:55808\n"
    "buffer_load_dwordx4 v247, s[56:59], s13 offen offset:1024 lds\n"
    "s_waitcnt vmcnt(17)\n"
    "v_add3_u32 v154, v154, v104, v105\n"
    "v_mfma_f32_32x32x16_bf16 v[48:63], a[156:159], a[76:79], v[48:63]\n"
    "ds_read_b64_tr_b16 a[140:141], v239 offset:55296\n"
    "ds_read_b64_tr_b16 a[142:143], v239 offset:55808\n"
    "v_add3_u32 v154, v154, v106, v107\n"
    "s_waitcnt vmcnt(16)\n"
    "v_add3_u32 v154, v154, v108, v109\n"
    "v_add3_u32 v154, v154, v110, v111\n"
    "s_waitcnt vmcnt(14)\n"
    "v_add3_u32 v154, v154, v112, v113\n"
    "s_waitcnt lgkmcnt(8)\n"
    "v_mfma_f32_32x32x16_bf16 v[0:15], a[160:163], a[80:83], v[0:15]\n"
    "ds_read_b64_tr_b16 a[144:145], v238 offset:57344\n"
    "ds_read_b64_tr_b16 a[146:147], v238 offset:57856\n"
    "v_add3_u32 v154, v154, v114, v115\n"
    "s_waitcnt vmcnt(13)\n"
    "v_add3_u32 v154, v154, v116, v117\n"
    "v_add3_u32 v154, v154, v118, v119\n"
    "s_waitcnt vmcnt(12)\n"
    "v_add3_u32 v154, v154, v120, v121\n"
    "v_mfma_f32_32x32x16_bf16 v[16:31], a[164:167], a[80:83], v[16:31]\n"
    "ds_read_b64_tr_b16 a[148:149], v239 offset:57344\n"
    "ds_read_b64_tr_b16 a[150:151], v239 offset:57856\n"
    "v_add3_u32 v154, v154, v122, v123\n"
    "s_waitcnt vmcnt(11)\n"
    "v_add3_u32 v154, v154, v124, v125\n"
    "v_add3_u32 v154, v154, v126, v127\n"
    "s_waitcnt vmcnt(10)\n"
    "v_add_u32 v151, v151, v152\n"
    "v_mfma_f32_32x32x16_bf16 v[32:47], a[168:171], a[80:83], v[32:47]\n"
    "ds_read_b64_tr_b16 a[152:153], v238 offset:59392\n"
    "ds_read_b64_tr_b16 a[154:155], v238 offset:59904\n"
    "v_cmp_ne_u32 vcc, v154, v151\n"
    "s_nop 0\n"
    "s_cbranch_vccnz LreB1\n"
    "LokB1:\n"
    "s_lshl_b32 s18, s24, 11\n"
    "v_mfma_f32_32x32x16_bf16 v[48:63], a[172:175], a[80:83], v[48:63]\n"
    "ds_read_b64_tr_b16 a[156:157], v239 offset:59392\n"
    "ds_read_b64_tr_b16 a[158:159], v239 offset:59904\n"
    "s_sub_u32 s18, s88, s18\n"
    "s_add_u32 s18, s18, 11264\n"
    "buffer_load_dwordx4 v[68:71], v248, s[60:63], s18 offen offset:512 sc1\n"
    "s_waitcnt lgkmcnt(8)\n"
    "v_mfma_f32_32x32x16_bf16 v[0:15], a[128:131], a[84:87], v[0:15]\n"
    "ds_read_b64_tr_b16 a[160:161], v238 offset:61440\n"
    "ds_read_b64_tr_b16 a[162:163], v238 offset:61952\n"
    "v_mfma_f32_32x32x16_bf16 v[16:31], a[132:135], a[84:87], v[16:31]\n"
    "ds_read_b64_tr_b16 a[164:165], v239 offset:61440\n"
    "ds_read_b64_tr_b16 a[166:167], v239 offset:61952\n"
    "buffer_load_dwordx4 v[76:79], v248, s[60:63], s18 offen offset:1536 sc1\n"
    "s_lshl_b32 s19, s24, 8\n"
    "s_sub_u32 s19, s88, s19\n"
    "v_mfma_f32_32x32x16_bf16 v[32:47], a[136:139], a[84:87], v[32:47]\n"
    "ds_read_b64_tr_b16 a[168:169], v238 offset:63488\n"
    "ds_read_b64_tr_b16 a[170:171], v238 offset:64000\n"
    "s_add_u32 s19, s19, 9472\n"
    "buffer_load_dword v151, v144, s[60:63], s19 offen offset:0 sc1\n"
    "v_mfma_f32_32x32x16_bf16 v[48:63], a[140:143], a[84:87], v[48:63]\n"
    "ds_read_b64_tr_b16 a[172:173], v239 offset:63488\n"
    "ds_read_b64_tr_b16 a[174:175], v239 offset:64000\n"
    "s_waitcnt lgkmcnt(8)\n"
    "s_barrier\n"
    "v_mfma_f32_32x32x16_bf16 v[0:15], a[144:147], a[88:91], v[0:15]\n"
    "ds_read_b64_tr_b16 a[128:129], v240 offset:0\n"
    "ds_read_b64_tr_b16 a[130:131], v240 offset:512\n"
    "s_add_u32 m0, s75, 98304\n"
    "s_add_u32 s15, s27, 65536\n"
    "buffer_load_dwordx4 v246, s[56:59], s15 offen offset:0 lds\n"
    "v_mfma_f32_32x32x16_bf16 v[16:31], a[148:151], a[88:91], v[16:31]\n"
    "ds_read_b64_tr_b16 a[132:133], v241 offset:0\n"
    "ds_read_b64_tr_b16 a[134:135], v241 offset:512\n"
    "v_mfma_f32_32x32x16_bf16 v[32:47], a[152:155], a[88:91], v[32:47]\n"
    "ds_read_b64_tr_b16 a[136:137], v240 offset:2048\n"
    "ds_read_b64_tr_b16 a[138:139], v240 offset:2560\n"
    "buffer_load_dwordx4 v247, s[56:59], s15 offen offset:1024 lds\n"
    "v_mfma_f32_32x32x16_bf16 v[48:63], a[156:159], a[88:91], v[48:63]\n"
    "ds_read_b64_tr_b16 a[140:141], v241 offset:2048\n"
    "ds_read_b64_tr_b16 a[142:143], v241 offset:2560\n"
    "s_waitcnt lgkmcnt(8)\n"
    "v_mfma_f32_32x32x16_bf16 v[0:15], a[160:163], a[92:95], v[0:15]\n"
    "ds_read_b64_tr_b16 a[144:145], v240 offset:4096\n"
    "ds_read_b64_tr_b16 a[146:147], v240 offset:4608\n"
    "s_xor_b32 s12, s15, 128\n"
    "s_add_u32 m0, s75, 100352\n"
    "s_nop 0\n"
    "buffer_load_dwordx4 v246, s[56:59], s12 offen offset:0 lds\n"
    "v_mfma_f32_32x32x16_bf16 v[16:31], a[164:167], a[92:95], v[16:31]\n"
    "ds_read_b64_tr_b16 a[148:149], v241 offset:4096\n"
    "ds_read_b64_tr_b16 a[150:151], v241 offset:4608\n"
    "v_mfma_f32_32x32x16_bf16 v[32:47], a[168:171], a[92:95], v[32:47]\n"
    "ds_read_b64_tr_b16 a[152:153], v240 offset:6144\n"
    "ds_read_b64_tr_b16 a[154:155], v240 offset:6656\n"
    "buffer_load_dwordx4 v247, s[56:59], s12 offen offset:1024 lds\n"
    "v_mfma_f32_32x32x16_bf16 v[48:63], a[172:175], a[92:95], v[48:63]\n"
    "ds_read_b64_tr_b16 a[156:157], v241 offset:6144\n"
    "ds_read_b64_tr_b16 a[158:159], v241 offset:6656\n"
    "s_waitcnt lgkmcnt(8)\n"
    "v_mfma_f32_32x32x16_bf16 v[0:15], a[128:131], a[96:99], v[0:15]\n"
    "ds_read_b64_tr_b16 a[160:161], v240 offset:8192\n"
    "ds_read_b64_tr_b16 a[162:163], v240 offset:8704\n"
    "v_mfma_f32_32x32x16_bf16 v[16:31], a[132:135], a[96:99], v[16:31]\n"
    "ds_read_b64_tr_b16 a[164:165], v241 offset:8192\n"
    "ds_read_b64_tr_b16 a[166:167], v241 offset:8704\n"
    "v_mfma_f32_32x32x16_bf16 v[32:47], a[136:139], a[96:99], v[32:47]\n"
    "ds_read_b64_tr_b16 a[168:169], v240 offset:10240\n"
    "ds_read_b64_tr_b16 a[170:171], v240 offset:10752\n"
    "v_mfma_f32_32x32x16_bf16 v[48:63], a[140:143], a[96:99], v[48:63]\n"
    "ds_read_b64_tr_b16 a[172:173], v241 offset:10240\n"
    "ds_read_b64_tr_b16 a[174:175], v241 offset:10752\n"
    "s_waitcnt lgkmcnt(8)\n"
    "v_mfma_f32_32x32x16_bf16 v[0:15], a[144:147], a[100:103], v[0:15]\n"
    "ds_read_b64_tr_b16 a[128:129], v240 offset:12288\n"
    "ds_read_b64_tr_b16 a[130:131], v240 offset:12800\n"
    "v_mfma_f32_32x32x16_bf16 v[16:31], a[148:151], a[100:103], v[16:31]\n"
    "ds_read_b64_tr_b16 a[132:133], v241 offset:12288\n"
    "ds_read_b64_tr_b16 a[134:135], v241 offset:12800\n"
    "v_mfma_f32_32x32x16_bf16 v[32:47], a[152:155], a[100:103], v[32:47]\n"
    "ds_read_b64_tr_b16 a[136:137], v240 offset:14336\n"
    "ds_read_b64_tr_b16 a[138:139], v240 offset:14848\n"
    "v_mfma_f32_32x32x16_bf16 v[48:63], a[156:159], a[100:103], v[48:63]\n"
    "ds_read_b64_tr_b16 a[140:141], v241 offset:14336\n"
    "ds_read_b64_tr_b16 a[142:143], v241 offset:14848\n"
    "s_waitcnt lgkmcnt(8)\n"
    "s_barrier\n"
    "v_mfma_f32_32x32x16_bf16 v[0:15], a[160:163], a[104:107], v[0:15]\n"
    "ds_read_b64_tr_b16 a[144:145], v236 offset:0\n"
    "ds_read_b64_tr_b16 a[146:147], v236 offset:512\n"
    "s_add_u32 m0, s75, 114688\n"
    "s_add_u32 s14, s26, 65536\n"
    "s_lshl_b32 s12, s24, 16\n"
    "s_sub_u32 s14, s14, s12\n"
    "v_mfma_f32_32x32x16_bf16 v[16:31], a[164:167], a[104:107], v[16:31]\n"
    "ds_read_b64_tr_b16 a[148:149], v237 offset:0\n"
    "ds_read_b64_tr_b16 a[150:151], v237 offset:512\n"
    "buffer_load_dwordx4 v246, s[52:55], s14 offen offset:0 lds\n"
    "v_mfma_f32_32x32x16_bf16 v[32:47], a[168:171], a[104:107], v[32:47]\n"
    "ds_read_b64_tr_b16 a[152:153], v236 offset:2048\n"
    "ds_read_b64_tr_b16 a[154:155], v236 offset:2560\n"
    "v_mfma_f32_32x32x16_bf16 v[48:63], a[172:175], a[104:107], v[48:63]\n"
    "ds_read_b64_tr_b16 a[156:157], v237 offset:2048\n"
    "ds_read_b64_tr_b16 a[158:159], v237 offset:2560\n"
    "buffer_load_dwordx4 v247, s[52:55], s14 offen offset:1024 lds\n"
    "s_waitcnt lgkmcnt(8)\n"
    "v_mfma_f32_32x32x16_bf16 v[0:15], a[128:131], a[108:111], v[0:15]\n"
    "ds_read_b64_tr_b16 a[160:161], v236 offset:4096\n"
    "ds_read_b64_tr_b16 a[162:163], v236 offset:4608\n"
    "v_mfma_f32_32x32x16_bf16 v[16:31], a[132:135], a[108:111], v[16:31]\n"
    "ds_read_b64_tr_b16 a[164:165], v237 offset:4096\n"
    "ds_read_b64_tr_b16 a[166:167], v237 offset:4608\n"
    "s_xor_b32 s15, s14, 128\n"
    "s_add_u32 m0, s75, 116736\n"
    "s_nop 0\n"
    "buffer_load_dwordx4 v246, s[52:55], s15 offen offset:0 lds\n"
    "v_mfma_f32_32x32x16_bf16 v[32:47], a[136:139], a[108:111], v[32:47]\n"
    "ds_read_b64_tr_b16 a[168:169], v236 offset:6144\n"
    "ds_read_b64_tr_b16 a[170:171], v236 offset:6656\n"
    "v_mfma_f32_32x32x16_bf16 v[48:63], a[140:143], a[108:111], v[48:63]\n"
    "ds_read_b64_tr_b16 a[172:173], v237 offset:6144\n"
    "ds_read_b64_tr_b16 a[174:175], v237 offset:6656\n"
    "buffer_load_dwordx4 v247, s[52:55], s15 offen offset:1024 lds\n"
    "s_waitcnt lgkmcnt(8)\n"
    "v_mfma_f32_32x32x16_bf16 v[0:15], a[144:147], a[112:115], v[0:15]\n"
    "ds_read_b64_tr_b16 a[128:129], v236 offset:8192\n"
    "ds_read_b64_tr_b16 a[130:131], v236 offset:8704\n"
    "v_mfma_f32_32x32x16_bf16 v[16:31], a[148:151], a[112:115], v[16:31]\n"
    "ds_read_b64_tr_b16 a[132:133], v237 offset:8192\n"
    "ds_read_b64_tr_b16 a[134:135], v237 offset:8704\n"
    "v_mfma_f32_32x32x16_bf16 v[32:47], a[152:155], a[112:115], v[32:47]\n"
    "ds_read_b64_tr_b16 a[136:137], v236 offset:10240\n"
    "ds_read_b64_tr_b16 a[138:139], v236 offset:10752\n"
    "s_waitcnt vmcnt(10)\n"
    "v_add3_u32 v154, v68, v69, s72\n"
    "v_add3_u32 v154, v154, v70, v71\n"
    "s_waitcnt vmcnt(9)\n"
    "v_add3_u32 v154, v154, v76, v77\n"
    "v_add3_u32 v154, v154, v78, v79\n"
    "v_mfma_f32_32x32x16_bf16 v[48:63], a[156:159], a[112:115], v[48:63]\n"
    "ds_read_b64_tr_b16 a[140:141], v237 offset:10240\n"
    "ds_read_b64_tr_b16 a[142:143], v237 offset:10752\n"
    "s_waitcnt vmcnt(8)\n"
    "v_cmp_ne_u32 vcc, v154, v151\n"
    "s_nop 0\n"
    "s_cbranch_vccnz LreB2\n"
    "LokB2:\n"
    "s_waitcnt lgkmcnt(8)\n"
    "v_mfma_f32_32x32x16_bf16 v[0:15], a[160:163], a[116:119], v[0:15]\n"
    "ds_read_b64_tr_b16 a[144:145], v236 offset:12288\n"
    "ds_read_b64_tr_b16 a[146:147], v236 offset:12800\n"
    "v_mfma_f32_32x32x16_bf16 v[16:31], a[164:167], a[116:119], v[16:31]\n"
    "ds_read_b64_tr_b16 a[148:149], v237 offset:12288\n"
    "ds_read_b64_tr_b16 a[150:151], v237 offset:12800\n"
    "v_mfma_f32_32x32x16_bf16 v[32:47], a[168:171], a[116:119], v[32:47]\n"
    "ds_read_b64_tr_b16 a[152:153], v236 offset:14336\n"
    "ds_read_b64_tr_b16 a[154:155], v236 offset:14848\n"
    "v_mfma_f32_32x32x16_bf16 v[48:63], a[172:175], a[116:119], v[48:63]\n"
    "ds_read_b64_tr_b16 a[156:157], v237 offset:14336\n"
    "ds_read_b64_tr_b16 a[158:159], v237 offset:14848\n"
    "s_waitcnt lgkmcnt(8)\n"
    "s_barrier\n"
    "v_mfma_f32_32x32x16_bf16 v[0:15], a[128:131], a[120:123], v[0:15]\n"
    "ds_read_b64_tr_b16 a[160:161], v146 offset:16384\n"
    "ds_read_b64_tr_b16 a[162:163], v146 offset:17408\n"
    "s_add_u32 m0, s75, 131072\n"
    "s_add_u32 s13, s25, 65536\n"
    "buffer_load_dwordx4 v246, s[52:55], s13 offen offset:0 lds\n"
    "v_mfma_f32_32x32x16_bf16 v[16:31], a[132:135], a[120:123], v[16:31]\n"
    "ds_read_b64_tr_b16 a[164:165], v145 offset:18432\n"
    "ds_read_b64_tr_b16 a[166:167], v145 offset:19456\n"
    "v_mfma_f32_32x32x16_bf16 v[32:47], a[136:139], a[120:123], v[32:47]\n"
    "ds_read_b64_tr_b16 a[168:169], v146 offset:20480\n"
    "ds_read_b64_tr_b16 a[170:171], v146 offset:21504\n"
    "buffer_load_dwordx4 v247, s[52:55], s13 offen offset:1024 lds\n"
    "v_mfma_f32_32x32x16_bf16 v[48:63], a[140:143], a[120:123], v[48:63]\n"
    "ds_read_b64_tr_b16 a[172:173], v145 offset:22528\n"
    "ds_read_b64_tr_b16 a[174:175], v145 offset:23552\n"
    "s_waitcnt lgkmcnt(8)\n"
    "v_mfma_f32_32x32x16_bf16 v[0:15], a[144:147], a[124:127], v[0:15]\n"
    "ds_read_b64_tr_b16 a[128:129], v146 offset:24576\n"
    "ds_read_b64_tr_b16 a[130:131], v146 offset:25600\n"
    "s_xor_b32 s14, s13, 128\n"
    "s_add_u32 m0, s75, 133120\n"
    "s_nop 0\n"
    "buffer_load_dwordx4 v246, s[52:55], s14 offen offset:0 lds\n"
    "v_mfma_f32_32x32x16_bf16 v[16:31], a[148:151], a[124:127], v[16:31]\n"
    "ds_read_b64_tr_b16 a[132:133], v145 offset:26624\n"
    "ds_read_b64_tr_b16 a[134:135], v145 offset:27648\n"
    "v_mfma_f32_32x32x16_bf16 v[32:47], a[152:155], a[124:127], v[32:47]\n"
    "ds_read_b64_tr_b16 a[136:137], v146 offset:28672\n"
    "ds_read_b64_tr_b16 a[138:139], v146 offset:29696\n"
    "buffer_load_dwordx4 v247, s[52:55], s14 offen offset:1024 lds\n"
    "v_mfma_f32_32x32x16_bf16 v[48:63], a[156:159], a[124:127], v[48:63]\n"
    "ds_read_b64_tr_b16 a[140:141], v145 offset:30720\n"
    "ds_read_b64_tr_b16 a[142:143], v145 offset:31744\n"
    "s_waitcnt lgkmcnt(8)\n"
    "s_barrier\n"
    "v_mfma_f32_32x32x16_bf16 v[156:171], a[160:163], v[96:99], 0\n"
    "ds_read_b64_tr_b16 a[144:145], v146 offset:32768\n"
    "ds_read_b64_tr_b16 a[146:147], v146 offset:33792\n"
    "s_add_u32 m0, s75, 0\n"
    "s_add_u32 s12, s25, 196608\n"
    "buffer_load_dwordx4 v246, s[52:55], s12 offen offset:0 lds\n"
    "s_add_u32 s76, s89, 32256\n"
    "v_pk_mul_f32 v[0:1], v[0:1], v[32:33]\n"
    "v_pk_mul_f32 v[2:3], v[2:3], v[34:35]\n"
    "v_pk_mul_f32 v[4:5], v[4:5], v[36:37]\n"
    "v_mfma_f32_32x32x16_bf16 v[220:235], a[164:167], v[96:99], 0\n"
    "ds_read_b64_tr_b16 a[148:149], v145 offset:34816\n"
    "ds_read_b64_tr_b16 a[150:151], v145 offset:35840\n"
    "v_pk_mul_f32 v[6:7], v[6:7], v[38:39]\n"
    "v_pk_mul_f32 v[8:9], v[8:9], v[40:41]\n"
    "v_pk_mul_f32 v[10:11], v[10:11], v[42:43]\n"
    "v_pk_mul_f32 v[12:13], v[12:13], v[44:45]\n"
    "v_pk_mul_f32 v[14:15], v[14:15], v[46:47]\n"
    "v_pk_mul_f32 v[16:17], v[16:17], v[48:49]\n"
    "v_pk_mul_f32 v[18:19], v[18:19], v[50:51]\n"
    "v_pk_mul_f32 v[20:21], v[20:21], v[52:53]\n"
    "v_pk_mul_f32 v[22:23], v[22:23], v[54:55]\n"
    "v_mfma_f32_32x32x16_bf16 v[156:171], a[168:171], v[100:103], v[156:171]\n"
    "ds_read_b64_tr_b16 a[152:153], v146 offset:36864\n"
    "ds_read_b64_tr_b16 a[154:155], v146 offset:37888\n"
    "v_pk_mul_f32 v[24:25], v[24:25], v[56:57]\n"
    "v_pk_mul_f32 v[26:27], v[26:27], v[58:59]\n"
    "v_pk_mul_f32 v[28:29], v[28:29], v[60:61]\n"
    "v_pk_mul_f32 v[30:31], v[30:31], v[62:63]\n"
    "v_pk_mul_f32 v[0:1], v[0:1], v[250:251] op_sel_hi:[1,0]\n"
    "v_pk_mul_f32 v[2:3], v[2:3], v[250:251] op_sel_hi:[1,0]\n"
    "v_pk_mul_f32 v[4:5], v[4:5], v[250:251] op_sel_hi:[1,0]\n"
    "v_pk_mul_f32 v[6:7], v[6:7], v[250:251] op_sel_hi:[1,0]\n"
    "v_cvt_pk_bf16_f32 v0, v0, v1\n"
    "v_mfma_f32_32x32x16_bf16 v[220:235], a[172:175], v[100:103], v[220:235]\n"
    "ds_read_b64_tr_b16 a[156:157], v145 offset:38912\n"
    "ds_read_b64_tr_b16 a[158:159], v145 offset:39936\n"
    "buffer_load_dwordx4 v247, s[52:55], s12 offen offset:1024 lds\n"
    "v_cvt_pk_bf16_f32 v1, v2, v3\n"
    "v_cvt_pk_bf16_f32 v2, v4, v5\n"
    "v_cvt_pk_bf16_f32 v3, v6, v7\n"
    "v_mov_b32 v5, s72\n"
    "buffer_store_dword v5, off, s[60:63], s1 offset:32 sc1\n"
    "s_waitcnt lgkmcnt(8)\n"
    "v_mfma_f32_32x32x16_bf16 v[156:171], a[128:131], v[104:107], v[156:171]\n"
    "ds_read_b64_tr_b16 a[160:161], v146 offset:40960\n"
    "ds_read_b64_tr_b16 a[162:163], v146 offset:41984\n"
    "buffer_store_dwordx4 v[0:3], v248, s[60:63], s76 offen offset:512 sc1\n"
    "v_add3_u32 v4, v0, v1, s72\n"
    "v_add3_u32 v4, v4, v2, v3\n"
    "v_pk_mul_f32 v[8:9], v[8:9], v[250:251] op_sel_hi:[1,0]\n"
    "v_pk_mul_f32 v[10:11], v[10:11], v[250:251] op_sel_hi:[1,0]\n"
    "v_pk_mul_f32 v[12:13], v[12:13], v[250:251] op_sel_hi:[1,0]\n"
    "v_pk_mul_f32 v[14:15], v[14:15], v[250:251] op_sel_hi:[1,0]\n"
    "v_mfma_f32_32x32x16_bf16 v[220:235], a[132:135], v[104:107], v[220:235]\n"
    "ds_read_b64_tr_b16 a[164:165], v145 offset:43008\n"
    "ds_read_b64_tr_b16 a[166:167], v145 offset:44032\n"
    "v_cvt_pk_bf16_f32 v8, v8, v9\n"
    "v_cvt_pk_bf16_f32 v9, v10, v11\n"
    "v_cvt_pk_bf16_f32 v10, v12, v13\n"
    "v_cvt_pk_bf16_f32 v11, v14, v15\n"
    "buffer_store_dwordx4 v[8:11], v248, s[60:63], s76 offen offset:1536 sc1\n"
    "v_add3_u32 v4, v4, v8, v9\n"
    "v_add3_u32 v4, v4, v10, v11\n"
    "v_mfma_f32_32x32x16_bf16 v[156:171], a[136:139], v[108:111], v[156:171]\n"
    "ds_read_b64_tr_b16 a[168:169], v146 offset:45056\n"
    "ds_read_b64_tr_b16 a[170:171], v146 offset:46080\n"
    "v_pk_mul_f32 v[16:17], v[16:17], v[250:251] op_sel_hi:[1,0]\n"
    "v_pk_mul_f32 v[18:19], v[18:19], v[250:251] op_sel_hi:[1,0]\n"
    "v_pk_mul_f32 v[20:21], v[20:21], v[250:251] op_sel_hi:[1,0]\n"
    "v_pk_mul_f32 v[22:23], v[22:23], v[250:251] op_sel_hi:[1,0]\n"
    "v_cvt_pk_bf16_f32 v16, v16, v17\n"
    "v_cvt_pk_bf16_f32 v17, v18, v19\n"
    "v_cvt_pk_bf16_f32 v18, v20, v21\n"
    "v_cvt_pk_bf16_f32 v19, v22, v23\n"
    "buffer_store_dwordx4 v[16:19], v248, s[60:63], s76 offen offset:2560 sc1\n"
    "v_mfma_f32_32x32x16_bf16 v[220:235], a[140:143], v[108:111], v[220:235]\n"
    "ds_read_b64_tr_b16 a[172:173], v145 offset:47104\n"
    "ds_read_b64_tr_b16 a[174:175], v145 offset:48128\n"
    "v_add3_u32 v4, v4, v16, v17\n"
    "v_add3_u32 v4, v4, v18, v19\n"
    "v_pk_mul_f32 v[24:25], v[24:25], v[250:251] op_sel_hi:[1,0]\n"
    "v_pk_mul_f32 v[26:27], v[26:27], v[250:251] op_sel_hi:[1,0]\n"
    "v_pk_mul_f32 v[28:29], v[28:29], v[250:251] op_sel_hi:[1,0]\n"
    "v_pk_mul_f32 v[30:31], v[30:31], v[250:251] op_sel_hi:[1,0]\n"
    "v_cvt_pk_bf16_f32 v24, v24, v25\n"
    "v_cvt_pk_bf16_f32 v25, v26, v27\n"
    "v_cvt_pk_bf16_f32 v26, v28, v29\n"
    "s_waitcnt lgkmcnt(8)\n"
    "v_mfma_f32_32x32x16_bf16 v[156:171], a[144:147], v[112:115], v[156:171]\n"
    "ds_read_b64_tr_b16 a[128:129], v146 offset:49152\n"
    "ds_read_b64_tr_b16 a[130:131], v146 offset:50176\n"
    "v_and_b32 v245, 0x7c, v144\n"
    "buffer_load_dword v245, v245, s[60:63], s0 offen sc1\n"
    "v_cvt_pk_bf16_f32 v27, v30, v31\n"
    "buffer_store_dwordx4 v[24:27], v248, s[60:63], s76 offen offset:3584 sc1\n"
    "v_add3_u32 v4, v4, v24, v25\n"
    "v_add3_u32 v4, v4, v26, v27\n"
    "v_mfma_f32_32x32x16_bf16 v[220:235], a[148:151], v[112:115], v[220:235]\n"
    "ds_read_b64_tr_b16 a[132:133], v145 offset:51200\n"
    "ds_read_b64_tr_b16 a[134:135], v145 offset:52224\n"
    "buffer_store_dword v4, v144, s[60:63], s76 offen offset:0 sc1\n"
    "v_and_b32 v242, 0xf0, v248\n"
    "v_lshlrev_b32 v242, 6, v242\n"
    "v_and_b32 v243, 0x300, v248\n"
    "v_lshrrev_b32 v243, 4, v243\n"
    "v_or_b32 v242, v242, v243\n"
    "v_mfma_f32_32x32x16_bf16 v[156:171], a[152:155], v[116:119], v[156:171]\n"
    "ds_read_b64_tr_b16 a[136:137], v146 offset:53248\n"
    "ds_read_b64_tr_b16 a[138:139], v146 offset:54272\n"
    "v_mfma_f32_32x32x16_bf16 v[220:235], a[156:159], v[116:119], v[220:235]\n"
    "ds_read_b64_tr_b16 a[140:141], v145 offset:55296\n"
    "ds_read_b64_tr_b16 a[142:143], v145 offset:56320\n"
    "s_waitcnt lgkmcnt(8)\n"
    "v_mfma_f32_32x32x16_bf16 v[156:171], a[160:163], v[120:123], v[156:171]\n"
    "ds_read_b64_tr_b16 a[144:145], v146 offset:57344\n"
    "ds_read_b64_tr_b16 a[146:147], v146 offset:58368\n"
    "v_mfma_f32_32x32x16_bf16 v[220:235], a[164:167], v[120:123], v[220:235]\n"
    "ds_read_b64_tr_b16 a[148:149], v145 offset:59392\n"
    "ds_read_b64_tr_b16 a[150:151], v145 offset:60416\n"
    "v_mfma_f32_32x32x16_bf16 v[156:171], a[168:171], v[124:127], v[156:171]\n"
    "ds_read_b64_tr_b16 a[152:153], v146 offset:61440\n"
    "ds_read_b64_tr_b16 a[154:155], v146 offset:62464\n"
    "v_mfma_f32_32x32x16_bf16 v[220:235], a[172:175], v[124:127], v[220:235]\n"
    "ds_read_b64_tr_b16 a[156:157], v145 offset:63488\n"
    "ds_read_b64_tr_b16 a[158:159], v145 offset:64512\n"
    "s_xor_b32 s13, s12, 128\n"
    "s_add_u32 m0, s75, 2048\n"
    "s_nop 0\n"
    "buffer_load_dwordx4 v246, s[52:55], s13 offen offset:0 lds\n"
    "s_waitcnt lgkmcnt(8)\n"
    "v_mfma_f32_32x32x16_bf16 v[156:171], a[128:131], v[64:67], v[156:171]\n"
    "ds_read_b64_tr_b16 a[160:161], v146 offset:18432\n"
    "ds_read_b64_tr_b16 a[162:163], v146 offset:19456\n"
    "v_mfma_f32_32x32x16_bf16 v[220:235], a[132:135], v[64:67], v[220:235]\n"
    "ds_read_b64_tr_b16 a[164:165], v146 offset:22528\n"
    "ds_read_b64_tr_b16 a[166:167], v146 offset:23552\n"
    "buffer_load_dwordx4 v247, s[52:55], s13 offen offset:1024 lds\n"
    "v_mfma_f32_32x32x16_bf16 v[156:171], a[136:139], v[72:75], v[156:171]\n"
    "ds_read_b64_tr_b16 a[168:169], v146 offset:26624\n"
    "ds_read_b64_tr_b16 a[170:171], v146 offset:27648\n"
    "v_mfma_f32_32x32x16_bf16 v[220:235], a[140:143], v[72:75], v[220:235]\n"
    "ds_read_b64_tr_b16 a[172:173], v146 offset:30720\n"
    "ds_read_b64_tr_b16 a[174:175], v146 offset:31744\n"
    "s_waitcnt lgkmcnt(8)\n"
    "v_mfma_f32_32x32x16_bf16 v[156:171], a[144:147], v[68:71], v[156:171]\n"
    "ds_read_b64_tr_b16 a[128:129], v146 offset:34816\n"
    "ds_read_b64_tr_b16 a[130:131], v146 offset:35840\n"
    "v_mfma_f32_32x32x16_bf16 v[220:235], a[148:151], v[68:71], v[220:235]\n"
    "ds_read_b64_tr_b16 a[132:133], v146 offset:38912\n"
    "ds_read_b64_tr_b16 a[134:135], v146 offset:39936\n"
    "v_mfma_f32_32x32x16_bf16 v[156:171], a[152:155], v[76:79], v[156:171]\n"
    "ds_read_b64_tr_b16 a[136:137], v146 offset:43008\n"
    "ds_read_b64_tr_b16 a[138:139], v146 offset:44032\n"
    "v_mfma_f32_32x32x16_bf16 v[220:235], a[156:159], v[76:79], v[220:235]\n"
    "ds_read_b64_tr_b16 a[140:141], v146 offset:47104\n"
    "ds_read_b64_tr_b16 a[142:143], v146 offset:48128\n"
    "s_waitcnt vmcnt(19) lgkmcnt(8)\n"
    "s_barrier\n"
    "v_mfma_f32_32x32x16_bf16 v[32:47], a[160:163], v[96:99], 0\n"
    "ds_read_b64_tr_b16 a[144:145], v146 offset:51200\n"
    "ds_read_b64_tr_b16 a[146:147], v146 offset:52224\n"
    "v_mfma_f32_32x32x16_bf16 v[32:47], a[164:167], v[100:103], v[32:47]\n"
    "ds_read_b64_tr_b16 a[148:149], v146 offset:55296\n"
    "ds_read_b64_tr_b16 a[150:151], v146 offset:56320\n"
    "v_mfma_f32_32x32x16_bf16 v[32:47], a[168:171], v[104:107], v[32:47]\n"
    "ds_read_b64_tr_b16 a[152:153], v146 offset:59392\n"
    "ds_read_b64_tr_b16 a[154:155], v146 offset:60416\n"
    "v_mfma_f32_32x32x16_bf16 v[32:47], a[172:175], v[108:111], v[32:47]\n"
    "ds_read_b64_tr_b16 a[156:157], v146 offset:63488\n"
    "ds_read_b64_tr_b16 a[158:159], v146 offset:64512\n"
    "s_waitcnt lgkmcnt(8)\n"
    "v_mfma_f32_32x32x16_bf16 v[32:47], a[128:131], v[112:115], v[32:47]\n"
    "ds_read_b64_tr_b16 a[160:161], v148 offset:0\n"
    "ds_read_b64_tr_b16 a[162:163], v148 offset:1024\n"
    "s_waitcnt vmcnt(4)\n"
    "v_cmp_eq_u32 vcc, s72, v245\n"
    "s_nop 0\n"
    "s_andn2_b32 s5, 0x30, vcc_lo\n"
    "s_cbranch_scc1 LfsB3\n"
    "LfoB3:\n"
    "v_mfma_f32_32x32x16_bf16 v[32:47], a[132:135], v[116:119], v[32:47]\n"
    "ds_read_b64_tr_b16 a[164:165], v147 offset:2048\n"
    "ds_read_b64_tr_b16 a[166:167], v147 offset:3072\n"
    "s_add_u32 s18, s88, 13824\n"
    "buffer_load_dwordx4 v[128:131], v248, s[60:63], s18 offen offset:512 sc1\n"
    "v_mfma_f32_32x32x16_bf16 v[32:47], a[136:139], v[120:123], v[32:47]\n"
    "ds_read_b64_tr_b16 a[168:169], v148 offset:2048\n"
    "ds_read_b64_tr_b16 a[170:171], v148 offset:3072\n"
    "v_mfma_f32_32x32x16_bf16 v[32:47], a[140:143], v[124:127], v[32:47]\n"
    "ds_read_b64_tr_b16 a[172:173], v148 offset:4096\n"
    "ds_read_b64_tr_b16 a[174:175], v148 offset:5120\n"
    "buffer_load_dwordx4 v[132:135], v248, s[60:63], s18 offen offset:1536 sc1\n"
    "s_waitcnt lgkmcnt(8)\n"
    "v_mfma_f32_32x32x16_bf16 v[32:47], a[144:147], v[64:67], v[32:47]\n"
    "ds_read_b64_tr_b16 a[128:129], v147 offset:6144\n"
    "ds_read_b64_tr_b16 a[130:131], v147 offset:7168\n"
    "v_mfma_f32_32x32x16_bf16 v[32:47], a[148:151], v[72:75], v[32:47]\n"
    "ds_read_b64_tr_b16 a[132:133], v148 offset:6144\n"
    "ds_read_b64_tr_b16 a[134:135], v148 offset:7168\n"
    "buffer_load_dwordx4 v[136:139], v248, s[60:63], s18 offen offset:2560 sc1\n"
    "v_mfma_f32_32x32x16_bf16 v[32:47], a[152:155], v[68:71], v[32:47]\n"
    "ds_read_b64_tr_b16 a[136:137], v148 offset:8192\n"
    "ds_read_b64_tr_b16 a[138:139], v148 offset:9216\n"
    "v_mfma_f32_32x32x16_bf16 v[32:47], a[156:159], v[76:79], v[32:47]\n"
    "ds_read_b64_tr_b16 a[140:141], v147 offset:10240\n"
    "ds_read_b64_tr_b16 a[142:143], v147 offset:11264\n"
    "buffer_load_dwordx4 v[140:143], v248, s[60:63], s18 offen offset:3584 sc1\n"
    "s_waitcnt lgkmcnt(8)\n"
    "v_mfma_f32_32x32x16_bf16 v[156:171], a[160:163], v[0:3], v[156:171]\n"
    "ds_read_b64_tr_b16 a[144:145], v148 offset:10240\n"
    "ds_read_b64_tr_b16 a[146:147], v148 offset:11264\n"
    "buffer_load_dword v151, v144, s[60:63], s18 offen offset:0 sc1\n"
    "v_mfma_f32_32x32x16_bf16 v[220:235], a[164:167], v[0:3], v[220:235]\n"
    "ds_read_b64_tr_b16 a[148:149], v148 offset:12288\n"
    "ds_read_b64_tr_b16 a[150:151], v148 offset:13312\n"
    "v_mfma_f32_32x32x16_bf16 v[32:47], a[168:171], v[0:3], v[32:47]\n"
    "ds_read_b64_tr_b16 a[152:153], v147 offset:14336\n"
    "ds_read_b64_tr_b16 a[154:155], v147 offset:15360\n"
    "s_add_u32 s19, s88, 18432\n"
    "buffer_load_dwordx4 v[80:83], v248, s[60:63], s19 offen offset:512 sc1\n"
    "v_mfma_f32_32x32x16_bf16 v[156:171], a[172:175], v[8:11], v[156:171]\n"
    "ds_read_b64_tr_b16 a[156:157], v148 offset:14336\n"
    "ds_read_b64_tr_b16 a[158:159], v148 offset:15360\n"
    "s_waitcnt lgkmcnt(8)\n"
    "v_mfma_f32_32x32x16_bf16 v[220:235], a[128:131], v[8:11], v[220:235]\n"
    "ds_read_b64_tr_b16 a[160:161], v148 offset:16384\n"
    "ds_read_b64_tr_b16 a[162:163], v148 offset:17408\n"
    "buffer_load_dwordx4 v[84:87], v248, s[60:63], s19 offen offset:1536 sc1\n"
    "buffer_load_dwordx4 v[88:91], v248, s[60:63], s19 offen offset:2560 sc1\n"
    "v_mfma_f32_32x32x16_bf16 v[32:47], a[132:135], v[8:11], v[32:47]\n"
    "ds_read_b64_tr_b16 a[164:165], v147 offset:18432\n"
    "ds_read_b64_tr_b16 a[166:167], v147 offset:19456\n"
    "buffer_load_dwordx4 v[92:95], v248, s[60:63], s19 offen offset:3584 sc1\n"
    "buffer_load_dword v152, v144, s[60:63], s19 offen offset:0 sc1\n"
    "v_mfma_f32_32x32x16_bf16 v[156:171], a[136:139], v[16:19], v[156:171]\n"
    "ds_read_b64_tr_b16 a[168:169], v148 offset:18432\n"
    "ds_read_b64_tr_b16 a[170:171], v148 offset:19456\n"
    "v_mfma_f32_32x32x16_bf16 v[220:235], a[140:143], v[16:19], v[220:235]\n"
    "ds_read_b64_tr_b16 a[172:173], v148 offset:20480\n"
    "ds_read_b64_tr_b16 a[174:175], v148 offset:21504\n"
    "s_waitcnt lgkmcnt(8)\n"
    "v_mfma_f32_32x32x16_bf16 v[32:47], a[144:147], v[16:19], v[32:47]\n"
    "ds_read_b64_tr_b16 a[128:129], v147 offset:22528\n"
    "ds_read_b64_tr_b16 a[130:131], v147 offset:23552\n"
    "v_mfma_f32_32x32x16_bf16 v[156:171], a[148:151], v[24:27], v[156:171]\n"
    "ds_read_b64_tr_b16 a[132:133], v148 offset:22528\n"
    "ds_read_b64_tr_b16 a[134:135], v148 offset:23552\n"
    "v_mfma_f32_32x32x16_bf16 v[220:235], a[152:155], v[24:27], v[220:235]\n"
    "ds_read_b64_tr_b16 a[136:137], v148 offset:24576\n"
    "ds_read_b64_tr_b16 a[138:139], v148 offset:25600\n"
    "v_mfma_f32_32x32x16_bf16 v[32:47], a[156:159], v[24:27], v[32:47]\n"
    "ds_read_b64_tr_b16 a[140:141], v147 offset:26624\n"
    "ds_read_b64_tr_b16 a[142:143], v147 offset:27648\n"
    "s_waitcnt vmcnt(9)\n"
    "v_add3_u32 v154, v128, v129, s68\n"
    "v_add3_u32 v154, v154, v130, v131\n"
    "s_waitcnt vmcnt(8)\n"
    "v_add3_u32 v154, v154, v132, v133\n"
    "v_add3_u32 v154, v154, v134, v135\n"
    "s_waitcnt vmcnt(7)\n"
    "v_add3_u32 v154, v154, v136, v137\n"
    "v_add3_u32 v154, v154, v138, v139\n"
    "s_waitcnt vmcnt(6)\n"
    "v_add3_u32 v154, v154, v140, v141\n"
    "v_add3_u32 v154, v154, v142, v143\n"
    "s_waitcnt vmcnt(4)\n"
    "v_add3_u32 v154, v154, v80, v81\n"
    "v_add3_u32 v154, v154, v82, v83\n"
    "s_waitcnt vmcnt(3)\n"
    "v_add3_u32 v154, v154, v84, v85\n"
    "v_add3_u32 v154, v154, v86, v87\n"
    "s_waitcnt vmcnt(2)\n"
    "v_add3_u32 v154, v154, v88, v89\n"
    "v_add3_u32 v154, v154, v90, v91\n"
    "s_waitcnt vmcnt(1)\n"
    "v_add3_u32 v154, v154, v92, v93\n"
    "v_add3_u32 v154, v154, v94, v95\n"
    "s_waitcnt vmcnt(0)\n"
    "v_add_u32 v151, v151, v152\n"
    "v_cmp_ne_u32 vcc, v154, v151\n"
    "s_nop 0\n"
    "s_cbranch_vccnz LreB4\n"
    "LokB4:\n"
    "s_waitcnt lgkmcnt(8)\n"
    "v_mfma_f32_32x32x16_bf16 v[156:171], a[160:163], v[128:131], v[156:171]\n"
    "ds_read_b64_tr_b16 a[144:145], v148 offset:26624\n"
    "ds_read_b64_tr_b16 a[146:147], v148 offset:27648\n"
    "s_add_u32 s79, s90, 32256\n"
    "buffer_load_dwordx4 v[172:175], v248, s[60:63], s79 offen offset:512 sc1\n"
    "buffer_load_dwordx4 v[176:179], v248, s[60:63], s79 offen offset:1536 sc1\n"
    "v_mfma_f32_32x32x16_bf16 v[220:235], a[164:167], v[128:131], v[220:235]\n"
    "ds_read_b64_tr_b16 a[148:149], v148 offset:28672\n"
    "ds_read_b64_tr_b16 a[150:151], v148 offset:29696\n"
    "v_and_b32 v244, 0x7c, v144\n"
    "v_mfma_f32_32x32x16_bf16 v[32:47], a[168:171], v[128:131], v[32:47]\n"
    "ds_read_b64_tr_b16 a[152:153], v147 offset:30720\n"
    "ds_read_b64_tr_b16 a[154:155], v147 offset:31744\n"
    "buffer_load_dword v244, v244, s[60:63], s0 offen sc1\n"
    "buffer_load_dwordx4 v[180:183], v248, s[60:63], s79 offen offset:2560 sc1\n"
    "v_mfma_f32_32x32x16_bf16 v[156:171], a[172:175], v[132:135], v[156:171]\n"
    "ds_read_b64_tr_b16 a[156:157], v148 offset:30720\n"
    "ds_read_b64_tr_b16 a[158:159], v148 offset:31744\n"
    "s_waitcnt lgkmcnt(8)\n"
    "v_mfma_f32_32x32x16_bf16 v[220:235], a[128:131], v[132:135], v[220:235]\n"
    "ds_read_b64_tr_b16 a[160:161], v148 offset:32768\n"
    "ds_read_b64_tr_b16 a[162:163], v148 offset:33792\n"
    "buffer_load_dwordx4 v[184:187], v248, s[60:63], s79 offen offset:3584 sc1\n"
    "v_mfma_f32_32x32x16_bf16 v[32:47], a[132:135], v[132:135], v[32:47]\n"
    "ds_read_b64_tr_b16 a[164:165], v147 offset:34816\n"
    "ds_read_b64_tr_b16 a[166:167], v147 offset:35840\n"
    "v_mfma_f32_32x32x16_bf16 v[156:171], a[136:139], v[136:139], v[156:171]\n"
    "ds_read_b64_tr_b16 a[168:169], v148 offset:34816\n"
    "ds_read_b64_tr_b16 a[170:171], v148 offset:35840\n"
    "buffer_load_dword v20, v144, s[60:63], s79 offen offset:0 sc1\n"
    "v_mfma_f32_32x32x16_bf16 v[220:235], a[140:143], v[136:139], v[220:235]\n"
    "ds_read_b64_tr_b16 a[172:173], v148 offset:36864\n"
    "ds_read_b64_tr_b16 a[174:175], v148 offset:37888\n"
    "s_waitcnt lgkmcnt(8)\n"
    "v_mfma_f32_32x32x16_bf16 v[32:47], a[144:147], v[136:139], v[32:47]\n"
    "ds_read_b64_tr_b16 a[128:129], v147 offset:38912\n"
    "ds_read_b64_tr_b16 a[130:131], v147 offset:39936\n"
    "v_mfma_f32_32x32x16_bf16 v[156:171], a[148:151], v[140:143], v[156:171]\n"
    "ds_read_b64_tr_b16 a[132:133], v148 offset:38912\n"
    "ds_read_b64_tr_b16 a[134:135], v148 offset:39936\n"
    "v_mfma_f32_32x32x16_bf16 v[220:235], a[152:155], v[140:143], v[220:235]\n"
    "ds_read_b64_tr_b16 a[136:137], v148 offset:40960\n"
    "ds_read_b64_tr_b16 a[138:139], v148 offset:41984\n"
    "v_mfma_f32_32x32x16_bf16 v[32:47], a[156:159], v[140:143], v[32:47]\n"
    "ds_read_b64_tr_b16 a[140:141], v147 offset:43008\n"
    "ds_read_b64_tr_b16 a[142:143], v147 offset:44032\n"
    "s_waitcnt lgkmcnt(8)\n"
    "v_mfma_f32_32x32x16_bf16 v[156:171], a[160:163], v[80:83], v[156:171]\n"
    "ds_read_b64_tr_b16 a[144:145], v148 offset:43008\n"
    "ds_read_b64_tr_b16 a[146:147], v148 offset:44032\n"
    "v_mfma_f32_32x32x16_bf16 v[220:235], a[164:167], v[80:83], v[220:235]\n"
    "ds_read_b64_tr_b16 a[148:149], v148 offset:45056\n"
    "ds_read_b64_tr_b16 a[150:151], v148 offset:46080\n"
    "v_mfma_f32_32x32x16_bf16 v[32:47], a[168:171], v[80:83], v[32:47]\n"
    "ds_read_b64_tr_b16 a[152:153], v147 offset:47104\n"
    "ds_read_b64_tr_b16 a[154:155], v147 offset:48128\n"
    "v_mfma_f32_32x32x16_bf16 v[156:171], a[172:175], v[84:87], v[156:171]\n"
    "ds_read_b64_tr_b16 a[156:157], v148 offset:47104\n"
    "ds_read_b64_tr_b16 a[158:159], v148 offset:48128\n"
    "s_waitcnt lgkmcnt(8)\n"
    "s_barrier\n"
    "v_mfma_f32_32x32x16_bf16 v[220:235], a[128:131], v[84:87], v[220:235]\n"
    "ds_read_b64_tr_b16 a[160:161], v148 offset:49152\n"
    "ds_read_b64_tr_b16 a[162:163], v148 offset:50176\n"
    "v_mfma_f32_32x32x16_bf16 v[32:47], a[132:135], v[84:87], v[32:47]\n"
    "ds_read_b64_tr_b16 a[164:165], v147 offset:51200\n"
    "ds_read_b64_tr_b16 a[166:167], v147 offset:52224\n"
    "v_mfma_f32_32x32x16_bf16 v[156:171], a[136:139], v[88:91], v[156:171]\n"
    "ds_read_b64_tr_b16 a[168:169], v148 offset:51200\n"
    "ds_read_b64_tr_b16 a[170:171], v148 offset:52224\n"
    "v_mfma_f32_32x32x16_bf16 v[220:235], a[140:143], v[88:91], v[220:235]\n"
    "ds_read_b64_tr_b16 a[172:173], v148 offset:53248\n"
    "ds_read_b64_tr_b16 a[174:175], v148 offset:54272\n"
    "s_waitcnt lgkmcnt(8)\n"
    "v_mfma_f32_32x32x16_bf16 v[32:47], a[144:147], v[88:91], v[32:47]\n"
    "ds_read_b64_tr_b16 a[128:129], v147 offset:55296\n"
    "ds_read_b64_tr_b16 a[130:131], v147 offset:56320\n"
    "v_mfma_f32_32x32x16_bf16 v[156:171], a[148:151], v[92:95], v[156:171]\n"
    "ds_read_b64_tr_b16 a[132:133], v148 offset:55296\n"
    "ds_read_b64_tr_b16 a[134:135], v148 offset:56320\n"
    "v_mfma_f32_32x32x16_bf16 v[220:235], a[152:155], v[92:95], v[220:235]\n"
    "ds_read_b64_tr_b16 a[136:137], v148 offset:57344\n"
    "ds_read_b64_tr_b16 a[138:139], v148 offset:58368\n"
    "s_waitcnt vmcnt(3)\n"
    "v_cmp_eq_u32 vcc, s72, v244\n"
    "s_nop 0\n"
    "s_andn2_b32 s5, 0xc0, vcc_lo\n"
    "s_cbranch_scc1 LfsB5\n"
    "LfoB5:\n"
    "v_mfma_f32_32x32x16_bf16 v[32:47], a[156:159], v[92:95], v[32:47]\n"
    "ds_read_b64_tr_b16 a[140:141], v147 offset:59392\n"
    "ds_read_b64_tr_b16 a[142:143], v147 offset:60416\n"
    "s_add_u32 s7, s88, 23040\n"
    "buffer_load_dwordx4 v[188:191], v248, s[60:63], s7 offen offset:512 sc1\n"
    "buffer_load_dwordx4 v[192:195], v248, s[60:63], s7 offen offset:1536 sc1\n"
    "v_add3_u32 v23, v172, v173, s72\n"
    "v_add3_u32 v23, v23, v174, v175\n"
    "v_add3_u32 v23, v23, v176, v177\n"
    "v_add3_u32 v23, v23, v178, v179\n"
    "s_waitcnt vmcnt(4)\n"
    "v_add3_u32 v23, v23, v180, v181\n"
    "v_add3_u32 v23, v23, v182, v183\n"
    "s_waitcnt vmcnt(3)\n"
    "v_add3_u32 v23, v23, v184, v185\n"
    "v_add3_u32 v23, v23, v186, v187\n"
    "s_waitcnt vmcnt(2)\n"
    "v_cmp_ne_u32 vcc, v23, v20\n"
    "s_nop 0\n"
    "s_cbranch_vccnz LreB6\n"
    "LokB6:\n"
    "s_waitcnt lgkmcnt(8)\n"
    "v_mfma_f32_32x32x16_bf16 v[156:171], a[160:163], v[172:175], v[156:171]\n"
    "ds_read_b64_tr_b16 a[144:145], v148 offset:61440\n"
    "ds_read_b64_tr_b16 a[146:147], v148 offset:62464\n"
    "buffer_load_dwordx4 v[196:199], v248, s[60:63], s7 offen offset:2560 sc1\n"
    "buffer_load_dwordx4 v[200:203], v248, s[60:63], s7 offen offset:3584 sc1\n"
    "v_mfma_f32_32x32x16_bf16 v[220:235], a[164:167], v[172:175], v[220:235]\n"
    "ds_read_b64_tr_b16 a[148:149], v145 offset:16384\n"
    "ds_read_b64_tr_b16 a[150:151], v145 offset:17408\n"
    "buffer_load_dword v22, v144, s[60:63], s7 offen offset:0 sc1\n"
    "s_add_u32 s6, s88, 27648\n"
    "buffer_load_dwordx4 v[204:207], v248, s[60:63], s6 offen offset:512 sc1\n"
    "v_mfma_f32_32x32x16_bf16 v[32:47], a[168:171], v[172:175], v[32:47]\n"
    "ds_read_b64_tr_b16 a[152:153], v145 offset:20480\n"
    "ds_read_b64_tr_b16 a[154:155], v145 offset:21504\n"
    "buffer_load_dwordx4 v[208:211], v248, s[60:63], s6 offen offset:1536 sc1\n"
    "buffer_load_dwordx4 v[212:215], v248, s[60:63], s6 offen offset:2560 sc1\n"
    "v_mfma_f32_32x32x16_bf16 v[156:171], a[172:175], v[176:179], v[156:171]\n"
    "ds_read_b64_tr_b16 a[156:157], v145 offset:24576\n"
    "ds_read_b64_tr_b16 a[158:159], v145 offset:25600\n"
    "buffer_load_dwordx4 v[216:219], v248, s[60:63], s6 offen offset:3584 sc1\n"
    "buffer_load_dword v14, v144, s[60:63], s6 offen offset:0 sc1\n"
    "s_waitcnt lgkmcnt(8)\n"
    "v_mfma_f32_32x32x16_bf16 v[220:235], a[128:131], v[176:179], v[220:235]\n"
    "ds_read_b64_tr_b16 a[160:161], v145 offset:28672\n"
    "ds_read_b64_tr_b16 a[162:163], v145 offset:29696\n"
    "v_mfma_f32_32x32x16_bf16 v[32:47], a[132:135], v[176:179], v[32:47]\n"
    "ds_read_b64_tr_b16 a[164:165], v150 offset:0\n"
    "ds_read_b64_tr_b16 a[166:167], v150 offset:1024\n"
    "v_mfma_f32_32x32x16_bf16 v[156:171], a[136:139], v[180:183], v[156:171]\n"
    "ds_read_b64_tr_b16 a[168:169], v150 offset:4096\n"
    "ds_read_b64_tr_b16 a[170:171], v150 offset:5120\n"
    "v_mfma_f32_32x32x16_bf16 v[220:235], a[140:143], v[180:183], v[220:235]\n"
    "ds_read_b64_tr_b16 a[172:173], v150 offset:8192\n"
    "ds_read_b64_tr_b16 a[174:175], v150 offset:9216\n"
    "s_waitcnt lgkmcnt(8)\n"
    "v_mfma_f32_32x32x16_bf16 v[156:171], a[144:147], v[184:187], v[156:171]\n"
    "ds_read_b64_tr_b16 a[128:129], v150 offset:12288\n"
    "ds_read_b64_tr_b16 a[130:131], v150 offset:13312\n"
    "v_mfma_f32_32x32x16_bf16 v[48:63], a[148:151], v[96:99], 0\n"
    "ds_read_b64_tr_b16 a[132:133], v146 offset:0\n"
    "ds_read_b64_tr_b16 a[134:135], v146 offset:1024\n"
    "v_mfma_f32_32x32x16_bf16 v[48:63], a[152:155], v[100:103], v[48:63]\n"
    "ds_read_b64_tr_b16 a[136:137], v146 offset:4096\n"
    "ds_read_b64_tr_b16 a[138:139], v146 offset:5120\n"
    "v_mfma_f32_32x32x16_bf16 v[48:63], a[156:159], v[104:107], v[48:63]\n"
    "ds_read_b64_tr_b16 a[140:141], v146 offset:8192\n"
    "ds_read_b64_tr_b16 a[142:143], v146 offset:9216\n"
    "s_waitcnt vmcnt(9)\n"
    "v_add3_u32 v31, v188, v189, s68\n"
    "v_add3_u32 v31, v31, v190, v191\n"
    "s_waitcnt vmcnt(8)\n"
    "v_add3_u32 v31, v31, v192, v193\n"
    "v_add3_u32 v31, v31, v194, v195\n"
    "s_waitcnt vmcnt(7)\n"
    "v_add3_u32 v31, v31, v196, v197\n"
    "v_add3_u32 v31, v31, v198, v199\n"
    "s_waitcnt vmcnt(6)\n"
    "v_add3_u32 v31, v31, v200, v201\n"
    "v_add3_u32 v31, v31, v202, v203\n"
    "s_waitcnt vmcnt(4)\n"
    "v_add3_u32 v31, v31, v204, v205\n"
    "v_add3_u32 v31, v31, v206, v207\n"
    "s_waitcnt vmcnt(3)\n"
    "v_add3_u32 v31, v31, v208, v209\n"
    "v_add3_u32 v31, v31, v210, v211\n"
    "s_waitcnt vmcnt(2)\n"
    "v_add3_u32 v31, v31, v212, v213\n"
    "v_add3_u32 v31, v31, v214, v215\n"
    "s_waitcnt vmcnt(1)\n"
    "v_add3_u32 v31, v31, v216, v217\n"
    "v_add3_u32 v31, v31, v218, v219\n"
    "s_waitcnt vmcnt(0)\n"
    "v_add_u32 v22, v22, v14\n"
    "v_cmp_ne_u32 vcc, v31, v22\n"
    "s_nop 0\n"
    "s_cbranch_vccnz LreB7\n"
    "LokB7:\n"
    "s_waitcnt lgkmcnt(8)\n"
    "v_mfma_f32_32x32x16_bf16 v[48:63], a[160:163], v[108:111], v[48:63]\n"
    "ds_read_b64_tr_b16 a[144:145], v146 offset:12288\n"
    "ds_read_b64_tr_b16 a[146:147], v146 offset:13312\n"
    "v_mfma_f32_32x32x16_bf16 v[156:171], a[164:167], v[188:191], v[156:171]\n"
    "ds_read_b64_tr_b16 a[148:149], v147 offset:63488\n"
    "ds_read_b64_tr_b16 a[150:151], v147 offset:64512\n"
    "v_mfma_f32_32x32x16_bf16 v[156:171], a[168:171], v[192:195], v[156:171]\n"
    "ds_read_b64_tr_b16 a[152:153], v149 offset:2048\n"
    "ds_read_b64_tr_b16 a[154:155], v149 offset:3072\n"
    "v_mfma_f32_32x32x16_bf16 v[156:171], a[172:175], v[196:199], v[156:171]\n"
    "ds_read_b64_tr_b16 a[156:157], v149 offset:6144\n"
    "ds_read_b64_tr_b16 a[158:159], v149 offset:7168\n"
    "s_waitcnt lgkmcnt(8)\n"
    "v_mfma_f32_32x32x16_bf16 v[156:171], a[128:131], v[200:203], v[156:171]\n"
    "ds_read_b64_tr_b16 a[160:161], v149 offset:10240\n"
    "ds_read_b64_tr_b16 a[162:163], v149 offset:11264\n"
    "v_mfma_f32_32x32x16_bf16 v[156:171], a[132:135], v[204:207], v[156:171]\n"
    "ds_read_b64_tr_b16 a[164:165], v149 offset:14336\n"
    "ds_read_b64_tr_b16 a[166:167], v149 offset:15360\n"
    "v_mfma_f32_32x32x16_bf16 v[156:171], a[136:139], v[208:211], v[156:171]\n"
    "ds_read_b64_tr_b16 a[168:169], v145 offset:2048\n"
    "ds_read_b64_tr_b16 a[170:171], v145 offset:3072\n"
    "v_mfma_f32_32x32x16_bf16 v[156:171], a[140:143], v[212:215], v[156:171]\n"
    "ds_read_b64_tr_b16 a[172:173], v145 offset:6144\n"
    "ds_read_b64_tr_b16 a[174:175], v145 offset:7168\n"
    "s_waitcnt lgkmcnt(8)\n"
    "v_mfma_f32_32x32x16_bf16 v[156:171], a[144:147], v[216:219], v[156:171]\n"
    "ds_read_b64_tr_b16 a[128:129], v145 offset:10240\n"
    "ds_read_b64_tr_b16 a[130:131], v145 offset:11264\n"
    "v_mfma_f32_32x32x16_bf16 v[220:235], a[148:151], v[184:187], v[220:235]\n"
    "ds_read_b64_tr_b16 a[132:133], v145 offset:14336\n"
    "ds_read_b64_tr_b16 a[134:135], v145 offset:15360\n"
    "s_nop 6\n"
    "v_cvt_pk_bf16_f32 v156, v156, v157\n"
    "v_cvt_pk_bf16_f32 v157, v158, v159\n"
    "v_cvt_pk_bf16_f32 v158, v160, v161\n"
    "v_cvt_pk_bf16_f32 v159, v162, v163\n"
    "buffer_store_dwordx4 v[156:159], v248, s[60:63], s91 offen offset:256 sc1\n"
    "v_mfma_f32_32x32x16_bf16 v[220:235], a[152:155], v[188:191], v[220:235]\n"
    "ds_read_b64_tr_b16 a[136:137], v148 offset:59392\n"
    "ds_read_b64_tr_b16 a[138:139], v148 offset:60416\n"
    "v_add3_u32 v5, v156, v157, s72\n"
    "v_add3_u32 v5, v5, v158, v159\n"
    "v_cvt_pk_bf16_f32 v160, v164, v165\n"
    "v_cvt_pk_bf16_f32 v161, v166, v167\n"
    "v_cvt_pk_bf16_f32 v162, v168, v169\n"
    "v_cvt_pk_bf16_f32 v163, v170, v171\n"
    "buffer_store_dwordx4 v[160:163], v248, s[60:63], s91 offen offset:1280 sc1\n"
    "v_mfma_f32_32x32x16_bf16 v[220:235], a[156:159], v[192:195], v[220:235]\n"
    "ds_read_b64_tr_b16 a[140:141], v148 offset:63488\n"
    "ds_read_b64_tr_b16 a[142:143], v148 offset:64512\n"
    "v_add3_u32 v5, v5, v160, v161\n"
    "v_add3_u32 v5, v5, v162, v163\n"
    "buffer_store_dword v5, v144, s[60:63], s91 offen sc1\n"
    "s_xor_b32 s6, s2, 1024\n"
    "v_mov_b32 v12, s72\n"
    "s_waitcnt lgkmcnt(8)\n"
    "v_mfma_f32_32x32x16_bf16 v[220:235], a[160:163], v[196:199], v[220:235]\n"
    "ds_read_b64_tr_b16 a[144:145], v150 offset:2048\n"
    "ds_read_b64_tr_b16 a[146:147], v150 offset:3072\n"
    "buffer_store_dword v12, off, s[60:63], s6 offset:4 sc1\n"
    "s_xor_b32 s7, s2, 2048\n"
    "v_mov_b32 v12, s72\n"
    "buffer_store_dword v12, off, s[60:63], s7 offset:8 sc1\n"
    "v_mfma_f32_32x32x16_bf16 v[220:235], a[164:167], v[200:203], v[220:235]\n"
    "ds_read_b64_tr_b16 a[148:149], v150 offset:6144\n"
    "ds_read_b64_tr_b16 a[150:151], v150 offset:7168\n"
    "s_xor_b32 s6, s2, 3072\n"
    "v_mov_b32 v12, s72\n"
    "buffer_store_dword v12, off, s[60:63], s6 offset:12 sc1\n"
    "v_mfma_f32_32x32x16_bf16 v[220:235], a[168:171], v[204:207], v[220:235]\n"
    "ds_read_b64_tr_b16 a[152:153], v150 offset:10240\n"
    "ds_read_b64_tr_b16 a[154:155], v150 offset:11264\n"
    "v_mfma_f32_32x32x16_bf16 v[220:235], a[172:175], v[208:211], v[220:235]\n"
    "ds_read_b64_tr_b16 a[156:157], v150 offset:14336\n"
    "ds_read_b64_tr_b16 a[158:159], v150 offset:15360\n"
    "s_waitcnt lgkmcnt(8)\n"
    "v_mfma_f32_32x32x16_bf16 v[220:235], a[128:131], v[212:215], v[220:235]\n"
    "ds_read_b64_tr_b16 a[160:161], v146 offset:2048\n"
    "ds_read_b64_tr_b16 a[162:163], v146 offset:3072\n"
    "v_mfma_f32_32x32x16_bf16 v[220:235], a[132:135], v[216:219], v[220:235]\n"
    "ds_read_b64_tr_b16 a[164:165], v146 offset:6144\n"
    "ds_read_b64_tr_b16 a[166:167], v146 offset:7168\n"
    "v_mfma_f32_32x32x16_bf16 v[32:47], a[136:139], v[180:183], v[32:47]\n"
    "ds_read_b64_tr_b16 a[168:169], v146 offset:10240\n"
    "ds_read_b64_tr_b16 a[170:171], v146 offset:11264\n"
    "s_nop 6\n"
    "v_cvt_pk_bf16_f32 v220, v220, v221\n"
    "v_cvt_pk_bf16_f32 v221, v222, v223\n"
    "v_cvt_pk_bf16_f32 v222, v224, v225\n"
    "v_cvt_pk_bf16_f32 v223, v226, v227\n"
    "buffer_store_dwordx4 v[220:223], v248, s[60:63], s92 offen offset:256 sc1\n"
    "v_mfma_f32_32x32x16_bf16 v[32:47], a[140:143], v[184:187], v[32:47]\n"
    "ds_read_b64_tr_b16 a[172:173], v146 offset:14336\n"
    "ds_read_b64_tr_b16 a[174:175], v146 offset:15360\n"
    "v_add3_u32 v6, v220, v221, s72\n"
    "v_add3_u32 v6, v6, v222, v223\n"
    "v_cvt_pk_bf16_f32 v224, v228, v229\n"
    "v_cvt_pk_bf16_f32 v225, v230, v231\n"
    "v_cvt_pk_bf16_f32 v226, v232, v233\n"
    "v_cvt_pk_bf16_f32 v227, v234, v235\n"
    "buffer_store_dwordx4 v[224:227], v248, s[60:63], s92 offen offset:1280 sc1\n"
    "s_waitcnt lgkmcnt(8)\n"
    "v_mfma_f32_32x32x16_bf16 v[32:47], a[144:147], v[188:191], v[32:47]\n"
    "ds_read_b64_tr_b16 a[128:129], v145 offset:32768\n"
    "ds_read_b64_tr_b16 a[130:131], v145 offset:33792\n"
    "v_add3_u32 v6, v6, v224, v225\n"
    "v_add3_u32 v6, v6, v226, v227\n"
    "buffer_store_dword v6, v144, s[60:63], s92 offen sc1\n"
    "v_mfma_f32_32x32x16_bf16 v[32:47], a[148:151], v[192:195], v[32:47]\n"
    "ds_read_b64_tr_b16 a[132:133], v145 offset:36864\n"
    "ds_read_b64_tr_b16 a[134:135], v145 offset:37888\n"
    "v_mfma_f32_32x32x16_bf16 v[32:47], a[152:155], v[196:199], v[32:47]\n"
    "ds_read_b64_tr_b16 a[136:137], v145 offset:40960\n"
    "ds_read_b64_tr_b16 a[138:139], v145 offset:41984\n"
    "v_mfma_f32_32x32x16_bf16 v[32:47], a[156:159], v[200:203], v[32:47]\n"
    "ds_read_b64_tr_b16 a[140:141], v145 offset:45056\n"
    "ds_read_b64_tr_b16 a[142:143], v145 offset:46080\n"
    "s_waitcnt lgkmcnt(8)\n"
    "v_mfma_f32_32x32x16_bf16 v[32:47], a[160:163], v[204:207], v[32:47]\n"
    "ds_read_b64_tr_b16 a[144:145], v145 offset:49152\n"
    "ds_read_b64_tr_b16 a[146:147], v145 offset:50176\n"
    "v_mfma_f32_32x32x16_bf16 v[32:47], a[164:167], v[208:211], v[32:47]\n"
    "ds_read_b64_tr_b16 a[148:149], v145 offset:53248\n"
    "ds_read_b64_tr_b16 a[150:151], v145 offset:54272\n"
    "v_mfma_f32_32x32x16_bf16 v[32:47], a[168:171], v[212:215], v[32:47]\n"
    "ds_read_b64_tr_b16 a[152:153], v145 offset:57344\n"
    "ds_read_b64_tr_b16 a[154:155], v145 offset:58368\n"
    "v_mfma_f32_32x32x16_bf16 v[32:47], a[172:175], v[216:219], v[32:47]\n"
    "ds_read_b64_tr_b16 a[156:157], v145 offset:61440\n"
    "ds_read_b64_tr_b16 a[158:159], v145 offset:62464\n"
    "s_waitcnt lgkmcnt(8)\n"
    "v_mfma_f32_32x32x16_bf16 v[48:63], a[128:131], v[112:115], v[48:63]\n"
    "ds_read_b64_tr_b16 a[160:161], v147 offset:0\n"
    "ds_read_b64_tr_b16 a[162:163], v147 offset:1024\n"
    "s_nop 5\n"
    "v_cvt_pk_bf16_f32 v32, v32, v33\n"
    "v_cvt_pk_bf16_f32 v33, v34, v35\n"
    "v_cvt_pk_bf16_f32 v34, v36, v37\n"
    "v_cvt_pk_bf16_f32 v35, v38, v39\n"
    "buffer_store_dwordx4 v[32:35], v248, s[60:63], s93 offen offset:256 sc1\n"
    "v_mfma_f32_32x32x16_bf16 v[48:63], a[132:135], v[116:119], v[48:63]\n"
    "ds_read_b64_tr_b16 a[164:165], v147 offset:4096\n"
    "ds_read_b64_tr_b16 a[166:167], v147 offset:5120\n"
    "v_add3_u32 v7, v32, v33, s72\n"
    "v_add3_u32 v7, v7, v34, v35\n"
    "v_cvt_pk_bf16_f32 v36, v40, v41\n"
    "v_cvt_pk_bf16_f32 v37, v42, v43\n"
    "v_cvt_pk_bf16_f32 v38, v44, v45\n"
    "v_cvt_pk_bf16_f32 v39, v46, v47\n"
    "buffer_store_dwordx4 v[36:39], v248, s[60:63], s93 offen offset:1280 sc1\n"
    "v_mfma_f32_32x32x16_bf16 v[48:63], a[136:139], v[120:123], v[48:63]\n"
    "ds_read_b64_tr_b16 a[168:169], v147 offset:8192\n"
    "ds_read_b64_tr_b16 a[170:171], v147 offset:9216\n"
    "v_add3_u32 v7, v7, v36, v37\n"
    "v_add3_u32 v7, v7, v38, v39\n"
    "buffer_store_dword v7, v144, s[60:63], s93 offen sc1\n"
    "v_mfma_f32_32x32x16_bf16 v[48:63], a[140:143], v[124:127], v[48:63]\n"
    "ds_read_b64_tr_b16 a[172:173], v147 offset:12288\n"
    "ds_read_b64_tr_b16 a[174:175], v147 offset:13312\n"
    "s_waitcnt lgkmcnt(8)\n"
    "v_mfma_f32_32x32x16_bf16 v[48:63], a[144:147], v[64:67], v[48:63]\n"
    "ds_read_b64_tr_b16 a[128:129], v147 offset:16384\n"
    "ds_read_b64_tr_b16 a[130:131], v147 offset:17408\n"
    "v_mfma_f32_32x32x16_bf16 v[48:63], a[148:151], v[72:75], v[48:63]\n"
    "ds_read_b64_tr_b16 a[132:133], v147 offset:20480\n"
    "ds_read_b64_tr_b16 a[134:135], v147 offset:21504\n"
    "v_and_b32 v13, 0x7c, v144\n"
    "buffer_load_dword v13, v13, s[60:63], s2 offen sc1\n"
    "v_mfma_f32_32x32x16_bf16 v[48:63], a[152:155], v[68:71], v[48:63]\n"
    "ds_read_b64_tr_b16 a[136:137], v147 offset:24576\n"
    "ds_read_b64_tr_b16 a[138:139], v147 offset:25600\n"
    "v_mfma_f32_32x32x16_bf16 v[48:63], a[156:159], v[76:79], v[48:63]\n"
    "ds_read_b64_tr_b16 a[140:141], v147 offset:28672\n"
    "ds_read_b64_tr_b16 a[142:143], v147 offset:29696\n"
    "s_waitcnt lgkmcnt(8)\n"
    "v_mfma_f32_32x32x16_bf16 v[48:63], a[160:163], v[0:3], v[48:63]\n"
    "ds_read_b64_tr_b16 a[144:145], v147 offset:32768\n"
    "ds_read_b64_tr_b16 a[146:147], v147 offset:33792\n"
    "v_mfma_f32_32x32x16_bf16 v[48:63], a[164:167], v[8:11], v[48:63]\n"
    "ds_read_b64_tr_b16 a[148:149], v147 offset:36864\n"
    "ds_read_b64_tr_b16 a[150:151], v147 offset:37888\n"
    "v_mfma_f32_32x32x16_bf16 v[48:63], a[168:171], v[16:19], v[48:63]\n"
    "ds_read_b64_tr_b16 a[152:153], v147 offset:40960\n"
    "ds_read_b64_tr_b16 a[154:155], v147 offset:41984\n"
    "v_mfma_f32_32x32x16_bf16 v[48:63], a[172:175], v[24:27], v[48:63]\n"
    "ds_read_b64_tr_b16 a[156:157], v147 offset:45056\n"
    "ds_read_b64_tr_b16 a[158:159], v147 offset:46080\n"
    "s_waitcnt lgkmcnt(8)\n"
    "v_mfma_f32_32x32x16_bf16 v[48:63], a[128:131], v[128:131], v[48:63]\n"
    "ds_read_b64_tr_b16 a[160:161], v147 offset:49152\n"
    "ds_read_b64_tr_b16 a[162:163], v147 offset:50176\n"
    "v_mfma_f32_32x32x16_bf16 v[48:63], a[132:135], v[132:135], v[48:63]\n"
    "ds_read_b64_tr_b16 a[164:165], v147 offset:53248\n"
    "ds_read_b64_tr_b16 a[166:167], v147 offset:54272\n"
    "v_mfma_f32_32x32x16_bf16 v[48:63], a[136:139], v[136:139], v[48:63]\n"
    "ds_read_b64_tr_b16 a[168:169], v147 offset:57344\n"
    "ds_read_b64_tr_b16 a[170:171], v147 offset:58368\n"
    "v_mfma_f32_32x32x16_bf16 v[48:63], a[140:143], v[140:143], v[48:63]\n"
    "ds_read_b64_tr_b16 a[172:173], v147 offset:61440\n"
    "ds_read_b64_tr_b16 a[174:175], v147 offset:62464\n"
    "s_waitcnt lgkmcnt(8)\n"
    "v_mfma_f32_32x32x16_bf16 v[48:63], a[144:147], v[80:83], v[48:63]\n"
    "ds_read_b64_tr_b16 a[128:129], v149 offset:0\n"
    "ds_read_b64_tr_b16 a[130:131], v149 offset:1024\n"
    "v_mfma_f32_32x32x16_bf16 v[48:63], a[148:151], v[84:87], v[48:63]\n"
    "ds_read_b64_tr_b16 a[132:133], v149 offset:4096\n"
    "ds_read_b64_tr_b16 a[134:135], v149 offset:5120\n"
    "v_mfma_f32_32x32x16_bf16 v[48:63], a[152:155], v[88:91], v[48:63]\n"
    "ds_read_b64_tr_b16 a[136:137], v149 offset:8192\n"
    "ds_read_b64_tr_b16 a[138:139], v149 offset:9216\n"
    "v_mfma_f32_32x32x16_bf16 v[48:63], a[156:159], v[92:95], v[48:63]\n"
    "ds_read_b64_tr_b16 a[140:141], v149 offset:12288\n"
    "ds_read_b64_tr_b16 a[142:143], v149 offset:13312\n"
    "s_waitcnt lgkmcnt(8)\n"
    "v_mfma_f32_32x32x16_bf16 v[48:63], a[160:163], v[172:175], v[48:63]\n"
    "ds_read_b64_tr_b16 a[144:145], v145 offset:0\n"
    "ds_read_b64_tr_b16 a[146:147], v145 offset:1024\n"
    "v_mfma_f32_32x32x16_bf16 v[48:63], a[164:167], v[176:179], v[48:63]\n"
    "ds_read_b64_tr_b16 a[148:149], v145 offset:4096\n"
    "ds_read_b64_tr_b16 a[150:151], v145 offset:5120\n"
    "v_mfma_f32_32x32x16_bf16 v[48:63], a[168:171], v[180:183], v[48:63]\n"
    "ds_read_b64_tr_b16 a[152:153], v145 offset:8192\n"
    "ds_read_b64_tr_b16 a[154:155], v145 offset:9216\n"
    "v_mfma_f32_32x32x16_bf16 v[48:63], a[172:175], v[184:187], v[48:63]\n"
    "ds_read_b64_tr_b16 a[156:157], v145 offset:12288\n"
    "ds_read_b64_tr_b16 a[158:159], v145 offset:13312\n"
    "s_waitcnt vmcnt(0)\n"
    "v_cmp_eq_u32 vcc, s72, v13\n"
    "s_nop 0\n"
    "s_andn2_b32 s5, 0xe, vcc_lo\n"
    "s_cbranch_scc1 LfsB8\n"
    "LfoB8:\n"
    "s_waitcnt lgkmcnt(8)\n"
    "v_mfma_f32_32x32x16_bf16 v[48:63], a[128:131], v[188:191], v[48:63]\n"
    "s_add_u32 s18, s94, 0\n"
    "buffer_load_dwordx4 v[164:167], v248, s[60:63], s18 offen offset:256 sc1\n"
    "buffer_load_dwordx4 v[168:171], v248, s[60:63], s18 offen offset:1280 sc1\n"
    "buffer_load_dword v28, v144, s[60:63], s18 offen offset:0 sc1\n"
    "v_mfma_f32_32x32x16_bf16 v[48:63], a[132:135], v[192:195], v[48:63]\n"
    "s_add_u32 s78, s94, 9216\n"
    "buffer_load_dwordx4 v[228:231], v248, s[60:63], s78 offen offset:256 sc1\n"
    "buffer_load_dwordx4 v[232:235], v248, s[60:63], s78 offen offset:1280 sc1\n"
    "buffer_load_dword v29, v144, s[60:63], s78 offen offset:0 sc1\n"
    "v_mfma_f32_32x32x16_bf16 v[48:63], a[136:139], v[196:199], v[48:63]\n"
    "s_add_u32 s19, s94, 18432\n"
    "buffer_load_dwordx4 v[40:43], v248, s[60:63], s19 offen offset:256 sc1\n"
    "buffer_load_dwordx4 v[44:47], v248, s[60:63], s19 offen offset:1280 sc1\n"
    "buffer_load_dword v30, v144, s[60:63], s19 offen offset:0 sc1\n"
    "v_mfma_f32_32x32x16_bf16 v[48:63], a[140:143], v[200:203], v[48:63]\n"
    "s_waitcnt lgkmcnt(0)\n"
    "v_mfma_f32_32x32x16_bf16 v[48:63], a[144:147], v[204:207], v[48:63]\n"
    "v_mfma_f32_32x32x16_bf16 v[48:63], a[148:151], v[208:211], v[48:63]\n"
    "v_mfma_f32_32x32x16_bf16 v[48:63], a[152:155], v[212:215], v[48:63]\n"
    "v_mfma_f32_32x32x16_bf16 v[48:63], a[156:159], v[216:219], v[48:63]\n"
    "s_waitcnt vmcnt(8)\n"
    "v_add3_u32 v13, v164, v165, s72\n"
    "v_add3_u32 v13, v13, v166, v167\n"
    "s_waitcnt vmcnt(7)\n"
    "v_add3_u32 v13, v13, v168, v169\n"
    "v_add3_u32 v13, v13, v170, v171\n"
    "s_waitcnt vmcnt(6)\n"
    "v_cmp_ne_u32 vcc, v13, v28\n"
    "s_nop 0\n"
    "s_cbranch_vccnz LreB9\n"
    "LokB9:\n"
    "v_mfma_f32_32x32x16_bf16 v[48:63], a[208:211], v[164:167], v[48:63]\n"
    "v_mfma_f32_32x32x16_bf16 v[48:63], a[212:215], v[168:171], v[48:63]\n"
    "s_waitcnt vmcnt(5)\n"
    "v_add3_u32 v14, v228, v229, s72\n"
    "v_add3_u32 v14, v14, v230, v231\n"
    "s_waitcnt vmcnt(4)\n"
    "v_add3_u32 v14, v14, v232, v233\n"
    "v_add3_u32 v14, v14, v234, v235\n"
    "s_waitcnt vmcnt(3)\n"
    "v_cmp_ne_u32 vcc, v14, v29\n"
    "s_nop 0\n"
    "s_cbranch_vccnz LreB10\n"
    "LokB10:\n"
    "v_mfma_f32_32x32x16_bf16 v[48:63], a[208:211], v[228:231], v[48:63]\n"
    "v_mfma_f32_32x32x16_bf16 v[48:63], a[212:215], v[232:235], v[48:63]\n"
    "s_waitcnt vmcnt(2)\n"
    "v_add3_u32 v15, v40, v41, s72\n"
    "v_add3_u32 v15, v15, v42, v43\n"
    "s_waitcnt vmcnt(1)\n"
    "v_add3_u32 v15, v15, v44, v45\n"
    "v_add3_u32 v15, v15, v46, v47\n"
    "s_waitcnt vmcnt(0)\n"
    "v_cmp_ne_u32 vcc, v15, v30\n"
    "s_nop 0\n"
    "s_cbranch_vccnz LreB11\n"
    "LokB11:\n"
    "v_mfma_f32_32x32x16_bf16 v[48:63], a[208:211], v[40:43], v[48:63]\n"
    "v_mfma_f32_32x32x16_bf16 v[48:63], a[212:215], v[44:47], v[48:63]\n"
    "s_nop 11\n"
    "v_cvt_pk_bf16_f32 v48, v48, v49\n"
    "v_cvt_pk_bf16_f32 v49, v50, v51\n"
    "v_cvt_pk_bf16_f32 v50, v52, v53\n"
    "v_cvt_pk_bf16_f32 v51, v54, v55\n"
    "v_cvt_pk_bf16_f32 v52, v56, v57\n"
    "v_cvt_pk_bf16_f32 v53, v58, v59\n"
    "v_cvt_pk_bf16_f32 v54, v60, v61\n"
    "v_cvt_pk_bf16_f32 v55, v62, v63\n"
    "v_permlane16_swap_b32 v48, v52\n"
    "v_permlane16_swap_b32 v49, v53\n"
    "v_permlane16_swap_b32 v50, v54\n"
    "v_permlane16_swap_b32 v51, v55\n"
    "buffer_store_dwordx4 v[48:51], v242, s[64:67], s101 offen\n"
    "buffer_store_dwordx4 v[52:55], v242, s[64:67], s95 offen\n"
    "s_endpgm\n"
    "LreB1:\n"
    "s_mov_b32 s71, 1000000\n"
    "LlpB1:\n"
    "s_add_u32 s97, s88, 0\n"
    "buffer_load_dwordx4 v[96:99], v248, s[60:63], s97 offen offset:512 sc1\n"
    "buffer_load_dwordx4 v[100:103], v248, s[60:63], s97 offen offset:1536 sc1\n"
    "buffer_load_dwordx4 v[104:107], v248, s[60:63], s97 offen offset:2560 sc1\n"
    "buffer_load_dwordx4 v[108:111], v248, s[60:63], s97 offen offset:3584 sc1\n"
    "buffer_load_dword v151, v144, s[60:63], s97 offen offset:0 sc1\n"
    "s_add_u32 s97, s88, 4608\n"
    "buffer_load_dwordx4 v[112:115], v248, s[60:63], s97 offen offset:512 sc1\n"
    "buffer_load_dwordx4 v[116:119], v248, s[60:63], s97 offen offset:1536 sc1\n"
    "buffer_load_dwordx4 v[120:123], v248, s[60:63], s97 offen offset:2560 sc1\n"
    "buffer_load_dwordx4 v[124:127], v248, s[60:63], s97 offen offset:3584 sc1\n"
    "buffer_load_dword v152, v144, s[60:63], s97 offen offset:0 sc1\n"
    "s_waitcnt vmcnt(0)\n"
    "v_add3_u32 v154, v96, v97, s68\n"
    "v_add3_u32 v154, v154, v98, v99\n"
    "v_add3_u32 v154, v154, v100, v101\n"
    "v_add3_u32 v154, v154, v102, v103\n"
    "v_add3_u32 v154, v154, v104, v105\n"
    "v_add3_u32 v154, v154, v106, v107\n"
    "v_add3_u32 v154, v154, v108, v109\n"
    "v_add3_u32 v154, v154, v110, v111\n"
    "v_add3_u32 v154, v154, v112, v113\n"
    "v_add3_u32 v154, v154, v114, v115\n"
    "v_add3_u32 v154, v154, v116, v117\n"
    "v_add3_u32 v154, v154, v118, v119\n"
    "v_add3_u32 v154, v154, v120, v121\n"
    "v_add3_u32 v154, v154, v122, v123\n"
    "v_add3_u32 v154, v154, v124, v125\n"
    "v_add3_u32 v154, v154, v126, v127\n"
    "v_add_u32 v151, v151, v152\n"
    "v_cmp_ne_u32 vcc, v154, v151\n"
    "s_nop 1\n"
    "s_cbranch_vccz LokB1\n"
    "s_sub_u32 s71, s71, 1\n"
    "s_cbranch_scc0 LlpB1\n"
    "s_branch LokB1\n"
    "LreB2:\n"
    "s_mov_b32 s71, 1000000\n"
    "LlpB2:\n"
    "s_lshl_b32 s97, s24, 11\n"
    "s_sub_u32 s97, s88, s97\n"
    "s_add_u32 s97, s97, 11264\n"
    "buffer_load_dwordx4 v[68:71], v248, s[60:63], s97 offen offset:512 sc1\n"
    "buffer_load_dwordx4 v[76:79], v248, s[60:63], s97 offen offset:1536 sc1\n"
    "s_lshl_b32 s97, s24, 8\n"
    "s_sub_u32 s97, s88, s97\n"
    "s_add_u32 s97, s97, 9472\n"
    "buffer_load_dword v151, v144, s[60:63], s97 offen offset:0 sc1\n"
    "s_waitcnt vmcnt(0)\n"
    "v_add3_u32 v154, v68, v69, s72\n"
    "v_add3_u32 v154, v154, v70, v71\n"
    "v_add3_u32 v154, v154, v76, v77\n"
    "v_add3_u32 v154, v154, v78, v79\n"
    "v_cmp_ne_u32 vcc, v154, v151\n"
    "s_nop 1\n"
    "s_cbranch_vccz LokB2\n"
    "s_sub_u32 s71, s71, 1\n"
    "s_cbranch_scc0 LlpB2\n"
    "s_branch LokB2\n"
    "LfsB3:\n"
    "s_mov_b32 s71, 1000000\n"
    "LflB3:\n"
    "s_sleep 4\n"
    "v_and_b32 v245, 0x7c, v144\n"
    "buffer_load_dword v245, v245, s[60:63], s0 offen sc1\n"
    "s_waitcnt vmcnt(0)\n"
    "v_cmp_eq_u32 vcc, s72, v245\n"
    "s_nop 0\n"
    "s_andn2_b32 s5, 0x30, vcc_lo\n"
    "s_cbranch_scc0 LfdB3\n"
    "s_sub_u32 s71, s71, 1\n"
    "s_cbranch_scc0 LflB3\n"
    "LfdB3:\n"
    "s_sleep 8\n"
    "s_branch LfoB3\n"
    "LreB4:\n"
    "s_mov_b32 s71, 1000000\n"
    "LlpB4:\n"
    "s_add_u32 s97, s88, 13824\n"
    "buffer_load_dwordx4 v[128:131], v248, s[60:63], s97 offen offset:512 sc1\n"
    "buffer_load_dwordx4 v[132:135], v248, s[60:63], s97 offen offset:1536 sc1\n"
    "buffer_load_dwordx4 v[136:139], v248, s[60:63], s97 offen offset:2560 sc1\n"
    "buffer_load_dwordx4 v[140:143], v248, s[60:63], s97 offen offset:3584 sc1\n"
    "buffer_load_dword v151, v144, s[60:63], s97 offen offset:0 sc1\n"
    "s_add_u32 s97, s88, 18432\n"
    "buffer_load_dwordx4 v[80:83], v248, s[60:63], s97 offen offset:512 sc1\n"
    "buffer_load_dwordx4 v[84:87], v248, s[60:63], s97 offen offset:1536 sc1\n"
    "buffer_load_dwordx4 v[88:91], v248, s[60:63], s97 offen offset:2560 sc1\n"
    "buffer_load_dwordx4 v[92:95], v248, s[60:63], s97 offen offset:3584 sc1\n"
    "buffer_load_dword v152, v144, s[60:63], s97 offen offset:0 sc1\n"
    "s_waitcnt vmcnt(0)\n"
    "v_add3_u32 v154, v128, v129, s68\n"
    "v_add3_u32 v154, v154, v130, v131\n"
    "v_add3_u32 v154, v154, v132, v133\n"
    "v_add3_u32 v154, v154, v134, v135\n"
    "v_add3_u32 v154, v154, v136, v137\n"
    "v_add3_u32 v154, v154, v138, v139\n"
    "v_add3_u32 v154, v154, v140, v141\n"
    "v_add3_u32 v154, v154, v142, v143\n"
    "v_add3_u32 v154, v154, v80, v81\n"
    "v_add3_u32 v154, v154, v82, v83\n"
    "v_add3_u32 v154, v154, v84, v85\n"
    "v_add3_u32 v154, v154, v86, v87\n"
    "v_add3_u32 v154, v154, v88, v89\n"
    "v_add3_u32 v154, v154, v90, v91\n"
    "v_add3_u32 v154, v154, v92, v93\n"
    "v_add3_u32 v154, v154, v94, v95\n"
    "v_add_u32 v151, v151, v152\n"
    "v_cmp_ne_u32 vcc, v154, v151\n"
    "s_nop 1\n"
    "s_cbranch_vccz LokB4\n"
    "s_sub_u32 s71, s71, 1\n"
    "s_cbranch_scc0 LlpB4\n"
    "s_branch LokB4\n"
    "LfsB5:\n"
    "s_mov_b32 s71, 1000000\n"
    "LflB5:\n"
    "s_sleep 4\n"
    "v_and_b32 v244, 0x7c, v144\n"
    "buffer_load_dword v244, v244, s[60:63], s0 offen sc1\n"
    "s_waitcnt vmcnt(0)\n"
    "v_cmp_eq_u32 vcc, s72, v244\n"
    "s_nop 0\n"
    "s_andn2_b32 s5, 0xc0, vcc_lo\n"
    "s_cbranch_scc0 LfdB5\n"
    "s_sub_u32 s71, s71, 1\n"
    "s_cbranch_scc0 LflB5\n"
    "LfdB5:\n"
    "s_sleep 6\n"
    "s_branch LfoB5\n"
    "LreB6:\n"
    "s_mov_b32 s71, 1000000\n"
    "LlpB6:\n"
    "s_add_u32 s97, s90, 32256\n"
    "buffer_load_dwordx4 v[172:175], v248, s[60:63], s97 offen offset:512 sc1\n"
    "buffer_load_dwordx4 v[176:179], v248, s[60:63], s97 offen offset:1536 sc1\n"
    "buffer_load_dwordx4 v[180:183], v248, s[60:63], s97 offen offset:2560 sc1\n"
    "buffer_load_dwordx4 v[184:187], v248, s[60:63], s97 offen offset:3584 sc1\n"
    "buffer_load_dword v20, v144, s[60:63], s97 offen offset:0 sc1\n"
    "s_waitcnt vmcnt(0)\n"
    "v_add3_u32 v23, v172, v173, s72\n"
    "v_add3_u32 v23, v23, v174, v175\n"
    "v_add3_u32 v23, v23, v176, v177\n"
    "v_add3_u32 v23, v23, v178, v179\n"
    "v_add3_u32 v23, v23, v180, v181\n"
    "v_add3_u32 v23, v23, v182, v183\n"
    "v_add3_u32 v23, v23, v184, v185\n"
    "v_add3_u32 v23, v23, v186, v187\n"
    "v_cmp_ne_u32 vcc, v23, v20\n"
    "s_nop 1\n"
    "s_cbranch_vccz LokB6\n"
    "s_sub_u32 s71, s71, 1\n"
    "s_cbranch_scc0 LlpB6\n"
    "s_branch LokB6\n"
    "LreB7:\n"
    "s_mov_b32 s71, 1000000\n"
    "LlpB7:\n"
    "s_add_u32 s97, s88, 23040\n"
    "buffer_load_dwordx4 v[188:191], v248, s[60:63], s97 offen offset:512 sc1\n"
    "buffer_load_dwordx4 v[192:195], v248, s[60:63], s97 offen offset:1536 sc1\n"
    "buffer_load_dwordx4 v[196:199], v248, s[60:63], s97 offen offset:2560 sc1\n"
    "buffer_load_dwordx4 v[200:203], v248, s[60:63], s97 offen offset:3584 sc1\n"
    "buffer_load_dword v22, v144, s[60:63], s97 offen offset:0 sc1\n"
    "s_add_u32 s97, s88, 27648\n"
    "buffer_load_dwordx4 v[204:207], v248, s[60:63], s97 offen offset:512 sc1\n"
    "buffer_load_dwordx4 v[208:211], v248, s[60:63], s97 offen offset:1536 sc1\n"
    "buffer_load_dwordx4 v[212:215], v248, s[60:63], s97 offen offset:2560 sc1\n"
    "buffer_load_dwordx4 v[216:219], v248, s[60:63], s97 offen offset:3584 sc1\n"
    "buffer_load_dword v14, v144, s[60:63], s97 offen offset:0 sc1\n"
    "s_waitcnt vmcnt(0)\n"
    "v_add3_u32 v31, v188, v189, s68\n"
    "v_add3_u32 v31, v31, v190, v191\n"
    "v_add3_u32 v31, v31, v192, v193\n"
    "v_add3_u32 v31, v31, v194, v195\n"
    "v_add3_u32 v31, v31, v196, v197\n"
    "v_add3_u32 v31, v31, v198, v199\n"
    "v_add3_u32 v31, v31, v200, v201\n"
    "v_add3_u32 v31, v31, v202, v203\n"
    "v_add3_u32 v31, v31, v204, v205\n"
    "v_add3_u32 v31, v31, v206, v207\n"
    "v_add3_u32 v31, v31, v208, v209\n"
    "v_add3_u32 v31, v31, v210, v211\n"
    "v_add3_u32 v31, v31, v212, v213\n"
    "v_add3_u32 v31, v31, v214, v215\n"
    "v_add3_u32 v31, v31, v216, v217\n"
    "v_add3_u32 v31, v31, v218, v219\n"
    "v_add_u32 v22, v22, v14\n"
    "v_cmp_ne_u32 vcc, v31, v22\n"
    "s_nop 1\n"
    "s_cbranch_vccz LokB7\n"
    "s_sub_u32 s71, s71, 1\n"
    "s_cbranch_scc0 LlpB7\n"
    "s_branch LokB7\n"
    "LfsB8:\n"
    "s_mov_b32 s71, 1000000\n"
    "LflB8:\n"
    "s_sleep 4\n"
    "v_and_b32 v13, 0x7c, v144\n"
    "buffer_load_dword v13, v13, s[60:63], s2 offen sc1\n"
    "s_waitcnt vmcnt(0)\n"
    "v_cmp_eq_u32 vcc, s72, v13\n"
    "s_nop 0\n"
    "s_andn2_b32 s5, 0xe, vcc_lo\n"
    "s_cbranch_scc0 LfdB8\n"
    "s_sub_u32 s71, s71, 1\n"
    "s_cbranch_scc0 LflB8\n"
    "LfdB8:\n"
    "s_branch LfoB8\n"
    "LreB9:\n"
    "s_mov_b32 s71, 1000000\n"
    "LlpB9:\n"
    "s_add_u32 s97, s94, 0\n"
    "buffer_load_dwordx4 v[164:167], v248, s[60:63], s97 offen offset:256 sc1\n"
    "buffer_load_dwordx4 v[168:171], v248, s[60:63], s97 offen offset:1280 sc1\n"
    "buffer_load_dword v28, v144, s[60:63], s97 offen offset:0 sc1\n"
    "s_waitcnt vmcnt(0)\n"
    "v_add3_u32 v13, v164, v165, s72\n"
    "v_add3_u32 v13, v13, v166, v167\n"
    "v_add3_u32 v13, v13, v168, v169\n"
    "v_add3_u32 v13, v13, v170, v171\n"
    "v_cmp_ne_u32 vcc, v13, v28\n"
    "s_nop 1\n"
    "s_cbranch_vccz LokB9\n"
    "s_sub_u32 s71, s71, 1\n"
    "s_cbranch_scc0 LlpB9\n"
    "s_branch LokB9\n"
    "LreB10:\n"
    "s_mov_b32 s71, 1000000\n"
    "LlpB10:\n"
    "s_add_u32 s97, s94, 9216\n"
    "buffer_load_dwordx4 v[228:231], v248, s[60:63], s97 offen offset:256 sc1\n"
    "buffer_load_dwordx4 v[232:235], v248, s[60:63], s97 offen offset:1280 sc1\n"
    "buffer_load_dword v29, v144, s[60:63], s97 offen offset:0 sc1\n"
    "s_waitcnt vmcnt(0)\n"
    "v_add3_u32 v14, v228, v229, s72\n"
    "v_add3_u32 v14, v14, v230, v231\n"
    "v_add3_u32 v14, v14, v232, v233\n"
    "v_add3_u32 v14, v14, v234, v235\n"
    "v_cmp_ne_u32 vcc, v14, v29\n"
    "s_nop 1\n"
    "s_cbranch_vccz LokB10\n"
    "s_sub_u32 s71, s71, 1\n"
    "s_cbranch_scc0 LlpB10\n"
    "s_branch LokB10\n"
    "LreB11:\n"
    "s_mov_b32 s71, 1000000\n"
    "LlpB11:\n"
    "s_add_u32 s97, s94, 18432\n"
    "buffer_load_dwordx4 v[40:43], v248, s[60:63], s97 offen offset:256 sc1\n"
    "buffer_load_dwordx4 v[44:47], v248, s[60:63], s97 offen offset:1280 sc1\n"
    "buffer_load_dword v30, v144, s[60:63], s97 offen offset:0 sc1\n"
    "s_waitcnt vmcnt(0)\n"
    "v_add3_u32 v15, v40, v41, s72\n"
    "v_add3_u32 v15, v15, v42, v43\n"
    "v_add3_u32 v15, v15, v44, v45\n"
    "v_add3_u32 v15, v15, v46, v47\n"
    "v_cmp_ne_u32 vcc, v15, v30\n"
    "s_nop 1\n"
    "s_cbranch_vccz LokB11\n"
    "s_sub_u32 s71, s71, 1\n"
    "s_cbranch_scc0 LlpB11\n"
    "s_branch LokB11\n"
    "LendB:\n"
    :: "s"(ka), "s"(b), "s"(w), "s"((unsigned)(unsigned long long)lds)
    : "memory", "vcc", "scc", "v0", "v1", "v2", "v3", "v4", "v5", "v6", "v7", "v8", "v9", "v10", "v11", "v12", "v13", "v14", "v15", "v16", "v17", "v18", "v19", "v20", "v21", "v22", "v23", "v24", "v25", "v26", "v27", "v28", "v29", "v30", "v31", "v32", "v33", "v34", "v35", "v36", "v37", "v38", "v39", "v40", "v41", "v42", "v43", "v44", "v45", "v46", "v47", "v48", "v49", "v50", "v51", "v52", "v53", "v54", "v55", "v56", "v57", "v58", "v59", "v60", "v61", "v62", "v63", "v64", "v65", "v66", "v67", "v68", "v69", "v70", "v71", "v72", "v73", "v74", "v75", "v76", "v77", "v78", "v79", "v80", "v81", "v82", "v83", "v84", "v85", "v86", "v87", "v88", "v89", "v90", "v91", "v92", "v93", "v94", "v95", "v96", "v97", "v98", "v99", "v100", "v101", "v102", "v103", "v104", "v105", "v106", "v107", "v108", "v109", "v110", "v111", "v112", "v113", "v114", "v115", "v116", "v117", "v118", "v119", "v120", "v121", "v122", "v123", "v124", "v125", "v126", "v127", "v128", "v129", "v130", "v131", "v132", "v133", "v134", "v135", "v136", "v137", "v138", "v139", "v140", "v141", "v142", "v143", "v144", "v145", "v146", "v147", "v148", "v149", "v150", "v151", "v152", "v153", "v154", "v155", "v156", "v157", "v158", "v159", "v160", "v161", "v162", "v163", "v164", "v165", "v166", "v167", "v168", "v169", "v170", "v171", "v172", "v173", "v174", "v175", "v176", "v177", "v178", "v179", "v180", "v181", "v182", "v183", "v184", "v185", "v186", "v187", "v188", "v189", "v190", "v191", "v192", "v193", "v194", "v195", "v196", "v197", "v198", "v199", "v200", "v201", "v202", "v203", "v204", "v205", "v206", "v207", "v208", "v209", "v210", "v211", "v212", "v213", "v214", "v215", "v216", "v217", "v218", "v219", "v220", "v221", "v222", "v223", "v224", "v225", "v226", "v227", "v228", "v229", "v230", "v231", "v232", "v233", "v234", "v235", "v236", "v237", "v238", "v239", "v240", "v241", "v242", "v243", "v244", "v245", "v246", "v247", "v248", "v249", "v250", "v251", "v252", "v253", "v254", "v255", "a0", "a1", "a2", "a3", "a4", "a5", "a6", "a7", "a8", "a9", "a10", "a11", "a12", "a13", "a14", "a15", "a16", "a17", "a18", "a19", "a20", "a21", "a22", "a23", "a24", "a25", "a26", "a27", "a28", "a29", "a30", "a31", "a32", "a33", "a34", "a35", "a36", "a37", "a38", "a39", "a40", "a41", "a42", "a43", "a44", "a45", "a46", "a47", "a48", "a49", "a50", "a51", "a52", "a53", "a54", "a55", "a56", "a57", "a58", "a59", "a60", "a61", "a62", "a63", "a64", "a65", "a66", "a67", "a68", "a69", "a70", "a71", "a72", "a73", "a74", "a75", "a76", "a77", "a78", "a79", "a80", "a81", "a82", "a83", "a84", "a85", "a86", "a87", "a88", "a89", "a90", "a91", "a92", "a93", "a94", "a95", "a96", "a97", "a98", "a99", "a100", "a101", "a102", "a103", "a104", "a105", "a106", "a107", "a108", "a109", "a110", "a111", "a112", "a113", "a114", "a115", "a116", "a117", "a118", "a119", "a120", "a121", "a122", "a123", "a124", "a125", "a126", "a127", "a128", "a129", "a130", "a131", "a132", "a133", "a134", "a135", "a136", "a137", "a138", "a139", "a140", "a141", "a142", "a143", "a144", "a145", "a146", "a147", "a148", "a149", "a150", "a151", "a152", "a153", "a154", "a155", "a156", "a157", "a158", "a159", "a160", "a161", "a162", "a163", "a164", "a165", "a166", "a167", "a168", "a169", "a170", "a171", "a172", "a173", "a174", "a175", "a176", "a177", "a178", "a179", "a180", "a181", "a182", "a183", "a184", "a185", "a186", "a187", "a188", "a189", "a190", "a191", "a192", "a193", "a194", "a195", "a196", "a197", "a198", "a199", "a200", "a201", "a202", "a203", "a204", "a205", "a206", "a207", "a208", "a209", "a210", "a211", "a212", "a213", "a214", "a215", "a216", "a217", "a218", "a219", "a220", "a221", "a222", "a223", "a224", "a225", "a226", "a227", "a228", "a229", "a230", "a231", "a232", "a233", "a234", "a235", "a236", "a237", "a238", "a239", "a240", "a241", "a242", "a243", "a244", "a245", "a246", "a247", "a248", "a249", "a250", "a251", "a252", "a253", "a254", "a255", "s8", "s9", "s10", "s11", "s12", "s13", "s14", "s15", "s16", "s17", "s18", "s19", "s20", "s21", "s22", "s23", "s24", "s25", "s26", "s27", "s36", "s37", "s38", "s39", "s40", "s41", "s42", "s43", "s44", "s45", "s46", "s47", "s48", "s49", "s50", "s51", "s52", "s53", "s54", "s55", "s56", "s57", "s58", "s59", "s60", "s61", "s62", "s63", "s64", "s65", "s66", "s67", "s68", "s69", "s70", "s71", "s72", "s73", "s74", "s75", "s76", "s77", "s78", "s79", "s80", "s81", "s82", "s83", "s84", "s85", "s86", "s87", "s88", "s89", "s90", "s91", "s92", "s93", "s94", "s95", "s96", "s97", "s98", "s99", "s100", "s101");
}

extern "C" void kernel_launch(void* const* d_in, const int* in_sizes, int n_in, void* d_out, int out_size,
                              void* d_ws, size_t ws_size, hipStream_t stream) {
  const unsigned F = 0x00020000u;
  Args a;
  a.x = Srd{d_in[0], 2048u * 512u * 2u, F};
  a.wg = Srd{d_in[4], 512u * 1792u * 2u, F};
  a.wu = Srd{d_in[5], 512u * 1792u * 2u, F};
  a.wq = Srd{d_in[1], 512u * 1536u * 2u, F};
  a.wd = Srd{d_in[6], 1792u * 512u * 2u, F};
  a.wo = Srd{d_in[2], 512u * 512u * 2u, F};
  a.ws = Srd{d_ws, 22020096u, F};
  a.out = Srd{d_out, 2048u * 512u * 2u, F};
  a.gw = Srd{d_in[7], 512u * 2u, F};
  fused_kernel<<<256, 256, 0, stream>>>(a);
}
```
